```python
import torch

def _softmax(x, axis=-1):
    return torch.softmax(x, dim=axis)

def _gelu(x):
    return torch.nn.functional.gelu(x, approximate='tanh')

def _silu(x):
    return torch.nn.functional.silu(x)

def _sigmoid(x):
    return torch.sigmoid(x)

def _layernorm(x, axis=-1, eps=1e-05):
    m = x.mean(dim=axis, keepdim=True)
    v = x.var(dim=axis, keepdim=True, unbiased=False)
    return (x - m) / torch.sqrt(v + eps)

def _scatter_add(tgt, idx, src, axis=0):
    return tgt.clone().index_add_(axis, idx, src)

def _segment_sum(src, seg, n):
    out = torch.zeros((n,) + src.shape[1:], dtype=src.dtype)
    return out.index_add_(0, seg, src)

def _pad(x, pw):
    flat = tuple((v for pair in reversed(pw) for v in pair))
    return torch.nn.functional.pad(x, flat)
kernel_name = 'recurrent_scan__memory_aug_recurrent_mem_tr__dense_activation_silu__prompt'

def setup_inputs():

    _w = lambda a: (lambda t: t.to(torch.bfloat16) if t.is_floating_point() else t)(torch.as_tensor(a))

    def _A_setup():
        B, S, Dm = (1, 2048, 576)
        H, Dh = (32, 128)
        M, seg = (8, 32)
        seg = min(seg, S)
        S = S // seg * seg
        return {'x': _w((lambda *s: torch.randn(*s, dtype=torch.bfloat16) * 0.05)(B, S, Dm)), 'mem0': _w((lambda *s: torch.randn(*s, dtype=torch.bfloat16) * 0.8)(B, M, Dm)), 'Wq': _w((lambda *s: torch.randn(*s, dtype=torch.bfloat16) * 1.6)(Dm, H, Dh) * Dm ** (-0.5)), 'Wk': _w((lambda *s: torch.randn(*s, dtype=torch.bfloat16) * 1.6)(Dm, H, Dh) * Dm ** (-0.5)), 'Wv': _w((lambda *s: torch.randn(*s, dtype=torch.bfloat16) * 0.8)(Dm, H, Dh) * Dm ** (-0.5)), 'Wo': _w((lambda *s: torch.randn(*s, dtype=torch.bfloat16) * 0.8)(H, Dh, Dm) * (H * Dh) ** (-0.5)), 'seg_len': seg}
    _A_in = _A_setup()

    def _B_setup():
        B, S, D, F = (1, 2048, 576, 4608)
        return {'x': _w((lambda *s: torch.randn(*s, dtype=torch.bfloat16) * 0.05)(B, S, D)), 'w1': _w((lambda *s: torch.randn(*s, dtype=torch.bfloat16) * 0.8)(D, F) * D ** (-0.5)), 'w2': _w((lambda *s: torch.randn(*s, dtype=torch.bfloat16) * 0.2)(F, D) * F ** (-0.5))}
    _B_in = _B_setup()
    return {'A_x': _A_in['x'], 'A_mem0': _A_in['mem0'], 'A_Wq': _A_in['Wq'], 'A_Wk': _A_in['Wk'], 'A_Wv': _A_in['Wv'], 'A_Wo': _A_in['Wo'], 'A_seg_len': _A_in['seg_len'], 'B_x': _B_in['x'], 'B_w1': _B_in['w1'], 'B_w2': _B_in['w2']}

def reference(A_x, A_mem0, A_Wq, A_Wk, A_Wv, A_Wo, A_seg_len, B_x, B_w1, B_w2):

    def _A_ref(x, mem0, Wq, Wk, Wv, Wo, seg_len):
        B, S, Dm = x.shape
        H, Dh = (Wq.shape[1], Wq.shape[2])
        M = mem0.shape[1]
        scale = Dh ** (-0.5)
        n_seg = S // seg_len
        L = M + seg_len
        idx = torch.arange(L)
        mask = (idx[:, None] < M) | (idx[:, None] >= idx[None, :])
        mem = mem0
        outs = []
        for s in range(n_seg):
            xs = x[:, s * seg_len:(s + 1) * seg_len, :].clone()
            h = torch.concatenate([mem, xs], axis=1)
            q = torch.einsum('btd,dhe->bhte', h, Wq)
            k = torch.einsum('btd,dhe->bhte', h, Wk)
            v = torch.einsum('btd,dhe->bhte', h, Wv)
            scores = torch.einsum('bhqe,bhke->bhqk', q, k) * scale
            scores = torch.where(mask[None, None], scores, -1000000000.0)
            p = _softmax(scores, axis=-1)
            o = torch.einsum('bhqk,bhke->bhqe', p, v)
            o = torch.einsum('bhte,hed->btd', o, Wo)
            mem = o[:, :M, :].clone()
            outs.append(o[:, M:, :])
        return torch.concatenate(outs, axis=1)

    def _B_ref(x, w1, w2):
        h = torch.einsum('bsd,df->bsf', x, w1)
        h = _silu(h)
        out = torch.einsum('bsf,fd->bsd', h, w2)
        return out
    x = A_x
    xn = _layernorm(x, axis=-1)
    s = _A_ref(xn, A_mem0, A_Wq, A_Wk, A_Wv, A_Wo, A_seg_len)
    g = _silu(_B_ref(xn, B_w1, B_w2))
    return x + s * g
```

```cpp
#define D0 84
#define D1 236
#define D2 2800
#define D3 40
#define D4 40
#define D5 -28
#define D6 0
#define D7 6
#define D8 512610
#include <hip/hip_runtime.h>
#include <hip/hip_bf16.h>
#include <stdint.h>

typedef unsigned short u16;
typedef unsigned v4u __attribute__((ext_vector_type(4)));
typedef unsigned v2u __attribute__((ext_vector_type(2)));
typedef float f4 __attribute__((ext_vector_type(4)));
typedef float f16v __attribute__((ext_vector_type(16)));
typedef __bf16 bfv __attribute__((ext_vector_type(8)));
typedef short s4 __attribute__((ext_vector_type(4)));
typedef __bf16 bf2 __attribute__((ext_vector_type(2)));
typedef float f2 __attribute__((ext_vector_type(2)));
typedef __amdgpu_buffer_rsrc_t rsrc_t;
#define LDSP(T) __attribute__((address_space(3))) T*

constexpr int NSEG = 64, DM = 576, NH = 32, FF = 4608;
constexpr int SC1 = 16;
constexpr int OFF_MEM = 0;
constexpr int OFF_PART = 2 << 20;
constexpr int OFF_FLAG = 4 << 20;
constexpr int FL_KV = OFF_FLAG;
constexpr int FL_KM = OFF_FLAG + 32 * 128;
constexpr int FL_G = OFF_FLAG + 64 * 128;
constexpr int FL_OF = OFF_FLAG + 16384;
constexpr int FL_HF = FL_OF + 68 * 1024;
constexpr int FL_ACK = FL_HF + 66 * 1024;
constexpr int FL_ALL = FL_ACK + 4096;
constexpr int FL_END = FL_ALL + 4096;
constexpr int OFF_GO = OFF_FLAG + (256 << 10);
constexpr int OFF_RDY = OFF_GO + 32768;
constexpr int OFF_DUMMY = OFF_FLAG + (512 << 10);
constexpr int OFF_XF = 5 << 20;
constexpr int OFF_KF = 8 << 20, OFF_VF = 24 << 20, OFF_GF = 40 << 20, OFF_OF = 56 << 20;
constexpr int OFF_HF = 72 << 20;
constexpr int OFF_KM = 92 << 20, OFF_VM = 96 << 20;
constexpr int OFF_DBG = 100 << 20;
constexpr int WS_SPAN = 104 << 20;
constexpr unsigned SPIN_MAX = 1u << 16;

__device__ __forceinline__ float bf2f(u16 x) { return __builtin_bit_cast(float, (unsigned)x << 16); }
__device__ __forceinline__ float bflo(unsigned x) { return __builtin_bit_cast(float, x << 16); }
__device__ __forceinline__ float bfhi(unsigned x) { return __builtin_bit_cast(float, x & 0xffff0000u); }
__device__ __forceinline__ unsigned pk2(float a, float b) { f2 v = {a, b}; return __builtin_bit_cast(unsigned, __builtin_convertvector(v, bf2)); }
__device__ __forceinline__ float rbf(float a) { return bflo(pk2(a, a)); }
__device__ __forceinline__ float silu_f(float x) { return x * __builtin_amdgcn_rcpf(1.0f + __builtin_amdgcn_exp2f(-1.4426950408889634f * x)); }
__device__ __forceinline__ bfv asbfv(v4u x) { return __builtin_bit_cast(bfv, x); }
__device__ __forceinline__ v4u ld16(rsrc_t r, int voff, int soff) { return __builtin_amdgcn_raw_buffer_load_b128(r, voff, soff, SC1); }
__device__ __forceinline__ unsigned ld4(rsrc_t r, int voff, int soff) { return __builtin_amdgcn_raw_buffer_load_b32(r, voff, soff, SC1); }
__device__ __forceinline__ void st16(v4u v, rsrc_t r, int voff, int soff) { __builtin_amdgcn_raw_buffer_store_b128(v, r, voff + soff, 0, SC1); }
__device__ __forceinline__ void st4(unsigned v, rsrc_t r, int voff, int soff) { __builtin_amdgcn_raw_buffer_store_b32(v, r, voff, soff, SC1); }
#define WAIT_VM(n) asm volatile("s_waitcnt vmcnt(" #n ")" ::: "memory")
#define RAW_BARRIER() do { asm volatile("s_waitcnt lgkmcnt(0)" ::: "memory"); __builtin_amdgcn_s_barrier(); } while (0)

__device__ __forceinline__ int lane_id() { int l; asm volatile("v_mbcnt_lo_u32_b32 %0, -1, 0\n\tv_mbcnt_hi_u32_b32 %0, -1, %0" : "=v"(l)); return l; }

#define GS 4
__device__ __forceinline__ rsrc_t ws_rsrc(unsigned char* ws) { return __builtin_amdgcn_make_buffer_rsrc(ws, 0, WS_SPAN, 0x00020000); }
__device__ __forceinline__ unsigned now() { return (unsigned)__builtin_amdgcn_s_memrealtime(); }
__device__ __forceinline__ v2u gate(rsrc_t rs, unsigned go0, int b, int w) {
    WAIT_VM(0);
    v2u go = {go0, 0};
    for (unsigned n = 0; n < SPIN_MAX && go[0] == go0; ++n) {
        if (!(n & 15) || n == 2 || n == 6) st4(now(), rs, 0, OFF_RDY + b * 128 + w * 4);
        if ((b & 7) < 4) __builtin_amdgcn_s_sleep(GS); else __builtin_amdgcn_s_sleep(4);
        asm volatile("" ::: "memory");
        go = __builtin_amdgcn_raw_buffer_load_b64(rs, 0, OFF_GO + b * 128, SC1);
        go[0] = __builtin_amdgcn_readfirstlane(go[0]);
    }
    st4(1, rs, 0, FL_ACK + b * 16 + w * 4);
    return v2u{go[0] >> 30, (unsigned)__builtin_amdgcn_readfirstlane(go[1])};
}
constexpr int COORD = 106;
static_assert(COORD == 13 * 8 + 2 && OFF_RDY == OFF_GO + 32768, "proj_in looks at the RDY words too");
#define L2 340
#define LK 460
#define H4 1
#define LND 40
#define NTO 4
#define CT 1
#define HS 2
#define HD 12
#define QE 1
#define KS 0
#define FPOL 16
#define QF 20
#define NPR 6
__device__ __forceinline__ void coordinate(rsrc_t rs, unsigned* lds, int lead, int keep) {
    const int t = threadIdx.x, xc = t & 7, i = t >> 3, w = t >> 6;
    const v4u r0 = ld16(rs, t * 128, OFF_RDY), p0 = ld16(rs, t * 128 + 16, OFF_RDY);
    const bool norm = (xc >> 1) == 1 && t != COORD, fill = xc >= 4 && i < 18, chain = xc < 2 && i < 16, probe = norm && i < 16, keys = LK && xc >= 4 && i >= 18 && i < 26;
    const unsigned from = xc >= 4 ? 1 : 2, la = (unsigned)(uintptr_t)(LDSP(unsigned))lds;
    bool w1 = norm, w2 = fill || probe || chain || keys, w3 = norm || t >= 252, open = t != COORD && t != 246 && t != 247 && (xc >= 2 || chain);
    if (t < 8) lds[t] = t == 3 ? now() : 0;
    __syncthreads();
    const unsigned t0 = __builtin_amdgcn_readfirstlane(lds[3]);
    for (unsigned n = 1, ph = 0; n < SPIN_MAX && ph < 4; ++n) {
        const bool look = ph ? w2 || w3 : w1, again = !(n & 7) || ph == 3;
        asm volatile("" ::: "memory");
        v4u l = ld16(rs, look ? OFF_RDY + t * 128 : WS_SPAN, 0);
        const v4u p = ld16(rs, ph && w2 && probe ? OFF_RDY + t * 128 + 16 : WS_SPAN, 0), k = ld16(rs, (again || keys) && ph >= from && open ? FL_ACK + t * 16 : WS_SPAN, 0);
        const unsigned d = now() - t0;
        if (t == 0 && ph < 2) lds[ph] = d + (ph ? 220 : lead >> 10);
        if (!look) l = r0;
        open = open && !(k[0] & k[1] & k[2] & k[3]);
        const bool rest = !open || (l[0] != r0[0] && l[1] != r0[1] && l[2] != r0[2] && l[3] != r0[3]);
        w1 = w1 && l[0] == r0[0]; w3 = w3 && !rest;
        if (w2 && (probe ? ph : 1) && (rest || (probe && p[0] != p0[0] && p[2] != p0[2]))) {
            w2 = false;
            if (chain) asm volatile("ds_max_u32 %0, %1 offset:8\n\tds_add_u32 %0, %1 offset:12" :: "v"(la), "v"(min(l[1] - t0, d + 100)) : "memory");
            if (keys) asm volatile("ds_max_u32 %0, %1 offset:8" :: "v"(la), "v"(min(k[1] - t0, d + 100) + LK - L2) : "memory");
            if (probe) asm volatile("ds_add_u32 %0, %1 offset:16\n\tds_add_u32 %0, %2 offset:20\n\tds_add_u32 %0, %3 offset:24\n\tds_add_u32 %0, %4 offset:28" ::
                                   "v"(la), "v"(p[0]), "v"(p[1]), "v"(p[2]), "v"(p[3]) : "memory");
        }
        unsigned* st = lds + 8 + (n & 1) * 4;
        st[w] = __ballot(w1) ? 0 : __ballot(w2) ? 1 : __ballot(w3) ? 2 : __ballot(open) ? 3 : 4;
        RAW_BARRIER();
        const unsigned was = ph;
        ph = min(min(st[0], st[1]), min(st[2], st[3]));
        unsigned par = (unsigned)(lds[5] > lds[4]) << 30 | (unsigned)(lds[7] > lds[6]) << 31;
        if (open && ph >= from && (was < from || !(n & 7)))
            __builtin_amdgcn_raw_buffer_store_b64(v2u{((t0 + d) & 0x3fffffffu) | par, t0 + max(max(lds[0], lds[1]), max(lds[2] + L2, ((lds[3] - t0) >> 5) + (lead & 1023)))}, rs, t * 128, OFF_GO, SC1);
        if (ph >= 3 && was < 3) st4(1, rs, t * 16, FL_ALL);
    }
}
__device__ __forceinline__ void probe_pages(rsrc_t rs, int b, int region, int k) {
    const int base = region ? OFF_PART + (36 + (k >> 2)) * 16384 + (k & 3) * 1024 : OFF_MEM + k * 32768 + 16384 + 1024;
    const unsigned far_side = ((unsigned)__builtin_amdgcn_s_getreg(20 | (3 << 11)) & 0xfu) >= 4;
    float best[2] = {0.f, 0.f};
    unsigned v = ld4(rs, 0, base + 512);
#pragma unroll
    for (int it = 0; it < NPR; ++it) {
        const int par = (it ^ k >> 1) & 1;
        unsigned long long t0, t1;
        asm volatile("s_waitcnt vmcnt(0)\n\ts_memtime %0\n\ts_waitcnt lgkmcnt(0)" : "=s"(t0), "+v"(v) :: "memory");
        st4(v, rs, 0, base + par * 8192 + (it >> 1) * 128);
        WAIT_VM(0);
        v = ld4(rs, 0, base + par * 8192 + (it >> 1) * 128);
        asm volatile("s_waitcnt vmcnt(0)\n\ts_memtime %0\n\ts_waitcnt lgkmcnt(0)" : "=s"(t1), "+v"(v) :: "memory");
        best[it & 1] += __builtin_amdgcn_exp2f(fminf(18.f - (float)(unsigned)(t1 - t0) * (1.f / 128), 14.f));
    }
    const bool swap = ((k >> 1) ^ far_side) & 1;
    __builtin_amdgcn_raw_buffer_store_b64(v2u{(unsigned)(swap ? best[1] : best[0]) << 8 | (now() & 0xffu), (unsigned)(swap ? best[0] : best[1]) << 8}, rs, 0, OFF_RDY + b * 128 + 16 + region * 8, SC1);
}
__device__ __forceinline__ void layer_norm(rsrc_t rs, const u16* x, int task, int lane) {
    const int tok = task * 8 + (lane & 7), pg = lane >> 3;
    v4u v[9];
#pragma unroll
    for (int i = 0; i < 9; ++i) v[i] = *(const v4u*)(x + tok * DM + (i * 8 + pg) * 8);
#pragma unroll
    for (int i = 0; i < 7; ++i) {
        const int g = (i * 63 + task) * 64 + lane;
        const unsigned c = g < 16384 ? ~0u : 0u;
        st16(v4u{c, c, c, c}, rs, task > 62 || g >= 16384 + (FL_END - OFF_FLAG) / 16 ? WS_SPAN : g >= 16384 ? OFF_FLAG + (g - 16384) * 16
                                  : OFF_MEM + (g >> 8) * 32768 + 20480 + ((g >> 7) & 1) * 8192 + (g & 126) * 32 + (g & 1) * 16, 0);
    }
    static_assert(7 * 63 * 64 >= 16384 + (FL_END - OFF_FLAG) / 16, "");
    float s = 0.f;
#pragma unroll
    for (int i = 0; i < 9; ++i)
#pragma unroll
        for (int j = 0; j < 4; ++j) s += bflo(v[i][j]) + bfhi(v[i][j]);
    s += __shfl_xor(s, 8); s += __shfl_xor(s, 16); s += __shfl_xor(s, 32);
    const float mean = s * (1.0f / DM);
    float q = 0.f;
#pragma unroll
    for (int i = 0; i < 9; ++i)
#pragma unroll
        for (int j = 0; j < 4; ++j) { const float a = bflo(v[i][j]) - mean, c = bfhi(v[i][j]) - mean; q += a * a + c * c; }
    q += __shfl_xor(q, 8); q += __shfl_xor(q, 16); q += __shfl_xor(q, 32);
    const float mb = rbf(mean), ri = 1.0f / rbf(sqrtf(rbf(rbf(q * (1.0f / DM)) + 1e-5f)));
#pragma unroll
    for (int i = 0; i < 9; ++i) {
        v4u o;
#pragma unroll
        for (int j = 0; j < 4; ++j) o[j] = pk2(rbf(bflo(v[i][j]) - mb) * ri, rbf(bfhi(v[i][j]) - mb) * ri);
        st16(o, rs, (tok >> 5) * 36864 + (tok & 31) * 16 + (i * 8 + pg) * 512, OFF_XF);
    }
}
__device__ __forceinline__ void prepare(rsrc_t rs, const u16* mem0, int g) {
    const v4u ones = {~0u, ~0u, ~0u, ~0u};
    v4u first = ones;
    if (g < 1152) first = *(const v4u*)(mem0 + ((g % 576 >> 1) & 7) * DM + (g % 576 >> 4) * 16 + (g & 1) * 8);
    for (int i = g; i < 73728; i += 18432) __builtin_amdgcn_raw_buffer_store_b128(ones, rs, (i < 36864 ? i : i + 65536 - 36864) * 16, OFF_PART, FPOL);
    if (g >= 2048) __builtin_amdgcn_raw_buffer_store_b128(ones, rs, (g & 8191) * 16 + (g < 10240 ? OFF_KM : OFF_VM), 62 * 65536, FPOL);
    for (int i = g; i < 32768; i += 18432) __builtin_amdgcn_raw_buffer_store_b128(ones, rs, i * 16, OFF_OF + 62 * 262144, FPOL);
    for (int i = g + 55296; i >= 0; i -= 18432) {
        const int s = i / 1152, r = i % 1152, par = r / 576, e = r % 576;
        v4u val = ones;
        if (s == 0)
#pragma unroll
            for (int j = 0; j < 4; ++j) val[j] = first[j] == ~0u ? 0x7fc07fc0u : first[j];
        __builtin_amdgcn_raw_buffer_store_b128(val, rs, s * 32768 + (e >> 9) * 16384 + par * 8192 + (e & 511) * 16, OFF_MEM, FPOL);
    }
}
__device__ __forceinline__ void pull_keys(const u16* W, int xc, int j, int lane) {
    const rsrc_t rw = __builtin_amdgcn_make_buffer_rsrc((void*)W, 0, 4718592, 0x00020000);
    for (int r = (j >> 1) + KS; r < 576; r += 36)
        asm volatile("s_mov_b32 m0, 0\n\ts_nop 0\n\tbuffer_load_dwordx4 %0, %1, %2 offen lds\n\tbuffer_load_dwordx4 %0, %1, %2 offen offset:1024 lds" :: "v"(lane * 16), "s"(rw), "s"(r * 8192 + xc * 2048) : "memory");
}
__device__ __forceinline__ void pull_flat(const u16* W, int j, int w, int lane) {
    const rsrc_t rw = __builtin_amdgcn_make_buffer_rsrc((void*)W, 0, 5308416, 0x00020000);
    for (int r = j; r < 2592; r += 288)
        asm volatile("s_mov_b32 m0, %3\n\ts_nop 0\n\tbuffer_load_dwordx4 %0, %1, %2 offen lds\n\tbuffer_load_dwordx4 %0, %1, %2 offen offset:1024 lds" :: "v"(lane * 16), "s"(rw), "s"(r * 2048), "s"(w * 40960 + 36864) : "memory");
}

static_assert(FL_ACK == 4347904 && FL_KM == 4198400 && FL_KV == 4194304 && OFF_GO == 4456448 && OFF_KF == 8388608 && OFF_KM == 96468992 && OFF_PART == 2097152 && OFF_RDY == 4489216 && OFF_VF == 25165824 && OFF_VM == 100663296 && SPIN_MAX == 65536 && WS_SPAN == 109051904, "constants of the assembly block");
__device__ __forceinline__ void chain_fast(int hd, const u16* Wq, const u16* Wk, const u16* Wv, const u16* Wo, unsigned char* ws, int blk, int hop, int off, int cyc, int extra, int bias, int bump, char* lds, int w, int lane) {
    if (w == 0) {
        const unsigned g = lane >> 4, n = lane & 15, hf = lane & 1, i = n >> 1, q = (lane >> 2) & 3, p = lane & 3;
        const unsigned y = (n & 7) * 16 + (n >> 3) * 256;
        v4u* c = (v4u*)lds + lane;
        c[0] = v4u{hf ? 0xeeeeu : 0x4444u, (n >> 3) * 1024 + g * 64 + (n & 7) * 4,
                   (8 * (q >> 1) + (q & 1)) * 8192 + g * 64 + p * 16, (8 * (i & 1) + 2 * (i & 2) + 2 * (i >> 2)) * 1152 + g * 32 + hf * 16};
        const unsigned tq = g * 1024 + q * 64 + 8 * (p & 1) + 32 * (p >> 1);
        c[64] = v4u{tq + 16 * (g & 1), tq + 16 * (1 ^ (g & 1)), (g >> 1) * 1024 + (lane & 31) * 8, 0};
        c[256] = v4u{0, 0, 0, 0};
        c[320] = v4u{g * 512 + y, (n & 7) * 32 + (n < 8 ? 4096 + (g & 1) * 16 + (g >> 1) * 8 : 4608 + (g >> 1) * 256 + (g & 1) * 8), n < 8 ? 1u : 0u, g * 4 + y};
        c[384] = v4u{2048 + (n >> 3) * 1024 + ((n >> 1) & 1) * 128 + g * 32 + (2 * (n & 1) + ((n >> 2) & 1)) * 4, g * 128 + (n & 7) * 16, 0, 0};
        c[448] = v4u{g * 2048 + hf * 512 + i * 16, (g >> 1) * 4096 + (lane & 31) * 4, 4096 + (lane >> 2) * 256 + (lane & 2) * 32 + hf * 16, 0};
    }
    __syncthreads();
    asm volatile(
        "s_mov_b64 s[40:41], %0\n"
        "s_mov_b32 s42, 0x6800000\n"
        "s_mov_b32 s43, 0x20000\n"
        "s_mov_b32 s46, 4718592\n"
        "s_mov_b32 s47, 0x20000\n"
        "s_mov_b32 s48, 0\n"
        "s_lshl_b32 s65, %6, 8\n"
        "s_lshl_b32 s78, %5, 13\n"
        "s_lshl_b32 s79, %6, 11\n"
        "s_add_u32 s53, s78, s79\n"
        "s_add_u32 s53, s53, 0x1800000\n"
        "s_add_u32 s78, s78, 0x800000\n"
        "s_lshr_b32 s79, %6, 1\n"
        "s_mul_i32 s83, s79, 0x1800000-0x800000\n"
        "s_lshl_b32 s79, s79, 7\n"
        "s_add_u32 s52, s78, s79\n"
        "s_add_u32 s83, s83, s78\n"
        "s_and_b32 s79, %6, 1\n"
        "s_lshl_b32 s79, s79, 8\n"
        "s_add_u32 s52, s52, s79\n"
        "s_lshl4_add_u32 s83, s79, s83\n"
        "s_add_u32 s39, s83, 262144\n"
        "s_lshl_b32 s78, %5, 11\n"
        "s_add_u32 s55, s78, 0x5c00000\n"
        "s_lshl_b32 s79, %6, 9\n"
        "s_add_u32 s54, s78, s79\n"
        "s_add_u32 s54, s54, 0x6000000\n"
        "s_lshl_b32 s78, %5, 7\n"
        "s_add_u32 s82, s78, 0x400000\n"
        "s_mov_b32 s66, 1\n"
        "s_mov_b32 s29, %8\n"
        "s_mov_b32 s28, %9\n"
        "s_mov_b32 s31, %9\n"
        "s_mov_b32 s80, %9\n"
        "s_mov_b32 s68, 0\n"
        "s_memrealtime s[84:85]\n"
        "s_memtime s[86:87]\n"
        "s_mov_b32 s69, 0\n"
        "s_mov_b32 s67, 0x10000\n"
        "s_mov_b32 s70, 0x3f803f80\n"
        "s_mov_b32 s71, 0x3f803f80\n"
        "s_mov_b32 s72, 0xaaaaaaaa\n"
        "s_mov_b32 s73, 0xaaaaaaaa\n"
        "s_lshr_b32 s78, %5, 1\n"
        "s_lshl_b32 s78, s78, 8\n"
        "s_and_b32 s79, %5, 1\n"
        "s_lshl_b32 s79, s79, 6\n"
        "s_add_u32 s81, s78, s79\n"
        "s_lshl_b32 s79, %6, 2\n"
        "s_add_u32 s81, s81, s79\n"
        "s_add_u32 s81, s81, 20480\n"
        "s_and_b32 s78, %6, 1\n"
        "s_lshl_b32 s78, s78, 8\n"
        "s_lshr_b32 s79, %6, 1\n"
        "s_lshl2_add_u32 s38, s79, s78\n"
        "s_lshl_b32 s78, %6, 4\n"
        "s_bfm_b64 s[36:37], 16, s78\n"
        "s_add_u32 s78, %6, 0\n"
        "s_add_u32 s79, s78, %5\n"
        "s_and_b32 s79, s79, 31\n"
        "s_lshl_b32 s79, s79, 7\n"
        "s_lshl_b32 s78, s78, 14\n"
        "s_add_u32 s56, s78, s79\n"
        "s_add_u32 s78, %6, 4\n"
        "s_add_u32 s79, s78, %5\n"
        "s_and_b32 s79, s79, 31\n"
        "s_lshl_b32 s79, s79, 7\n"
        "s_lshl_b32 s78, s78, 14\n"
        "s_add_u32 s57, s78, s79\n"
        "s_add_u32 s78, %6, 8\n"
        "s_add_u32 s79, s78, %5\n"
        "s_and_b32 s79, s79, 31\n"
        "s_lshl_b32 s79, s79, 7\n"
        "s_lshl_b32 s78, s78, 14\n"
        "s_add_u32 s58, s78, s79\n"
        "s_add_u32 s78, %6, 12\n"
        "s_add_u32 s79, s78, %5\n"
        "s_and_b32 s79, s79, 31\n"
        "s_lshl_b32 s79, s79, 7\n"
        "s_lshl_b32 s78, s78, 14\n"
        "s_add_u32 s59, s78, s79\n"
        "s_add_u32 s78, %6, 16\n"
        "s_add_u32 s79, s78, %5\n"
        "s_and_b32 s79, s79, 31\n"
        "s_lshl_b32 s79, s79, 7\n"
        "s_lshl_b32 s78, s78, 14\n"
        "s_add_u32 s60, s78, s79\n"
        "s_add_u32 s78, %6, 20\n"
        "s_add_u32 s79, s78, %5\n"
        "s_and_b32 s79, s79, 31\n"
        "s_lshl_b32 s79, s79, 7\n"
        "s_lshl_b32 s78, s78, 14\n"
        "s_add_u32 s61, s78, s79\n"
        "s_add_u32 s78, %6, 24\n"
        "s_add_u32 s79, s78, %5\n"
        "s_and_b32 s79, s79, 31\n"
        "s_lshl_b32 s79, s79, 7\n"
        "s_lshl_b32 s78, s78, 14\n"
        "s_add_u32 s62, s78, s79\n"
        "s_add_u32 s78, %6, 28\n"
        "s_add_u32 s79, s78, %5\n"
        "s_and_b32 s79, s79, 31\n"
        "s_lshl_b32 s79, s79, 7\n"
        "s_lshl_b32 s78, s78, 14\n"
        "s_add_u32 s63, s78, s79\n"
        "s_add_u32 s78, %6, 32\n"
        "s_add_u32 s79, s78, %5\n"
        "s_and_b32 s79, s79, 31\n"
        "s_lshl_b32 s79, s79, 7\n"
        "s_lshl_b32 s78, s78, 14\n"
        "s_add_u32 s64, s78, s79\n"
        "v_mbcnt_lo_u32_b32 v85, -1, 0\n"
        "v_mbcnt_hi_u32_b32 v85, -1, v85\n"
        "v_lshlrev_b32 v85, 4, v85\n"
        "ds_read_b128 v[20:23], v85\n"
        "ds_read_b128 v[24:27], v85 offset:1024\n"
        "s_mul_i32 s74, %6, 38912\n"
        "s_add_u32 s74, s74, 8192\n"
        "s_lshr_b32 s77, %6, 1\n"
        "s_and_b32 s79, %6, 1\n"
        "s_lshl4_add_u32 s78, s77, s79\n"
        "s_lshl_b32 s78, s78, 14\n"
        "s_lshl_b32 s75, %5, 8\n"
        "s_add_u32 s75, s75, s78\n"
        "s_lshl1_add_u32 s76, %5, s79\n"
        "s_mul_i32 s76, s76, 73728\n"
        "v_add_u32 v86, s74, v85\n"
        "s_waitcnt lgkmcnt(1)\n"
        "v_mov_b32 v84, v20\n"
        "v_add_u32 v87, s65, v21\n"
        "s_waitcnt lgkmcnt(0)\n"
        "v_xor_b32 v27, 16, v22\n"
        "s_add_u32 s78, s65, 8192\n"
        "v_add_u32 v44, s78, v24\n"
        "v_add_u32 v48, s78, v25\n"
        "v_add_u32 v52, s78, v26\n"
        "s_add_u32 s78, s65, 47104\n"
        "v_add_u32 v45, s78, v24\n"
        "v_add_u32 v49, s78, v25\n"
        "v_add_u32 v53, s78, v26\n"
        "s_add_u32 s78, s65, 86016\n"
        "v_add_u32 v46, s78, v24\n"
        "v_add_u32 v50, s78, v25\n"
        "v_add_u32 v54, s78, v26\n"
        "s_add_u32 s78, s65, 124928\n"
        "v_add_u32 v47, s78, v24\n"
        "v_add_u32 v51, s78, v25\n"
        "v_add_u32 v55, s78, v26\n"
        "s_lshl_b32 s78, %7, 7\n"
        "s_add_u32 s78, s78, 0x440000\n"
        "buffer_load_dwordx2 v[16:17], off, s[40:43], s78 sc1\n"
        "s_lshl_b32 s27, %7, 5\n"
        "s_add_u32 s27, s27, %6\n"
        "s_lshl2_add_u32 s27, s27, 0x448000\n"
        "s_mov_b64 s[44:45], %1\n"
        "s_add_u32 m0, s74, 0\n"
        "s_add_u32 s78, s75, 524288\n"
        "buffer_load_dwordx4 v22, s[44:47], s78 offen lds\n"
        "s_add_u32 m0, s74, 1024\n"
        "s_add_u32 s78, s75, 557056\n"
        "buffer_load_dwordx4 v27, s[44:47], s78 offen lds\n"
        "s_add_u32 m0, s74, 2048\n"
        "s_add_u32 s78, s75, 655360\n"
        "buffer_load_dwordx4 v22, s[44:47], s78 offen lds\n"
        "s_add_u32 m0, s74, 3072\n"
        "s_add_u32 s78, s75, 688128\n"
        "buffer_load_dwordx4 v27, s[44:47], s78 offen lds\n"
        "s_add_u32 m0, s74, 4096\n"
        "s_add_u32 s78, s75, 1572864\n"
        "buffer_load_dwordx4 v22, s[44:47], s78 offen lds\n"
        "s_add_u32 m0, s74, 5120\n"
        "s_add_u32 s78, s75, 1605632\n"
        "buffer_load_dwordx4 v27, s[44:47], s78 offen lds\n"
        "s_add_u32 m0, s74, 6144\n"
        "s_add_u32 s78, s75, 1703936\n"
        "buffer_load_dwordx4 v22, s[44:47], s78 offen lds\n"
        "s_add_u32 m0, s74, 7168\n"
        "s_add_u32 s78, s75, 1736704\n"
        "buffer_load_dwordx4 v27, s[44:47], s78 offen lds\n"
        "s_add_u32 m0, s74, 8192\n"
        "s_add_u32 s78, s75, 2621440\n"
        "buffer_load_dwordx4 v22, s[44:47], s78 offen lds\n"
        "s_add_u32 m0, s74, 9216\n"
        "s_add_u32 s78, s75, 2654208\n"
        "buffer_load_dwordx4 v27, s[44:47], s78 offen lds\n"
        "s_add_u32 m0, s74, 10240\n"
        "s_add_u32 s78, s75, 2752512\n"
        "buffer_load_dwordx4 v22, s[44:47], s78 offen lds\n"
        "s_add_u32 m0, s74, 11264\n"
        "s_add_u32 s78, s75, 2785280\n"
        "buffer_load_dwordx4 v27, s[44:47], s78 offen lds\n"
        "s_add_u32 m0, s74, 12288\n"
        "s_add_u32 s78, s75, 3670016\n"
        "buffer_load_dwordx4 v22, s[44:47], s78 offen lds\n"
        "s_add_u32 m0, s74, 13312\n"
        "s_add_u32 s78, s75, 3702784\n"
        "buffer_load_dwordx4 v27, s[44:47], s78 offen lds\n"
        "s_add_u32 m0, s74, 14336\n"
        "s_add_u32 s78, s75, 3801088\n"
        "buffer_load_dwordx4 v22, s[44:47], s78 offen lds\n"
        "s_add_u32 m0, s74, 15360\n"
        "s_add_u32 s78, s75, 3833856\n"
        "buffer_load_dwordx4 v27, s[44:47], s78 offen lds\n"
        "s_add_u32 m0, s74, 16384\n"
        "s_add_u32 s78, s75, 4194304\n"
        "buffer_load_dwordx4 v22, s[44:47], s78 offen lds\n"
        "s_add_u32 m0, s74, 17408\n"
        "s_add_u32 s78, s75, 4227072\n"
        "buffer_load_dwordx4 v27, s[44:47], s78 offen lds\n"
        "s_add_u32 m0, s74, 18432\n"
        "s_add_u32 s78, s75, 4325376\n"
        "buffer_load_dwordx4 v22, s[44:47], s78 offen lds\n"
        "s_add_u32 m0, s74, 19456\n"
        "s_add_u32 s78, s75, 4358144\n"
        "buffer_load_dwordx4 v27, s[44:47], s78 offen lds\n"
        "s_mov_b64 s[44:45], %4\n"
        "s_mul_i32 s79, s77, 128\n"
        "s_add_u32 s79, s79, s76\n"
        "s_add_u32 m0, s74, 20480\n"
        "s_add_u32 s78, s79, 0\n"
        "buffer_load_dwordx4 v23, s[44:47], s78 offen lds\n"
        "s_add_u32 m0, s74, 21504\n"
        "s_add_u32 s78, s79, 36864\n"
        "buffer_load_dwordx4 v23, s[44:47], s78 offen lds\n"
        "s_add_u32 m0, s74, 22528\n"
        "s_add_u32 s78, s79, 1152\n"
        "buffer_load_dwordx4 v23, s[44:47], s78 offen lds\n"
        "s_add_u32 m0, s74, 23552\n"
        "s_add_u32 s78, s79, 38016\n"
        "buffer_load_dwordx4 v23, s[44:47], s78 offen lds\n"
        "s_mul_i32 s79, s77, 17408\n"
        "s_add_u32 s79, s79, s76\n"
        "s_add_u32 m0, s74, 24576\n"
        "s_add_u32 s78, s79, 1024\n"
        "buffer_load_dwordx4 v23, s[44:47], s78 offen lds\n"
        "s_add_u32 m0, s74, 25600\n"
        "s_add_u32 s78, s79, 37888\n"
        "buffer_load_dwordx4 v23, s[44:47], s78 offen lds\n"
        "s_add_u32 m0, s74, 26624\n"
        "s_add_u32 s78, s79, 2176\n"
        "buffer_load_dwordx4 v23, s[44:47], s78 offen lds\n"
        "s_add_u32 m0, s74, 27648\n"
        "s_add_u32 s78, s79, 39040\n"
        "buffer_load_dwordx4 v23, s[44:47], s78 offen lds\n"
        "s_mul_i32 s79, s77, 128\n"
        "s_add_u32 s79, s79, s76\n"
        "s_add_u32 m0, s74, 28672\n"
        "s_add_u32 s78, s79, 256\n"
        "buffer_load_dwordx4 v23, s[44:47], s78 offen lds\n"
        "s_add_u32 m0, s74, 29696\n"
        "s_add_u32 s78, s79, 37120\n"
        "buffer_load_dwordx4 v23, s[44:47], s78 offen lds\n"
        "s_add_u32 m0, s74, 30720\n"
        "s_add_u32 s78, s79, 1408\n"
        "buffer_load_dwordx4 v23, s[44:47], s78 offen lds\n"
        "s_add_u32 m0, s74, 31744\n"
        "s_add_u32 s78, s79, 38272\n"
        "buffer_load_dwordx4 v23, s[44:47], s78 offen lds\n"
        "s_mul_i32 s79, s77, 128\n"
        "s_add_u32 s79, s79, s76\n"
        "s_add_u32 m0, s74, 32768\n"
        "s_add_u32 s78, s79, 512\n"
        "buffer_load_dwordx4 v23, s[44:47], s78 offen lds\n"
        "s_add_u32 m0, s74, 33792\n"
        "s_add_u32 s78, s79, 37376\n"
        "buffer_load_dwordx4 v23, s[44:47], s78 offen lds\n"
        "s_add_u32 m0, s74, 34816\n"
        "s_add_u32 s78, s79, 1664\n"
        "buffer_load_dwordx4 v23, s[44:47], s78 offen lds\n"
        "s_add_u32 m0, s74, 35840\n"
        "s_add_u32 s78, s79, 38528\n"
        "buffer_load_dwordx4 v23, s[44:47], s78 offen lds\n"
        "s_waitcnt vmcnt(32) lgkmcnt(0)\n"
        "s_barrier\n"
        "ds_read_b64_tr_b16 a[0:1], v44 offset:0\n"
        "ds_read_b64_tr_b16 a[2:3], v45 offset:0\n"
        "ds_read_b64_tr_b16 a[4:5], v46 offset:0\n"
        "ds_read_b64_tr_b16 a[6:7], v47 offset:0\n"
        "ds_read_b64_tr_b16 a[8:9], v48 offset:0\n"
        "ds_read_b64_tr_b16 a[10:11], v49 offset:0\n"
        "ds_read_b64_tr_b16 a[12:13], v50 offset:0\n"
        "ds_read_b64_tr_b16 a[14:15], v51 offset:0\n"
        "s_waitcnt vmcnt(28) lgkmcnt(0)\n"
        "s_barrier\n"
        "ds_read_b64_tr_b16 a[16:17], v44 offset:4096\n"
        "ds_read_b64_tr_b16 a[18:19], v45 offset:4096\n"
        "ds_read_b64_tr_b16 a[20:21], v46 offset:4096\n"
        "ds_read_b64_tr_b16 a[22:23], v47 offset:4096\n"
        "ds_read_b64_tr_b16 a[24:25], v48 offset:4096\n"
        "ds_read_b64_tr_b16 a[26:27], v49 offset:4096\n"
        "ds_read_b64_tr_b16 a[28:29], v50 offset:4096\n"
        "ds_read_b64_tr_b16 a[30:31], v51 offset:4096\n"
        "s_mul_i32 s79, s77, 128\n"
        "s_add_u32 s79, s79, s76\n"
        "s_add_u32 m0, s74, 0\n"
        "s_add_u32 s78, s79, 768\n"
        "buffer_load_dwordx4 v23, s[44:47], s78 offen lds\n"
        "s_add_u32 m0, s74, 1024\n"
        "s_add_u32 s78, s79, 37632\n"
        "buffer_load_dwordx4 v23, s[44:47], s78 offen lds\n"
        "s_add_u32 m0, s74, 2048\n"
        "s_add_u32 s78, s79, 1920\n"
        "buffer_load_dwordx4 v23, s[44:47], s78 offen lds\n"
        "s_add_u32 m0, s74, 3072\n"
        "s_add_u32 s78, s79, 38784\n"
        "buffer_load_dwordx4 v23, s[44:47], s78 offen lds\n"
        "s_waitcnt vmcnt(28) lgkmcnt(0)\n"
        "s_barrier\n"
        "ds_read_b64_tr_b16 a[32:33], v44 offset:8192\n"
        "ds_read_b64_tr_b16 a[34:35], v45 offset:8192\n"
        "ds_read_b64_tr_b16 a[36:37], v46 offset:8192\n"
        "ds_read_b64_tr_b16 a[38:39], v47 offset:8192\n"
        "ds_read_b64_tr_b16 a[40:41], v48 offset:8192\n"
        "ds_read_b64_tr_b16 a[42:43], v49 offset:8192\n"
        "ds_read_b64_tr_b16 a[44:45], v50 offset:8192\n"
        "ds_read_b64_tr_b16 a[46:47], v51 offset:8192\n"
        "s_mov_b64 s[44:45], %2\n"
        "s_add_u32 m0, s74, 4096\n"
        "s_add_u32 s78, s75, 524288\n"
        "buffer_load_dwordx4 v22, s[44:47], s78 offen lds\n"
        "s_add_u32 m0, s74, 5120\n"
        "s_add_u32 s78, s75, 557056\n"
        "buffer_load_dwordx4 v27, s[44:47], s78 offen lds\n"
        "s_add_u32 m0, s74, 6144\n"
        "s_add_u32 s78, s75, 655360\n"
        "buffer_load_dwordx4 v22, s[44:47], s78 offen lds\n"
        "s_add_u32 m0, s74, 7168\n"
        "s_add_u32 s78, s75, 688128\n"
        "buffer_load_dwordx4 v27, s[44:47], s78 offen lds\n"
        "s_waitcnt vmcnt(28) lgkmcnt(0)\n"
        "s_barrier\n"
        "ds_read_b64_tr_b16 a[48:49], v44 offset:12288\n"
        "ds_read_b64_tr_b16 a[50:51], v45 offset:12288\n"
        "ds_read_b64_tr_b16 a[52:53], v46 offset:12288\n"
        "ds_read_b64_tr_b16 a[54:55], v47 offset:12288\n"
        "ds_read_b64_tr_b16 a[56:57], v48 offset:12288\n"
        "ds_read_b64_tr_b16 a[58:59], v49 offset:12288\n"
        "ds_read_b64_tr_b16 a[60:61], v50 offset:12288\n"
        "ds_read_b64_tr_b16 a[62:63], v51 offset:12288\n"
        "s_add_u32 m0, s74, 8192\n"
        "s_add_u32 s78, s75, 1048576\n"
        "buffer_load_dwordx4 v22, s[44:47], s78 offen lds\n"
        "s_add_u32 m0, s74, 9216\n"
        "s_add_u32 s78, s75, 1081344\n"
        "buffer_load_dwordx4 v27, s[44:47], s78 offen lds\n"
        "s_add_u32 m0, s74, 10240\n"
        "s_add_u32 s78, s75, 1179648\n"
        "buffer_load_dwordx4 v22, s[44:47], s78 offen lds\n"
        "s_add_u32 m0, s74, 11264\n"
        "s_add_u32 s78, s75, 1212416\n"
        "buffer_load_dwordx4 v27, s[44:47], s78 offen lds\n"
        "s_waitcnt vmcnt(28) lgkmcnt(0)\n"
        "s_barrier\n"
        "ds_read_b64_tr_b16 a[64:65], v44 offset:16384\n"
        "ds_read_b64_tr_b16 a[66:67], v45 offset:16384\n"
        "ds_read_b64_tr_b16 a[68:69], v46 offset:16384\n"
        "ds_read_b64_tr_b16 a[70:71], v47 offset:16384\n"
        "ds_read_b64_tr_b16 a[72:73], v48 offset:16384\n"
        "ds_read_b64_tr_b16 a[74:75], v49 offset:16384\n"
        "ds_read_b64_tr_b16 a[76:77], v50 offset:16384\n"
        "ds_read_b64_tr_b16 a[78:79], v51 offset:16384\n"
        "s_add_u32 m0, s74, 12288\n"
        "s_add_u32 s78, s75, 1572864\n"
        "buffer_load_dwordx4 v22, s[44:47], s78 offen lds\n"
        "s_add_u32 m0, s74, 13312\n"
        "s_add_u32 s78, s75, 1605632\n"
        "buffer_load_dwordx4 v27, s[44:47], s78 offen lds\n"
        "s_add_u32 m0, s74, 14336\n"
        "s_add_u32 s78, s75, 1703936\n"
        "buffer_load_dwordx4 v22, s[44:47], s78 offen lds\n"
        "s_add_u32 m0, s74, 15360\n"
        "s_add_u32 s78, s75, 1736704\n"
        "buffer_load_dwordx4 v27, s[44:47], s78 offen lds\n"
        "s_waitcnt vmcnt(28) lgkmcnt(0)\n"
        "s_barrier\n"
        "ds_read_b64_tr_b16 v[184:185], v52 offset:20480\n"
        "ds_read_b64_tr_b16 v[186:187], v52 offset:22528\n"
        "ds_read_b64_tr_b16 v[188:189], v53 offset:20480\n"
        "ds_read_b64_tr_b16 v[190:191], v53 offset:22528\n"
        "ds_read_b64_tr_b16 v[192:193], v54 offset:20480\n"
        "ds_read_b64_tr_b16 v[194:195], v54 offset:22528\n"
        "ds_read_b64_tr_b16 v[196:197], v55 offset:20480\n"
        "ds_read_b64_tr_b16 v[198:199], v55 offset:22528\n"
        "s_add_u32 m0, s74, 16384\n"
        "s_add_u32 s78, s75, 2097152\n"
        "buffer_load_dwordx4 v22, s[44:47], s78 offen lds\n"
        "s_add_u32 m0, s74, 17408\n"
        "s_add_u32 s78, s75, 2129920\n"
        "buffer_load_dwordx4 v27, s[44:47], s78 offen lds\n"
        "s_add_u32 m0, s74, 18432\n"
        "s_add_u32 s78, s75, 2228224\n"
        "buffer_load_dwordx4 v22, s[44:47], s78 offen lds\n"
        "s_add_u32 m0, s74, 19456\n"
        "s_add_u32 s78, s75, 2260992\n"
        "buffer_load_dwordx4 v27, s[44:47], s78 offen lds\n"
        "s_waitcnt vmcnt(28) lgkmcnt(0)\n"
        "s_barrier\n"
        "ds_read_b64_tr_b16 v[200:201], v52 offset:24576\n"
        "ds_read_b64_tr_b16 v[202:203], v52 offset:26624\n"
        "ds_read_b64_tr_b16 v[204:205], v53 offset:24576\n"
        "ds_read_b64_tr_b16 v[206:207], v53 offset:26624\n"
        "ds_read_b64_tr_b16 v[8:9], v54 offset:24576\n"
        "ds_read_b64_tr_b16 v[10:11], v54 offset:26624\n"
        "ds_read_b64_tr_b16 v[12:13], v55 offset:24576\n"
        "ds_read_b64_tr_b16 v[14:15], v55 offset:26624\n"
        "s_add_u32 m0, s74, 20480\n"
        "s_add_u32 s78, s75, 2621440\n"
        "buffer_load_dwordx4 v22, s[44:47], s78 offen lds\n"
        "s_add_u32 m0, s74, 21504\n"
        "s_add_u32 s78, s75, 2654208\n"
        "buffer_load_dwordx4 v27, s[44:47], s78 offen lds\n"
        "s_add_u32 m0, s74, 22528\n"
        "s_add_u32 s78, s75, 2752512\n"
        "buffer_load_dwordx4 v22, s[44:47], s78 offen lds\n"
        "s_add_u32 m0, s74, 23552\n"
        "s_add_u32 s78, s75, 2785280\n"
        "buffer_load_dwordx4 v27, s[44:47], s78 offen lds\n"
        "s_waitcnt vmcnt(28) lgkmcnt(0)\n"
        "s_barrier\n"
        "ds_read_b64_tr_b16 v[208:209], v52 offset:28672\n"
        "ds_read_b64_tr_b16 v[210:211], v52 offset:30720\n"
        "ds_read_b64_tr_b16 v[212:213], v53 offset:28672\n"
        "ds_read_b64_tr_b16 v[214:215], v53 offset:30720\n"
        "ds_read_b64_tr_b16 v[216:217], v54 offset:28672\n"
        "ds_read_b64_tr_b16 v[218:219], v54 offset:30720\n"
        "ds_read_b64_tr_b16 v[220:221], v55 offset:28672\n"
        "ds_read_b64_tr_b16 v[222:223], v55 offset:30720\n"
        "ds_write_b128 v86, v[8:11] offset:36864\n"
        "ds_write_b128 v86, v[12:15] offset:37888\n"
        "s_add_u32 m0, s74, 24576\n"
        "s_add_u32 s78, s75, 3145728\n"
        "buffer_load_dwordx4 v22, s[44:47], s78 offen lds\n"
        "s_add_u32 m0, s74, 25600\n"
        "s_add_u32 s78, s75, 3178496\n"
        "buffer_load_dwordx4 v27, s[44:47], s78 offen lds\n"
        "s_add_u32 m0, s74, 26624\n"
        "s_add_u32 s78, s75, 3276800\n"
        "buffer_load_dwordx4 v22, s[44:47], s78 offen lds\n"
        "s_add_u32 m0, s74, 27648\n"
        "s_add_u32 s78, s75, 3309568\n"
        "buffer_load_dwordx4 v27, s[44:47], s78 offen lds\n"
        "s_waitcnt vmcnt(28) lgkmcnt(0)\n"
        "s_barrier\n"
        "ds_read_b64_tr_b16 v[224:225], v52 offset:32768\n"
        "ds_read_b64_tr_b16 v[226:227], v52 offset:34816\n"
        "ds_read_b64_tr_b16 v[228:229], v53 offset:32768\n"
        "ds_read_b64_tr_b16 v[230:231], v53 offset:34816\n"
        "ds_read_b64_tr_b16 v[232:233], v54 offset:32768\n"
        "ds_read_b64_tr_b16 v[234:235], v54 offset:34816\n"
        "ds_read_b64_tr_b16 v[236:237], v55 offset:32768\n"
        "ds_read_b64_tr_b16 v[238:239], v55 offset:34816\n"
        "s_add_u32 m0, s74, 28672\n"
        "s_add_u32 s78, s75, 3670016\n"
        "buffer_load_dwordx4 v22, s[44:47], s78 offen lds\n"
        "s_add_u32 m0, s74, 29696\n"
        "s_add_u32 s78, s75, 3702784\n"
        "buffer_load_dwordx4 v27, s[44:47], s78 offen lds\n"
        "s_add_u32 m0, s74, 30720\n"
        "s_add_u32 s78, s75, 3801088\n"
        "buffer_load_dwordx4 v22, s[44:47], s78 offen lds\n"
        "s_add_u32 m0, s74, 31744\n"
        "s_add_u32 s78, s75, 3833856\n"
        "buffer_load_dwordx4 v27, s[44:47], s78 offen lds\n"
        "s_waitcnt vmcnt(28) lgkmcnt(0)\n"
        "s_barrier\n"
        "ds_read_b64_tr_b16 v[240:241], v52 offset:0\n"
        "ds_read_b64_tr_b16 v[242:243], v52 offset:2048\n"
        "ds_read_b64_tr_b16 v[244:245], v53 offset:0\n"
        "ds_read_b64_tr_b16 v[246:247], v53 offset:2048\n"
        "ds_read_b64_tr_b16 v[248:249], v54 offset:0\n"
        "ds_read_b64_tr_b16 v[250:251], v54 offset:2048\n"
        "ds_read_b64_tr_b16 v[252:253], v55 offset:0\n"
        "ds_read_b64_tr_b16 v[254:255], v55 offset:2048\n"
        "s_add_u32 m0, s74, 32768\n"
        "s_add_u32 s78, s75, 4194304\n"
        "buffer_load_dwordx4 v22, s[44:47], s78 offen lds\n"
        "s_add_u32 m0, s74, 33792\n"
        "s_add_u32 s78, s75, 4227072\n"
        "buffer_load_dwordx4 v27, s[44:47], s78 offen lds\n"
        "s_add_u32 m0, s74, 34816\n"
        "s_add_u32 s78, s75, 4325376\n"
        "buffer_load_dwordx4 v22, s[44:47], s78 offen lds\n"
        "s_add_u32 m0, s74, 35840\n"
        "s_add_u32 s78, s75, 4358144\n"
        "buffer_load_dwordx4 v27, s[44:47], s78 offen lds\n"
        "s_waitcnt vmcnt(28) lgkmcnt(0)\n"
        "s_barrier\n"
        "ds_read_b64_tr_b16 a[80:81], v44 offset:4096\n"
        "ds_read_b64_tr_b16 a[82:83], v45 offset:4096\n"
        "ds_read_b64_tr_b16 a[84:85], v46 offset:4096\n"
        "ds_read_b64_tr_b16 a[86:87], v47 offset:4096\n"
        "ds_read_b64_tr_b16 a[88:89], v48 offset:4096\n"
        "ds_read_b64_tr_b16 a[90:91], v49 offset:4096\n"
        "ds_read_b64_tr_b16 a[92:93], v50 offset:4096\n"
        "ds_read_b64_tr_b16 a[94:95], v51 offset:4096\n"
        "s_mov_b64 s[44:45], %3\n"
        "s_add_u32 m0, s74, 0\n"
        "s_add_u32 s78, s75, 0\n"
        "buffer_load_dwordx4 v22, s[44:47], s78 offen lds\n"
        "s_add_u32 m0, s74, 1024\n"
        "s_add_u32 s78, s75, 32768\n"
        "buffer_load_dwordx4 v27, s[44:47], s78 offen lds\n"
        "s_add_u32 m0, s74, 2048\n"
        "s_add_u32 s78, s75, 131072\n"
        "buffer_load_dwordx4 v22, s[44:47], s78 offen lds\n"
        "s_add_u32 m0, s74, 3072\n"
        "s_add_u32 s78, s75, 163840\n"
        "buffer_load_dwordx4 v27, s[44:47], s78 offen lds\n"
        "s_waitcnt vmcnt(28) lgkmcnt(0)\n"
        "s_barrier\n"
        "ds_read_b64_tr_b16 a[96:97], v44 offset:8192\n"
        "ds_read_b64_tr_b16 a[98:99], v45 offset:8192\n"
        "ds_read_b64_tr_b16 a[100:101], v46 offset:8192\n"
        "ds_read_b64_tr_b16 a[102:103], v47 offset:8192\n"
        "ds_read_b64_tr_b16 a[104:105], v48 offset:8192\n"
        "ds_read_b64_tr_b16 a[106:107], v49 offset:8192\n"
        "ds_read_b64_tr_b16 a[108:109], v50 offset:8192\n"
        "ds_read_b64_tr_b16 a[110:111], v51 offset:8192\n"
        "s_add_u32 m0, s74, 4096\n"
        "s_add_u32 s78, s75, 524288\n"
        "buffer_load_dwordx4 v22, s[44:47], s78 offen lds\n"
        "s_add_u32 m0, s74, 5120\n"
        "s_add_u32 s78, s75, 557056\n"
        "buffer_load_dwordx4 v27, s[44:47], s78 offen lds\n"
        "s_add_u32 m0, s74, 6144\n"
        "s_add_u32 s78, s75, 655360\n"
        "buffer_load_dwordx4 v22, s[44:47], s78 offen lds\n"
        "s_add_u32 m0, s74, 7168\n"
        "s_add_u32 s78, s75, 688128\n"
        "buffer_load_dwordx4 v27, s[44:47], s78 offen lds\n"
        "s_waitcnt vmcnt(28) lgkmcnt(0)\n"
        "s_barrier\n"
        "ds_read_b64_tr_b16 a[112:113], v44 offset:12288\n"
        "ds_read_b64_tr_b16 a[114:115], v45 offset:12288\n"
        "ds_read_b64_tr_b16 a[116:117], v46 offset:12288\n"
        "ds_read_b64_tr_b16 a[118:119], v47 offset:12288\n"
        "ds_read_b64_tr_b16 a[120:121], v48 offset:12288\n"
        "ds_read_b64_tr_b16 a[122:123], v49 offset:12288\n"
        "ds_read_b64_tr_b16 a[124:125], v50 offset:12288\n"
        "ds_read_b64_tr_b16 a[126:127], v51 offset:12288\n"
        "s_add_u32 m0, s74, 8192\n"
        "s_add_u32 s78, s75, 1048576\n"
        "buffer_load_dwordx4 v22, s[44:47], s78 offen lds\n"
        "s_add_u32 m0, s74, 9216\n"
        "s_add_u32 s78, s75, 1081344\n"
        "buffer_load_dwordx4 v27, s[44:47], s78 offen lds\n"
        "s_add_u32 m0, s74, 10240\n"
        "s_add_u32 s78, s75, 1179648\n"
        "buffer_load_dwordx4 v22, s[44:47], s78 offen lds\n"
        "s_add_u32 m0, s74, 11264\n"
        "s_add_u32 s78, s75, 1212416\n"
        "buffer_load_dwordx4 v27, s[44:47], s78 offen lds\n"
        "s_waitcnt vmcnt(28) lgkmcnt(0)\n"
        "s_barrier\n"
        "ds_read_b64_tr_b16 a[128:129], v44 offset:16384\n"
        "ds_read_b64_tr_b16 a[130:131], v45 offset:16384\n"
        "ds_read_b64_tr_b16 a[132:133], v46 offset:16384\n"
        "ds_read_b64_tr_b16 a[134:135], v47 offset:16384\n"
        "ds_read_b64_tr_b16 a[136:137], v48 offset:16384\n"
        "ds_read_b64_tr_b16 a[138:139], v49 offset:16384\n"
        "ds_read_b64_tr_b16 a[140:141], v50 offset:16384\n"
        "ds_read_b64_tr_b16 a[142:143], v51 offset:16384\n"
        "s_add_u32 m0, s74, 12288\n"
        "s_add_u32 s78, s75, 1572864\n"
        "buffer_load_dwordx4 v22, s[44:47], s78 offen lds\n"
        "s_add_u32 m0, s74, 13312\n"
        "s_add_u32 s78, s75, 1605632\n"
        "buffer_load_dwordx4 v27, s[44:47], s78 offen lds\n"
        "s_add_u32 m0, s74, 14336\n"
        "s_add_u32 s78, s75, 1703936\n"
        "buffer_load_dwordx4 v22, s[44:47], s78 offen lds\n"
        "s_add_u32 m0, s74, 15360\n"
        "s_add_u32 s78, s75, 1736704\n"
        "buffer_load_dwordx4 v27, s[44:47], s78 offen lds\n"
        "s_waitcnt vmcnt(28) lgkmcnt(0)\n"
        "s_barrier\n"
        "ds_read_b64_tr_b16 a[144:145], v44 offset:20480\n"
        "ds_read_b64_tr_b16 a[146:147], v45 offset:20480\n"
        "ds_read_b64_tr_b16 a[148:149], v46 offset:20480\n"
        "ds_read_b64_tr_b16 a[150:151], v47 offset:20480\n"
        "ds_read_b64_tr_b16 a[152:153], v48 offset:20480\n"
        "ds_read_b64_tr_b16 a[154:155], v49 offset:20480\n"
        "ds_read_b64_tr_b16 a[156:157], v50 offset:20480\n"
        "ds_read_b64_tr_b16 a[158:159], v51 offset:20480\n"
        "s_add_u32 m0, s74, 16384\n"
        "s_add_u32 s78, s75, 2097152\n"
        "buffer_load_dwordx4 v22, s[44:47], s78 offen lds\n"
        "s_add_u32 m0, s74, 17408\n"
        "s_add_u32 s78, s75, 2129920\n"
        "buffer_load_dwordx4 v27, s[44:47], s78 offen lds\n"
        "s_add_u32 m0, s74, 18432\n"
        "s_add_u32 s78, s75, 2228224\n"
        "buffer_load_dwordx4 v22, s[44:47], s78 offen lds\n"
        "s_add_u32 m0, s74, 19456\n"
        "s_add_u32 s78, s75, 2260992\n"
        "buffer_load_dwordx4 v27, s[44:47], s78 offen lds\n"
        "s_waitcnt vmcnt(28) lgkmcnt(0)\n"
        "s_barrier\n"
        "ds_read_b64_tr_b16 a[160:161], v44 offset:24576\n"
        "ds_read_b64_tr_b16 a[162:163], v45 offset:24576\n"
        "ds_read_b64_tr_b16 a[164:165], v46 offset:24576\n"
        "ds_read_b64_tr_b16 a[166:167], v47 offset:24576\n"
        "ds_read_b64_tr_b16 a[168:169], v48 offset:24576\n"
        "ds_read_b64_tr_b16 a[170:171], v49 offset:24576\n"
        "ds_read_b64_tr_b16 a[172:173], v50 offset:24576\n"
        "ds_read_b64_tr_b16 a[174:175], v51 offset:24576\n"
        "s_add_u32 m0, s74, 20480\n"
        "s_add_u32 s78, s75, 2621440\n"
        "buffer_load_dwordx4 v22, s[44:47], s78 offen lds\n"
        "s_add_u32 m0, s74, 21504\n"
        "s_add_u32 s78, s75, 2654208\n"
        "buffer_load_dwordx4 v27, s[44:47], s78 offen lds\n"
        "s_add_u32 m0, s74, 22528\n"
        "s_add_u32 s78, s75, 2752512\n"
        "buffer_load_dwordx4 v22, s[44:47], s78 offen lds\n"
        "s_add_u32 m0, s74, 23552\n"
        "s_add_u32 s78, s75, 2785280\n"
        "buffer_load_dwordx4 v27, s[44:47], s78 offen lds\n"
        "s_waitcnt vmcnt(28) lgkmcnt(0)\n"
        "s_barrier\n"
        "ds_read_b64_tr_b16 a[176:177], v44 offset:28672\n"
        "ds_read_b64_tr_b16 a[178:179], v45 offset:28672\n"
        "ds_read_b64_tr_b16 a[180:181], v46 offset:28672\n"
        "ds_read_b64_tr_b16 a[182:183], v47 offset:28672\n"
        "ds_read_b64_tr_b16 a[184:185], v48 offset:28672\n"
        "ds_read_b64_tr_b16 a[186:187], v49 offset:28672\n"
        "ds_read_b64_tr_b16 a[188:189], v50 offset:28672\n"
        "ds_read_b64_tr_b16 a[190:191], v51 offset:28672\n"
        "s_memrealtime vcc\n"
        "s_waitcnt lgkmcnt(0)\n"
        "v_mov_b32 v56, vcc_lo\n"
        "buffer_store_dword v56, off, s[40:43], s27 sc1\n"
        "s_add_u32 m0, s74, 24576\n"
        "s_add_u32 s78, s75, 3145728\n"
        "buffer_load_dwordx4 v22, s[44:47], s78 offen lds\n"
        "s_add_u32 m0, s74, 25600\n"
        "s_add_u32 s78, s75, 3178496\n"
        "buffer_load_dwordx4 v27, s[44:47], s78 offen lds\n"
        "s_add_u32 m0, s74, 26624\n"
        "s_add_u32 s78, s75, 3276800\n"
        "buffer_load_dwordx4 v22, s[44:47], s78 offen lds\n"
        "s_add_u32 m0, s74, 27648\n"
        "s_add_u32 s78, s75, 3309568\n"
        "buffer_load_dwordx4 v27, s[44:47], s78 offen lds\n"
        "s_waitcnt vmcnt(28) lgkmcnt(0)\n"
        "s_barrier\n"
        "ds_read_b64_tr_b16 a[192:193], v44 offset:32768\n"
        "ds_read_b64_tr_b16 a[194:195], v45 offset:32768\n"
        "ds_read_b64_tr_b16 a[196:197], v46 offset:32768\n"
        "ds_read_b64_tr_b16 a[198:199], v47 offset:32768\n"
        "ds_read_b64_tr_b16 a[200:201], v48 offset:32768\n"
        "ds_read_b64_tr_b16 a[202:203], v49 offset:32768\n"
        "ds_read_b64_tr_b16 a[204:205], v50 offset:32768\n"
        "ds_read_b64_tr_b16 a[206:207], v51 offset:32768\n"
        "s_add_u32 m0, s74, 28672\n"
        "s_add_u32 s78, s75, 3670016\n"
        "buffer_load_dwordx4 v22, s[44:47], s78 offen lds\n"
        "s_add_u32 m0, s74, 29696\n"
        "s_add_u32 s78, s75, 3702784\n"
        "buffer_load_dwordx4 v27, s[44:47], s78 offen lds\n"
        "s_add_u32 m0, s74, 30720\n"
        "s_add_u32 s78, s75, 3801088\n"
        "buffer_load_dwordx4 v22, s[44:47], s78 offen lds\n"
        "s_add_u32 m0, s74, 31744\n"
        "s_add_u32 s78, s75, 3833856\n"
        "buffer_load_dwordx4 v27, s[44:47], s78 offen lds\n"
        "s_waitcnt vmcnt(28) lgkmcnt(0)\n"
        "s_barrier\n"
        "ds_read_b64_tr_b16 a[208:209], v44 offset:0\n"
        "ds_read_b64_tr_b16 a[210:211], v45 offset:0\n"
        "ds_read_b64_tr_b16 a[212:213], v46 offset:0\n"
        "ds_read_b64_tr_b16 a[214:215], v47 offset:0\n"
        "ds_read_b64_tr_b16 a[216:217], v48 offset:0\n"
        "ds_read_b64_tr_b16 a[218:219], v49 offset:0\n"
        "ds_read_b64_tr_b16 a[220:221], v50 offset:0\n"
        "ds_read_b64_tr_b16 a[222:223], v51 offset:0\n"
        "s_add_u32 m0, s74, 32768\n"
        "s_add_u32 s78, s75, 4194304\n"
        "buffer_load_dwordx4 v22, s[44:47], s78 offen lds\n"
        "s_add_u32 m0, s74, 33792\n"
        "s_add_u32 s78, s75, 4227072\n"
        "buffer_load_dwordx4 v27, s[44:47], s78 offen lds\n"
        "s_add_u32 m0, s74, 34816\n"
        "s_add_u32 s78, s75, 4325376\n"
        "buffer_load_dwordx4 v22, s[44:47], s78 offen lds\n"
        "s_add_u32 m0, s74, 35840\n"
        "s_add_u32 s78, s75, 4358144\n"
        "buffer_load_dwordx4 v27, s[44:47], s78 offen lds\n"
        "s_waitcnt vmcnt(28) lgkmcnt(0)\n"
        "s_barrier\n"
        "ds_read_b64_tr_b16 a[224:225], v44 offset:4096\n"
        "ds_read_b64_tr_b16 a[226:227], v45 offset:4096\n"
        "ds_read_b64_tr_b16 a[228:229], v46 offset:4096\n"
        "ds_read_b64_tr_b16 a[230:231], v47 offset:4096\n"
        "ds_read_b64_tr_b16 a[232:233], v48 offset:4096\n"
        "ds_read_b64_tr_b16 a[234:235], v49 offset:4096\n"
        "ds_read_b64_tr_b16 a[236:237], v50 offset:4096\n"
        "ds_read_b64_tr_b16 a[238:239], v51 offset:4096\n"
        "s_mov_b64 s[44:45], %1\n"
        "s_add_u32 m0, s74, 0\n"
        "s_add_u32 s78, s75, 0\n"
        "buffer_load_dwordx4 v22, s[44:47], s78 offen lds\n"
        "s_add_u32 m0, s74, 1024\n"
        "s_add_u32 s78, s75, 32768\n"
        "buffer_load_dwordx4 v27, s[44:47], s78 offen lds\n"
        "s_add_u32 m0, s74, 2048\n"
        "s_add_u32 s78, s75, 131072\n"
        "buffer_load_dwordx4 v22, s[44:47], s78 offen lds\n"
        "s_add_u32 m0, s74, 3072\n"
        "s_add_u32 s78, s75, 163840\n"
        "buffer_load_dwordx4 v27, s[44:47], s78 offen lds\n"
        "s_waitcnt vmcnt(28) lgkmcnt(0)\n"
        "s_barrier\n"
        "ds_read_b64_tr_b16 a[240:241], v44 offset:8192\n"
        "ds_read_b64_tr_b16 a[242:243], v45 offset:8192\n"
        "ds_read_b64_tr_b16 a[244:245], v46 offset:8192\n"
        "ds_read_b64_tr_b16 a[246:247], v47 offset:8192\n"
        "ds_read_b64_tr_b16 a[248:249], v48 offset:8192\n"
        "ds_read_b64_tr_b16 a[250:251], v49 offset:8192\n"
        "ds_read_b64_tr_b16 a[252:253], v50 offset:8192\n"
        "ds_read_b64_tr_b16 a[254:255], v51 offset:8192\n"
        "s_mov_b64 s[44:45], %2\n"
        "s_add_u32 m0, s74, 4096\n"
        "s_add_u32 s78, s75, 0\n"
        "buffer_load_dwordx4 v22, s[44:47], s78 offen lds\n"
        "s_add_u32 m0, s74, 5120\n"
        "s_add_u32 s78, s75, 32768\n"
        "buffer_load_dwordx4 v27, s[44:47], s78 offen lds\n"
        "s_add_u32 m0, s74, 6144\n"
        "s_add_u32 s78, s75, 131072\n"
        "buffer_load_dwordx4 v22, s[44:47], s78 offen lds\n"
        "s_add_u32 m0, s74, 7168\n"
        "s_add_u32 s78, s75, 163840\n"
        "buffer_load_dwordx4 v27, s[44:47], s78 offen lds\n"
        "s_waitcnt vmcnt(28) lgkmcnt(0)\n"
        "s_barrier\n"
        "ds_read_b64_tr_b16 v[88:89], v44 offset:12288\n"
        "ds_read_b64_tr_b16 v[90:91], v45 offset:12288\n"
        "ds_read_b64_tr_b16 v[92:93], v46 offset:12288\n"
        "ds_read_b64_tr_b16 v[94:95], v47 offset:12288\n"
        "ds_read_b64_tr_b16 v[96:97], v48 offset:12288\n"
        "ds_read_b64_tr_b16 v[98:99], v49 offset:12288\n"
        "ds_read_b64_tr_b16 v[100:101], v50 offset:12288\n"
        "ds_read_b64_tr_b16 v[102:103], v51 offset:12288\n"
        "s_mov_b64 s[44:45], %1\n"
        "s_add_u32 m0, s74, 8192\n"
        "s_add_u32 s78, s75, 1048576\n"
        "buffer_load_dwordx4 v22, s[44:47], s78 offen lds\n"
        "s_add_u32 m0, s74, 9216\n"
        "s_add_u32 s78, s75, 1081344\n"
        "buffer_load_dwordx4 v27, s[44:47], s78 offen lds\n"
        "s_add_u32 m0, s74, 10240\n"
        "s_add_u32 s78, s75, 1179648\n"
        "buffer_load_dwordx4 v22, s[44:47], s78 offen lds\n"
        "s_add_u32 m0, s74, 11264\n"
        "s_add_u32 s78, s75, 1212416\n"
        "buffer_load_dwordx4 v27, s[44:47], s78 offen lds\n"
        "s_waitcnt vmcnt(28) lgkmcnt(0)\n"
        "s_barrier\n"
        "ds_read_b64_tr_b16 v[104:105], v44 offset:16384\n"
        "ds_read_b64_tr_b16 v[106:107], v45 offset:16384\n"
        "ds_read_b64_tr_b16 v[108:109], v46 offset:16384\n"
        "ds_read_b64_tr_b16 v[110:111], v47 offset:16384\n"
        "ds_read_b64_tr_b16 v[112:113], v48 offset:16384\n"
        "ds_read_b64_tr_b16 v[114:115], v49 offset:16384\n"
        "ds_read_b64_tr_b16 v[116:117], v50 offset:16384\n"
        "ds_read_b64_tr_b16 v[118:119], v51 offset:16384\n"
        "s_add_u32 m0, s74, 12288\n"
        "s_add_u32 s78, s75, 2097152\n"
        "buffer_load_dwordx4 v22, s[44:47], s78 offen lds\n"
        "s_add_u32 m0, s74, 13312\n"
        "s_add_u32 s78, s75, 2129920\n"
        "buffer_load_dwordx4 v27, s[44:47], s78 offen lds\n"
        "s_add_u32 m0, s74, 14336\n"
        "s_add_u32 s78, s75, 2228224\n"
        "buffer_load_dwordx4 v22, s[44:47], s78 offen lds\n"
        "s_add_u32 m0, s74, 15360\n"
        "s_add_u32 s78, s75, 2260992\n"
        "buffer_load_dwordx4 v27, s[44:47], s78 offen lds\n"
        "s_waitcnt vmcnt(28) lgkmcnt(0)\n"
        "s_barrier\n"
        "ds_read_b64_tr_b16 v[120:121], v44 offset:20480\n"
        "ds_read_b64_tr_b16 v[122:123], v45 offset:20480\n"
        "ds_read_b64_tr_b16 v[124:125], v46 offset:20480\n"
        "ds_read_b64_tr_b16 v[126:127], v47 offset:20480\n"
        "ds_read_b64_tr_b16 v[128:129], v48 offset:20480\n"
        "ds_read_b64_tr_b16 v[130:131], v49 offset:20480\n"
        "ds_read_b64_tr_b16 v[132:133], v50 offset:20480\n"
        "ds_read_b64_tr_b16 v[134:135], v51 offset:20480\n"
        "s_add_u32 m0, s74, 16384\n"
        "s_add_u32 s78, s75, 3145728\n"
        "buffer_load_dwordx4 v22, s[44:47], s78 offen lds\n"
        "s_add_u32 m0, s74, 17408\n"
        "s_add_u32 s78, s75, 3178496\n"
        "buffer_load_dwordx4 v27, s[44:47], s78 offen lds\n"
        "s_add_u32 m0, s74, 18432\n"
        "s_add_u32 s78, s75, 3276800\n"
        "buffer_load_dwordx4 v22, s[44:47], s78 offen lds\n"
        "s_add_u32 m0, s74, 19456\n"
        "s_add_u32 s78, s75, 3309568\n"
        "buffer_load_dwordx4 v27, s[44:47], s78 offen lds\n"
        "s_waitcnt vmcnt(28) lgkmcnt(0)\n"
        "s_barrier\n"
        "ds_read_b64_tr_b16 v[136:137], v44 offset:24576\n"
        "ds_read_b64_tr_b16 v[138:139], v45 offset:24576\n"
        "ds_read_b64_tr_b16 v[140:141], v46 offset:24576\n"
        "ds_read_b64_tr_b16 v[142:143], v47 offset:24576\n"
        "ds_read_b64_tr_b16 v[144:145], v48 offset:24576\n"
        "ds_read_b64_tr_b16 v[146:147], v49 offset:24576\n"
        "ds_read_b64_tr_b16 v[148:149], v50 offset:24576\n"
        "ds_read_b64_tr_b16 v[150:151], v51 offset:24576\n"
        "s_mov_b64 s[44:45], %4\n"
        "s_mul_i32 s79, s77, 128\n"
        "s_add_u32 s79, s79, s76\n"
        "s_add_u32 m0, s74, 20480\n"
        "s_add_u32 s78, s79, 18560\n"
        "buffer_load_dwordx4 v23, s[44:47], s78 offen lds\n"
        "s_add_u32 m0, s74, 21504\n"
        "s_add_u32 s78, s79, 55424\n"
        "buffer_load_dwordx4 v23, s[44:47], s78 offen lds\n"
        "s_add_u32 m0, s74, 22528\n"
        "s_add_u32 s78, s79, 19712\n"
        "buffer_load_dwordx4 v23, s[44:47], s78 offen lds\n"
        "s_add_u32 m0, s74, 23552\n"
        "s_add_u32 s78, s79, 56576\n"
        "buffer_load_dwordx4 v23, s[44:47], s78 offen lds\n"
        "s_waitcnt vmcnt(28) lgkmcnt(0)\n"
        "s_barrier\n"
        "ds_read_b64_tr_b16 v[152:153], v44 offset:28672\n"
        "ds_read_b64_tr_b16 v[154:155], v45 offset:28672\n"
        "ds_read_b64_tr_b16 v[156:157], v46 offset:28672\n"
        "ds_read_b64_tr_b16 v[158:159], v47 offset:28672\n"
        "ds_read_b64_tr_b16 v[160:161], v48 offset:28672\n"
        "ds_read_b64_tr_b16 v[162:163], v49 offset:28672\n"
        "ds_read_b64_tr_b16 v[164:165], v50 offset:28672\n"
        "ds_read_b64_tr_b16 v[166:167], v51 offset:28672\n"
        "s_mul_i32 s79, s77, 128\n"
        "s_add_u32 s79, s79, s76\n"
        "s_add_u32 m0, s74, 24576\n"
        "s_add_u32 s78, s79, 18816\n"
        "buffer_load_dwordx4 v23, s[44:47], s78 offen lds\n"
        "s_add_u32 m0, s74, 25600\n"
        "s_add_u32 s78, s79, 55680\n"
        "buffer_load_dwordx4 v23, s[44:47], s78 offen lds\n"
        "s_add_u32 m0, s74, 26624\n"
        "s_add_u32 s78, s79, 19968\n"
        "buffer_load_dwordx4 v23, s[44:47], s78 offen lds\n"
        "s_add_u32 m0, s74, 27648\n"
        "s_add_u32 s78, s79, 56832\n"
        "buffer_load_dwordx4 v23, s[44:47], s78 offen lds\n"
        "s_waitcnt vmcnt(28) lgkmcnt(0)\n"
        "s_barrier\n"
        "ds_read_b64_tr_b16 v[168:169], v44 offset:32768\n"
        "ds_read_b64_tr_b16 v[170:171], v45 offset:32768\n"
        "ds_read_b64_tr_b16 v[172:173], v46 offset:32768\n"
        "ds_read_b64_tr_b16 v[174:175], v47 offset:32768\n"
        "ds_read_b64_tr_b16 v[176:177], v48 offset:32768\n"
        "ds_read_b64_tr_b16 v[178:179], v49 offset:32768\n"
        "ds_read_b64_tr_b16 v[180:181], v50 offset:32768\n"
        "ds_read_b64_tr_b16 v[182:183], v51 offset:32768\n"
        "s_getpc_b64 vcc\n"
        "41:\n"
        "s_lshl_b32 s78, %6, 12\n"
        "v_lshl_add_u32 v57, v85, 2, s78\n"
        "v_min_u32 v57, .Lcode_to-41b, v57\n"
        "global_load_dword v57, v57, vcc\n"
        "s_mul_i32 s79, s77, 128\n"
        "s_add_u32 s79, s79, s76\n"
        "s_add_u32 m0, s74, 28672\n"
        "s_add_u32 s78, s79, 19072\n"
        "buffer_load_dwordx4 v23, s[44:47], s78 offen lds\n"
        "s_add_u32 m0, s74, 29696\n"
        "s_add_u32 s78, s79, 55936\n"
        "buffer_load_dwordx4 v23, s[44:47], s78 offen lds\n"
        "s_add_u32 m0, s74, 30720\n"
        "s_add_u32 s78, s79, 20224\n"
        "buffer_load_dwordx4 v23, s[44:47], s78 offen lds\n"
        "s_add_u32 m0, s74, 31744\n"
        "s_add_u32 s78, s79, 57088\n"
        "buffer_load_dwordx4 v23, s[44:47], s78 offen lds\n"
        "s_waitcnt vmcnt(28) lgkmcnt(0)\n"
        "s_barrier\n"
        "ds_read_b64_tr_b16 v[28:29], v44 offset:0\n"
        "ds_read_b64_tr_b16 v[30:31], v45 offset:0\n"
        "ds_read_b64_tr_b16 v[32:33], v46 offset:0\n"
        "ds_read_b64_tr_b16 v[34:35], v47 offset:0\n"
        "ds_read_b64_tr_b16 v[36:37], v48 offset:0\n"
        "ds_read_b64_tr_b16 v[38:39], v49 offset:0\n"
        "ds_read_b64_tr_b16 v[40:41], v50 offset:0\n"
        "ds_read_b64_tr_b16 v[42:43], v51 offset:0\n"
        "s_mul_i32 s79, s77, 128\n"
        "s_add_u32 s79, s79, s76\n"
        "s_add_u32 m0, s74, 32768\n"
        "s_add_u32 s78, s79, 19328\n"
        "buffer_load_dwordx4 v23, s[44:47], s78 offen lds\n"
        "s_add_u32 m0, s74, 33792\n"
        "s_add_u32 s78, s79, 56192\n"
        "buffer_load_dwordx4 v23, s[44:47], s78 offen lds\n"
        "s_add_u32 m0, s74, 34816\n"
        "s_add_u32 s78, s79, 20480\n"
        "buffer_load_dwordx4 v23, s[44:47], s78 offen lds\n"
        "s_add_u32 m0, s74, 35840\n"
        "s_add_u32 s78, s79, 57344\n"
        "buffer_load_dwordx4 v23, s[44:47], s78 offen lds\n"
        "s_waitcnt vmcnt(28) lgkmcnt(0)\n"
        "s_barrier\n"
        "ds_read_b64_tr_b16 v[0:1], v44 offset:4096\n"
        "ds_read_b64_tr_b16 v[2:3], v45 offset:4096\n"
        "ds_read_b64_tr_b16 v[4:5], v46 offset:4096\n"
        "ds_read_b64_tr_b16 v[6:7], v47 offset:4096\n"
        "ds_read_b64_tr_b16 v[8:9], v48 offset:4096\n"
        "ds_read_b64_tr_b16 v[10:11], v49 offset:4096\n"
        "ds_read_b64_tr_b16 v[12:13], v50 offset:4096\n"
        "ds_read_b64_tr_b16 v[14:15], v51 offset:4096\n"
        "ds_write_b128 v86, v[28:31] offset:0\n"
        "ds_write_b128 v86, v[32:35] offset:1024\n"
        "ds_write_b128 v86, v[36:39] offset:2048\n"
        "ds_write_b128 v86, v[40:43] offset:3072\n"
        "s_waitcnt vmcnt(24) lgkmcnt(0)\n"
        "s_barrier\n"
        "ds_read_b64_tr_b16 v[28:29], v44 offset:8192\n"
        "ds_read_b64_tr_b16 v[30:31], v45 offset:8192\n"
        "ds_read_b64_tr_b16 v[32:33], v46 offset:8192\n"
        "ds_read_b64_tr_b16 v[34:35], v47 offset:8192\n"
        "ds_read_b64_tr_b16 v[36:37], v48 offset:8192\n"
        "ds_read_b64_tr_b16 v[38:39], v49 offset:8192\n"
        "ds_read_b64_tr_b16 v[40:41], v50 offset:8192\n"
        "ds_read_b64_tr_b16 v[42:43], v51 offset:8192\n"
        "ds_write_b128 v86, v[0:3] offset:4096\n"
        "ds_write_b128 v86, v[4:7] offset:5120\n"
        "ds_write_b128 v86, v[8:11] offset:6144\n"
        "ds_write_b128 v86, v[12:15] offset:7168\n"
        "s_waitcnt vmcnt(20) lgkmcnt(0)\n"
        "s_barrier\n"
        "ds_read_b64_tr_b16 v[0:1], v44 offset:12288\n"
        "ds_read_b64_tr_b16 v[2:3], v45 offset:12288\n"
        "ds_read_b64_tr_b16 v[4:5], v46 offset:12288\n"
        "ds_read_b64_tr_b16 v[6:7], v47 offset:12288\n"
        "ds_read_b64_tr_b16 v[8:9], v48 offset:12288\n"
        "ds_read_b64_tr_b16 v[10:11], v49 offset:12288\n"
        "ds_read_b64_tr_b16 v[12:13], v50 offset:12288\n"
        "ds_read_b64_tr_b16 v[14:15], v51 offset:12288\n"
        "ds_write_b128 v86, v[28:31] offset:8192\n"
        "ds_write_b128 v86, v[32:35] offset:9216\n"
        "ds_write_b128 v86, v[36:39] offset:10240\n"
        "ds_write_b128 v86, v[40:43] offset:11264\n"
        "s_waitcnt vmcnt(16) lgkmcnt(0)\n"
        "s_barrier\n"
        "ds_read_b64_tr_b16 v[28:29], v44 offset:16384\n"
        "ds_read_b64_tr_b16 v[30:31], v45 offset:16384\n"
        "ds_read_b64_tr_b16 v[32:33], v46 offset:16384\n"
        "ds_read_b64_tr_b16 v[34:35], v47 offset:16384\n"
        "ds_read_b64_tr_b16 v[36:37], v48 offset:16384\n"
        "ds_read_b64_tr_b16 v[38:39], v49 offset:16384\n"
        "ds_read_b64_tr_b16 v[40:41], v50 offset:16384\n"
        "ds_read_b64_tr_b16 v[42:43], v51 offset:16384\n"
        "ds_write_b128 v86, v[0:3] offset:12288\n"
        "ds_write_b128 v86, v[4:7] offset:13312\n"
        "ds_write_b128 v86, v[8:11] offset:14336\n"
        "ds_write_b128 v86, v[12:15] offset:15360\n"
        "s_waitcnt vmcnt(12) lgkmcnt(0)\n"
        "s_barrier\n"
        "ds_read_b64_tr_b16 v[0:1], v52 offset:20480\n"
        "ds_read_b64_tr_b16 v[2:3], v52 offset:22528\n"
        "ds_read_b64_tr_b16 v[4:5], v53 offset:20480\n"
        "ds_read_b64_tr_b16 v[6:7], v53 offset:22528\n"
        "ds_read_b64_tr_b16 v[8:9], v54 offset:20480\n"
        "ds_read_b64_tr_b16 v[10:11], v54 offset:22528\n"
        "ds_read_b64_tr_b16 v[12:13], v55 offset:20480\n"
        "ds_read_b64_tr_b16 v[14:15], v55 offset:22528\n"
        "s_lshl_b32 s78, %7, 7\n"
        "s_add_u32 s78, s78, 0x440000\n"
        "buffer_load_dwordx2 v[18:19], off, s[40:43], s78 sc1\n"
        "ds_write_b128 v86, v[28:31] offset:16384\n"
        "ds_write_b128 v86, v[32:35] offset:17408\n"
        "ds_write_b128 v86, v[36:39] offset:18432\n"
        "ds_write_b128 v86, v[40:43] offset:19456\n"
        "s_waitcnt vmcnt(9) lgkmcnt(0)\n"
        "s_barrier\n"
        "ds_read_b64_tr_b16 v[28:29], v52 offset:24576\n"
        "ds_read_b64_tr_b16 v[30:31], v52 offset:26624\n"
        "ds_read_b64_tr_b16 v[32:33], v53 offset:24576\n"
        "ds_read_b64_tr_b16 v[34:35], v53 offset:26624\n"
        "ds_read_b64_tr_b16 v[36:37], v54 offset:24576\n"
        "ds_read_b64_tr_b16 v[38:39], v54 offset:26624\n"
        "ds_read_b64_tr_b16 v[40:41], v55 offset:24576\n"
        "ds_read_b64_tr_b16 v[42:43], v55 offset:26624\n"
        "ds_write_b128 v86, v[0:3] offset:20480\n"
        "ds_write_b128 v86, v[4:7] offset:21504\n"
        "ds_write_b128 v86, v[8:11] offset:22528\n"
        "ds_write_b128 v86, v[12:15] offset:23552\n"
        "s_waitcnt vmcnt(5) lgkmcnt(0)\n"
        "s_barrier\n"
        "ds_read_b64_tr_b16 v[0:1], v52 offset:28672\n"
        "ds_read_b64_tr_b16 v[2:3], v52 offset:30720\n"
        "ds_read_b64_tr_b16 v[4:5], v53 offset:28672\n"
        "ds_read_b64_tr_b16 v[6:7], v53 offset:30720\n"
        "ds_read_b64_tr_b16 v[8:9], v54 offset:28672\n"
        "ds_read_b64_tr_b16 v[10:11], v54 offset:30720\n"
        "ds_read_b64_tr_b16 v[12:13], v55 offset:28672\n"
        "ds_read_b64_tr_b16 v[14:15], v55 offset:30720\n"
        "ds_write_b128 v86, v[28:31] offset:24576\n"
        "ds_write_b128 v86, v[32:35] offset:25600\n"
        "ds_write_b128 v86, v[36:39] offset:26624\n"
        "ds_write_b128 v86, v[40:43] offset:27648\n"
        "s_waitcnt vmcnt(0) lgkmcnt(0)\n"
        "s_barrier\n"
        "ds_read_b64_tr_b16 v[28:29], v52 offset:32768\n"
        "ds_read_b64_tr_b16 v[30:31], v52 offset:34816\n"
        "ds_read_b64_tr_b16 v[32:33], v53 offset:32768\n"
        "ds_read_b64_tr_b16 v[34:35], v53 offset:34816\n"
        "ds_read_b64_tr_b16 v[36:37], v54 offset:32768\n"
        "ds_read_b64_tr_b16 v[38:39], v54 offset:34816\n"
        "ds_read_b64_tr_b16 v[40:41], v55 offset:32768\n"
        "ds_read_b64_tr_b16 v[42:43], v55 offset:34816\n"
        "ds_write_b128 v86, v[0:3] offset:28672\n"
        "ds_write_b128 v86, v[4:7] offset:29696\n"
        "ds_write_b128 v86, v[8:11] offset:30720\n"
        "ds_write_b128 v86, v[12:15] offset:31744\n"
        "s_mov_b32 s79, 0x10000\n"
        "s_lshl_b32 s78, %7, 7\n"
        "s_add_u32 s78, s78, 0x440000\n"
        "40:\n"
        "v_cmp_ne_u32 vcc, v16, v18\n"
        "s_cbranch_vccnz 41f\n"
        "s_sub_u32 s79, s79, 1\n"
        "s_cbranch_scc1 41f\n"
        "s_and_b32 vcc_lo, s79, 15\n"
        "s_cbranch_scc1 42f\n"
        "s_memrealtime vcc\n"
        "s_waitcnt lgkmcnt(0)\n"
        "v_mov_b32 v56, vcc_lo\n"
        "buffer_store_dword v56, off, s[40:43], s27 sc1\n"
        "42:\n"
        "buffer_load_dwordx2 v[18:19], off, s[40:43], s78 sc1\n"
        "s_waitcnt vmcnt(0)\n"
        "s_branch 40b\n"
        "41:\n"
        "v_bfe_u32 v0, v85, 2, 4\n"
        "buffer_load_dword v1, v0, s[40:43], s82 offen sc1\n"
        "s_lshl2_add_u32 s78, %7, %6\n"
        "s_lshl2_add_u32 s78, s78, 0x425800\n"
        "s_memrealtime vcc\n"
        "s_waitcnt lgkmcnt(0)\n"
        "v_or_b32 v16, vcc_lo, 1\n"
        "buffer_store_dword v16, off, s[40:43], s78 sc1\n"
        "v_readfirstlane_b32 s78, v18\n"
        "s_bfe_u32 s49, s78, 0x1001e\n"
        "s_lshl_b32 s49, s49, 13\n"
        "s_add_u32 s50, s49, 4096\n"
        "s_add_u32 s51, s49, 16384\n"
        "s_add_u32 s81, s81, s49\n"
        "s_lshr_b32 s78, s78, 31\n"
        "s_lshl_b32 s78, s78, 13\n"
        "s_add_u32 s78, s78, 0x200000\n"
        "s_add_u32 s56, s56, s78\n"
        "s_add_u32 s57, s57, s78\n"
        "s_add_u32 s58, s58, s78\n"
        "s_add_u32 s59, s59, s78\n"
        "s_add_u32 s60, s60, s78\n"
        "s_add_u32 s61, s61, s78\n"
        "s_add_u32 s62, s62, s78\n"
        "s_add_u32 s63, s63, s78\n"
        "s_add_u32 s64, s64, s78\n"
        "s_waitcnt lgkmcnt(0)\n"
        "s_barrier\n"
        "ds_write_b128 v86, v[28:31] offset:32768\n"
        "ds_write_b128 v86, v[32:35] offset:33792\n"
        "ds_write_b128 v86, v[36:39] offset:34816\n"
        "ds_write_b128 v86, v[40:43] offset:35840\n"
        "s_waitcnt lgkmcnt(0)\n"
        "s_lshl_b32 s34, s84, 1\n"
        "s_mov_b32 s26, s86\n"
        "s_mov_b32 m0, 3072\n"
        "s_barrier\n"
        "s_memrealtime s[76:77]\n"
        "s_getpc_b64 %1\n"
        "20:\n"
        "v_readfirstlane_b32 s44, v19\n"
        "s_waitcnt lgkmcnt(0)\n"
        "s_sub_u32 s45, s44, s76\n"
        "s_and_b32 s45, s45, 3\n"
        "s_sub_u32 s44, s44, s45\n"
        "s_lshl_b32 s45, s45, 1\n"
        "s_waitcnt vmcnt(1)\n"
        "s_branch 3f\n"
        ".p2align 6\n"
        "s_nop 2\n"
        "s_nop 2\n"
        "s_nop 2\n"
        "s_nop 2\n"
        "s_nop 2\n"
        "s_nop 2\n"
        "s_nop 2\n"
        "s_nop 2\n"
        "s_nop 2\n"
        "s_nop 2\n"
        "s_nop 2\n"
        "s_nop 2\n"
        "s_nop 2\n"
        "s_nop 2\n"
        "5:\n"
        "buffer_load_dwordx4 v[0:3], v85, s[40:43], s49 offen offset:0 sc1\n"
        "ds_read_b128 v[60:63], v86 offset:0\n"
        "v_mov_b64 v[36:37], 0\n"
        "s_memtime s[74:75]\n"
        "v_cvt_f32_u32 v48, s68\n"
        "s_mul_i32 s79, s35, %10\n"
        "v_rcp_f32 v48, v48\n"
        "v_cvt_f32_u32 v49, s79\n"
        "s_nop 3\n"
        "buffer_load_dwordx4 v[4:7], v85, s[40:43], s49 offen offset:1024 sc1\n"
        "ds_read_b128 v[64:67], v86 offset:1024\n"
        "v_mov_b64 v[38:39], 0\n"
        "v_mul_f32 v48, v48, v49\n"
        "v_cvt_u32_f32 v48, v48\n"
        "v_min_u32 v48, 400, v48\n"
        "s_or_b32 s30, s27, 0x80000001\n"
        "v_readfirstlane_b32 s78, v48\n"
        "s_bitset0_b32 s30, 0\n"
        "s_nop 3\n"
        "buffer_load_dwordx4 v[8:11], v85, s[40:43], s49 offen offset:2048 sc1\n"
        "ds_read_b128 v[52:55], v86 offset:4096\n"
        "v_mov_b64 v[40:41], 0\n"
        "s_add_u32 s78, s78, %12\n"
        "s_cmp_eq_u32 s68, 0\n"
        "s_cselect_b32 s31, s31, s78\n"
        "s_cmp_lt_u32 s27, s44\n"
        "s_cselect_b32 s30, s31, s30\n"
        "s_nop 4\n"
        "buffer_load_dwordx4 v[12:15], v85, s[40:43], s49 offen offset:3072 sc1\n"
        "ds_read_b128 v[56:59], v86 offset:5120\n"
        "v_mov_b64 v[42:43], 0\n"
        "s_nop 9\n"
        "buffer_load_dwordx4 v[16:19], v85, s[40:43], s50 offen offset:0 sc1\n"
        "ds_read_b128 v[68:71], v86 offset:2048\n"
        "v_mov_b64 v[44:45], 0\n"
        "s_nop 9\n"
        "buffer_load_dwordx4 v[20:23], v85, s[40:43], s50 offen offset:1024 sc1\n"
        "ds_read_b128 v[72:75], v86 offset:3072\n"
        "v_mov_b64 v[46:47], 0\n"
        "s_nop 1\n"
        "v_mov_b64 v[36:37], 0\n"
        "s_nop 3\n"
        "v_mov_b64 v[36:37], 0\n"
        "s_nop 1\n"
        "buffer_load_dwordx4 v[24:27], v85, s[40:43], s50 offen offset:2048 sc1\n"
        "ds_read_b128 v[76:79], v86 offset:6144\n"
        "v_mov_b64 v[36:37], 0\n"
        "s_nop 1\n"
        "v_mov_b64 v[36:37], 0\n"
        "s_nop 3\n"
        "v_mov_b64 v[36:37], 0\n"
        "s_nop 1\n"
        "buffer_load_dwordx4 v[28:31], v85, s[40:43], s50 offen offset:3072 sc1\n"
        "ds_read_b128 v[80:83], v86 offset:7168\n"
        "v_mov_b64 v[36:37], 0\n"
        "s_nop 1\n"
        "v_mov_b64 v[36:37], 0\n"
        "s_nop 3\n"
        "v_mov_b64 v[36:37], 0\n"
        "s_nop 1\n"
        "buffer_load_dwordx4 v[32:35], v85, s[40:43], s51 offen offset:0 sc1\n"
        "s_waitcnt lgkmcnt(0)\n"
        "v_mfma_f32_16x16x32_bf16 v[48:51], v[36:39], v[36:39], 0\n"
        "6:\n"
        "s_waitcnt vmcnt(8)\n"
        "v_smfmac_f32_16x16x64_bf16 v[36:39], v[0:3], v[60:67], v84\n"
        "ds_read_b128 v[60:63], v86 offset:8192\n"
        "v_smfmac_f32_16x16x64_bf16 v[40:43], v[0:3], v[68:75], v84\n"
        "ds_read_b128 v[64:67], v86 offset:9216\n"
        "v_smfmac_f32_16x16x64_bf16 v[44:47], v[0:3], v[52:59], v84\n"
        "ds_read_b128 v[68:71], v86 offset:10240\n"
        "v_smfmac_f32_16x16x64_bf16 v[48:51], v[0:3], v[76:83], v84\n"
        "s_waitcnt vmcnt(7)\n"
        "ds_read_b128 v[72:75], v86 offset:11264\n"
        "v_smfmac_f32_16x16x64_bf16 v[36:39], v[4:7], a[0:7], v84\n"
        "ds_read_b32 v59, v85 offset:7168\n"
        "v_mov_b64 v[76:77], 0\n"
        "v_smfmac_f32_16x16x64_bf16 v[40:43], v[4:7], a[8:15], v84\n"
        "v_mov_b64 v[78:79], 0\n"
        "v_smfmac_f32_16x16x64_bf16 v[44:47], v[4:7], a[80:87], v84\n"
        "v_mov_b64 v[80:81], 0\n"
        "v_smfmac_f32_16x16x64_bf16 v[48:51], v[4:7], a[88:95], v84\n"
        "s_waitcnt vmcnt(6) lgkmcnt(3)\n"
        "v_mov_b64 v[82:83], 0\n"
        "v_smfmac_f32_16x16x64_bf16 v[36:39], v[8:11], v[60:67], v84\n"
        "s_waitcnt lgkmcnt(1)\n"
        "ds_read_b128 v[60:63], v86 offset:12288\n"
        "v_smfmac_f32_16x16x64_bf16 v[40:43], v[8:11], v[68:75], v84\n"
        "ds_read_b128 v[64:67], v86 offset:13312\n"
        "v_smfmac_f32_16x16x64_bf16 v[44:47], v[8:11], a[96:103], v84\n"
        "ds_read_b128 v[68:71], v86 offset:14336\n"
        "v_smfmac_f32_16x16x64_bf16 v[48:51], v[8:11], a[104:111], v84\n"
        "s_waitcnt vmcnt(5)\n"
        "ds_read_b128 v[72:75], v86 offset:15360\n"
        "v_smfmac_f32_16x16x64_bf16 v[36:39], v[12:15], a[16:23], v84\n"
        "s_waitcnt lgkmcnt(4)\n"
        "buffer_load_dwordx4 v[52:55], v59, s[40:43], s52 offen sc1\n"
        "v_smfmac_f32_16x16x64_bf16 v[40:43], v[12:15], a[24:31], v84\n"
        "buffer_load_dwordx4 v[56:59], v59, s[40:43], s52 offen offset:1024 sc1\n"
        "v_smfmac_f32_16x16x64_bf16 v[44:47], v[12:15], a[112:119], v84\n"
        "v_smfmac_f32_16x16x64_bf16 v[48:51], v[12:15], a[120:127], v84\n"
        "s_waitcnt vmcnt(6) lgkmcnt(2)\n"
        "v_smfmac_f32_16x16x64_bf16 v[36:39], v[16:19], v[60:67], v84\n"
        "s_waitcnt lgkmcnt(0)\n"
        "ds_read_b128 v[60:63], v86 offset:16384\n"
        "v_smfmac_f32_16x16x64_bf16 v[40:43], v[16:19], v[68:75], v84\n"
        "ds_read_b128 v[64:67], v86 offset:17408\n"
        "v_smfmac_f32_16x16x64_bf16 v[44:47], v[16:19], a[128:135], v84\n"
        "ds_read_b128 v[68:71], v86 offset:18432\n"
        "v_smfmac_f32_16x16x64_bf16 v[48:51], v[16:19], a[136:143], v84\n"
        "s_waitcnt vmcnt(5)\n"
        "ds_read_b128 v[72:75], v86 offset:19456\n"
        "v_smfmac_f32_16x16x64_bf16 v[36:39], v[20:23], a[32:39], v84\n"
        "v_smfmac_f32_16x16x64_bf16 v[40:43], v[20:23], a[40:47], v84\n"
        "v_smfmac_f32_16x16x64_bf16 v[44:47], v[20:23], a[144:151], v84\n"
        "v_smfmac_f32_16x16x64_bf16 v[48:51], v[20:23], a[152:159], v84\n"
        "s_waitcnt vmcnt(4) lgkmcnt(2)\n"
        "v_smfmac_f32_16x16x64_bf16 v[36:39], v[24:27], v[60:67], v84\n"
        "s_waitcnt lgkmcnt(0)\n"
        "ds_read_b128 v[64:67], v85 offset:5120\n"
        "v_smfmac_f32_16x16x64_bf16 v[40:43], v[24:27], v[68:75], v84\n"
        "ds_read_b64 v[74:75], v85 offset:6144\n"
        "v_smfmac_f32_16x16x64_bf16 v[44:47], v[24:27], a[160:167], v84\n"
        "v_smfmac_f32_16x16x64_bf16 v[48:51], v[24:27], a[168:175], v84\n"
        "s_waitcnt vmcnt(3)\n"
        "v_smfmac_f32_16x16x64_bf16 v[36:39], v[28:31], a[48:55], v84\n"
        "v_smfmac_f32_16x16x64_bf16 v[40:43], v[28:31], a[56:63], v84\n"
        "s_waitcnt lgkmcnt(1)\n"
        "buffer_load_dwordx4 v[60:63], v64, s[40:43], s53 offen offset:0 sc1\n"
        "v_mad_u32_u24 v65, v66, s38, v65\n"
        "v_smfmac_f32_16x16x64_bf16 v[44:47], v[28:31], a[176:183], v84\n"
        "s_waitcnt lgkmcnt(0)\n"
        "v_add_u32 v74, s65, v74\n"
        "v_smfmac_f32_16x16x64_bf16 v[48:51], v[28:31], a[184:191], v84\n"
        "s_waitcnt vmcnt(3)\n"
        "ds_read_b32 v66, v85 offset:7172\n"
        "v_smfmac_f32_16x16x64_bf16 v[36:39], v[32:35], a[64:71], v84\n"
        "v_smfmac_f32_16x16x64_bf16 v[40:43], v[32:35], a[72:79], v84\n"
        "v_smfmac_f32_16x16x64_bf16 v[44:47], v[32:35], a[192:199], v84\n"
        "v_add_f32 v36, v36, v37\n"
        "v_smfmac_f32_16x16x64_bf16 v[48:51], v[32:35], a[200:207], v84\n"
        "v_add_f32 v38, v38, v39\n"
        "v_add_f32 v40, v40, v41\n"
        "v_add_f32 v42, v42, v43\n"
        "v_cvt_pk_bf16_f32 v37, v36, v40\n"
        "v_cvt_pk_bf16_f32 v39, v38, v42\n"
        "v_smfmac_f32_16x16x64_bf16 v[80:83], v[0:3], a[208:215], v84\n"
        "ds_write2_b32 v74, v37, v39 offset1:4\n"
        "v_cmp_u_f32 vcc, v36, v38\n"
        "v_add_f32 v44, v44, v45\n"
        "v_add_f32 v46, v46, v47\n"
        "v_add_f32 v48, v48, v49\n"
        "v_add_f32 v50, v50, v51\n"
        "v_cvt_pk_bf16_f32 v44, v44, v48\n"
        "v_cvt_pk_bf16_f32 v46, v46, v50\n"
        "v_smfmac_f32_16x16x64_bf16 v[76:79], v[0:3], a[216:223], v84\n"
        "ds_write2_b32 v87, v44, v46 offset1:8\n"
        "v_smfmac_f32_16x16x64_bf16 v[80:83], v[4:7], a[224:231], v84\n"
        "s_cbranch_vccnz 8f\n"
        "7:\n"
        "v_smfmac_f32_16x16x64_bf16 v[80:83], v[8:11], a[240:247], v84\n"
        "buffer_load_dwordx4 v[0:3], v64, s[40:43], s53 offen offset:128 sc1\n"
        "v_smfmac_f32_16x16x64_bf16 v[76:79], v[8:11], a[248:255], v84\n"
        "v_mov_b64 v[8:9], 0\n"
        "v_smfmac_f32_16x16x64_bf16 v[80:83], v[12:15], v[88:95], v84\n"
        "s_waitcnt lgkmcnt(0)\n"
        "s_barrier\n"
        "ds_read_b128 v[36:39], v75 offset:2048\n"
        "ds_read_b128 v[40:43], v75 offset:2560\n"
        "v_smfmac_f32_16x16x64_bf16 v[76:79], v[12:15], v[96:103], v84\n"
        "ds_read_b128 v[68:71], v75 offset:3072\n"
        "ds_read_b128 v[72:75], v75 offset:3584\n"
        "v_smfmac_f32_16x16x64_bf16 v[80:83], v[16:19], v[104:111], v84\n"
        "ds_read_b128 v[44:47], v85\n"
        "v_mov_b32 v14, s30\n"
        "v_smfmac_f32_16x16x64_bf16 v[80:83], v[20:23], v[120:127], v84\n"
        "ds_read_b128 v[48:51], v85 offset:1024\n"
        "v_mov_b64 v[10:11], 0\n"
        "v_smfmac_f32_16x16x64_bf16 v[80:83], v[24:27], v[136:143], v84\n"
        "s_mov_b64 exec, 0x101\n"
        "buffer_store_dword v14, v85, s[40:43], s81 offen sc1\n"
        "s_mov_b64 exec, -1\n"
        "v_mov_b64 v[12:13], 0\n"
        "s_waitcnt vmcnt(4) lgkmcnt(4)\n"
        "v_smfmac_f32_16x16x64_bf16 v[8:11], v[52:55], v[36:43], v84\n"
        "v_mov_b64 v[14:15], 0\n"
        "v_smfmac_f32_16x16x64_bf16 v[80:83], v[28:31], v[152:159], v84\n"
        "s_waitcnt vmcnt(3) lgkmcnt(2)\n"
        "v_mov_b64 v[52:53], 0\n"
        "v_smfmac_f32_16x16x64_bf16 v[8:11], v[56:59], v[68:75], v84\n"
        "s_waitcnt lgkmcnt(1)\n"
        "v_mov_b64 v[54:55], 0\n"
        "v_smfmac_f32_16x16x64_bf16 v[12:15], v[44:47], v[36:43], v84\n"
        "s_waitcnt lgkmcnt(0)\n"
        "v_mov_b64 v[56:57], 0\n"
        "v_smfmac_f32_16x16x64_bf16 v[12:15], v[48:51], v[68:75], v84\n"
        "v_add_f32 v8, v8, v9\n"
        "v_smfmac_f32_16x16x64_bf16 v[80:83], v[32:35], v[168:175], v84\n"
        "s_nop 0\n"
        "ds_read_b128 v[68:71], v86 offset:36864\n"
        "v_add_f32 v10, v10, v11\n"
        "v_mov_b64 v[58:59], 0\n"
        "v_add_f32_dpp v8, v12, v13 quad_perm:[0,1,2,3] row_mask:0xf bank_mask:0xc\n"
        "v_add_f32_dpp v10, v14, v15 quad_perm:[0,1,2,3] row_mask:0xf bank_mask:0xc\n"
        "v_mul_f32 v9, 0x3e0293ee, v8\n"
        "v_mul_f32 v11, 0x3e0293ee, v10\n"
        "v_exp_f32 v9, v9\n"
        "v_exp_f32 v11, v11\n"
        "v_lshlrev_b32 v40, 3, v85\n"
        "v_cvt_pk_bf16_f32 v9, v9, v11\n"
        "v_smfmac_f32_16x16x64_bf16 v[76:79], v[16:19], v[112:119], v84\n"
        "ds_write_b32 v65, v9\n"
        "v_add_f32 v80, v80, v81\n"
        "v_smfmac_f32_16x16x64_bf16 v[76:79], v[20:23], v[128:135], v84\n"
        "ds_read_b128 v[72:75], v86 offset:37888\n"
        "v_add_f32 v82, v82, v83\n"
        "v_smfmac_f32_16x16x64_bf16 v[76:79], v[4:7], a[232:239], v84\n"
        "ds_read_b128 v[16:19], v86 offset:20480\n"
        "v_cvt_pk_bf16_f32 v64, v80, v82\n"
        "v_smfmac_f32_16x16x64_bf16 v[76:79], v[24:27], v[144:151], v84\n"
        "s_cmp_eq_u32 s48, 63\n"
        "ds_read_b128 v[20:23], v86 offset:21504\n"
        "v_mov_b64 v[80:81], s[70:71]\n"
        "v_smfmac_f32_16x16x64_bf16 v[76:79], v[28:31], v[160:167], v84\n"
        "s_waitcnt lgkmcnt(3)\n"
        "s_barrier\n"
        "ds_read_b128 v[36:39], v85 offset:4096\n"
        "v_smfmac_f32_16x16x64_bf16 v[76:79], v[32:35], v[176:183], v84\n"
        "s_mov_b32 exec_hi, 0\n"
        "buffer_load_dword v40, s[40:43], s39 offen sc1 lds\n"
        "s_mov_b32 exec_hi, -1\n"
        "v_mov_b64 v[82:83], s[70:71]\n"
        "s_waitcnt vmcnt(2)\n"
        "s_nop 2\n"
        "v_add_f32 v76, v76, v77\n"
        "v_add_f32 v78, v78, v79\n"
        "v_cvt_pk_bf16_f32 v4, v76, v78\n"
        "s_cbranch_scc1 9f\n"
        "s_waitcnt lgkmcnt(0)\n"
        "v_smfmac_f32_16x16x64_bf16 v[52:55], v[36:39], v[60:67], v84\n"
        "ds_read_b128 v[24:27], v86 offset:22528\n"
        "v_mov_b64 v[32:33], 0\n"
        "v_smfmac_f32_16x16x64_bf16 v[56:59], v[36:39], v[0:7], v84\n"
        "ds_read_b128 v[28:31], v86 offset:23552\n"
        "v_mov_b64 v[34:35], 0\n"
        "v_mfma_f32_16x16x32_bf16 v[76:79], v[36:39], v[80:83], 0\n"
        "s_nop 0\n"
        "v_add_f32 v52, v52, v53\n"
        "v_add_f32 v54, v54, v55\n"
        "s_nop 0\n"
        "v_add_f32 v56, v56, v57\n"
        "v_add_f32 v58, v58, v59\n"
        "v_cvt_pk_bf16_f32 v52, v52, v56\n"
        "v_cvt_pk_bf16_f32 v54, v54, v58\n"
        "ds_write2_b32 v87, v52, v54 offset1:8\n"
        "v_add_f32 v80, v76, v77\n"
        "v_add_f32 v81, v78, v79\n"
        "v_rcp_f32 v76, v80\n"
        "v_rcp_f32 v77, v81\n"
        "v_max3_f32 v80, v80, v81, v76\n"
        "v_max_f32 v80, v80, v77\n"
        "v_cmp_lt_f32 vcc, 0x4b800000, v80\n"
        "12:\n"
        "v_mov_b64 v[12:13], 0\n"
        "v_mov_b64 v[14:15], 0\n"
        "v_mov_b64 v[40:41], 0\n"
        "v_mfma_f32_16x16x32_bf16 v[36:39], v[32:35], v[32:35], 0\n"
        "s_waitcnt lgkmcnt(0)\n"
        "s_barrier\n"
        "ds_read_b128 v[52:55], v85\n"
        "ds_read_b128 v[56:59], v85 offset:1024\n"
        "v_mov_b64 v[42:43], 0\n"
        "s_cbranch_vccnz 11f\n"
        "ds_read_b32 v5, v85 offset:7176\n"
        "v_mov_b64 v[8:9], 0\n"
        "v_mov_b64 v[10:11], 0\n"
        "v_mov_b64 v[0:1], 0\n"
        "v_mov_b64 v[2:3], 0\n"
        "v_mov_b64 v[60:61], 0\n"
        "v_mov_b64 v[62:63], 0\n"
        "s_waitcnt lgkmcnt(2)\n"
        "v_smfmac_f32_16x16x64_bf16 v[32:35], v[52:55], v[184:191], v84\n"
        "s_waitcnt lgkmcnt(1)\n"
        "v_smfmac_f32_16x16x64_bf16 v[32:35], v[56:59], v[68:75], v84\n"
        "ds_read_b128 v[68:71], v86 offset:24576\n"
        "ds_read_b128 v[72:75], v86 offset:25600\n"
        "v_smfmac_f32_16x16x64_bf16 v[40:43], v[52:55], v[192:199], v84\n"
        "s_waitcnt lgkmcnt(2)\n"
        "buffer_load_dwordx4 v[80:83], v5, s[40:43], s51 offen sc1\n"
        "v_smfmac_f32_16x16x64_bf16 v[40:43], v[56:59], v[16:23], v84\n"
        "ds_read_b128 v[16:19], v86 offset:26624\n"
        "v_add_f32 v32, v32, v33\n"
        "v_add_f32 v34, v34, v35\n"
        "v_mul_f32 v32, v32, v76\n"
        "v_mul_f32 v34, v34, v77\n"
        "v_cvt_pk_bf16_f32 v32, v32, v34\n"
        "v_smfmac_f32_16x16x64_bf16 v[36:39], v[52:55], v[208:215], v84\n"
        "buffer_store_dword v32, v66, s[40:43], s56 offen sc1\n"
        "v_add_f32 v40, v40, v41\n"
        "v_smfmac_f32_16x16x64_bf16 v[36:39], v[56:59], v[24:31], v84\n"
        "s_xor_b32 s56, s56, 0x100000\n"
        "ds_read_b128 v[20:23], v86 offset:27648\n"
        "ds_read_b128 v[24:27], v86 offset:28672\n"
        "v_add_f32 v42, v42, v43\n"
        "v_mul_f32 v40, v40, v76\n"
        "v_mul_f32 v42, v42, v77\n"
        "v_cvt_pk_bf16_f32 v40, v40, v42\n"
        "v_smfmac_f32_16x16x64_bf16 v[8:11], v[52:55], v[216:223], v84\n"
        "s_waitcnt lgkmcnt(3)\n"
        "buffer_store_dword v40, v66, s[40:43], s57 offen sc1\n"
        "v_add_f32 v36, v36, v37\n"
        "v_smfmac_f32_16x16x64_bf16 v[8:11], v[56:59], v[68:75], v84\n"
        "s_xor_b32 s57, s57, 0x100000\n"
        "ds_read_b128 v[28:31], v86 offset:29696\n"
        "ds_read_b128 v[68:71], v86 offset:30720\n"
        "ds_read_b128 v[72:75], v86 offset:31744\n"
        "v_mov_b64 v[32:33], 0\n"
        "v_add_f32 v38, v38, v39\n"
        "v_mul_f32 v36, v36, v76\n"
        "v_mul_f32 v38, v38, v77\n"
        "v_cvt_pk_bf16_f32 v36, v36, v38\n"
        "v_smfmac_f32_16x16x64_bf16 v[12:15], v[52:55], v[224:231], v84\n"
        "s_waitcnt lgkmcnt(4)\n"
        "buffer_store_dword v36, v66, s[40:43], s58 offen sc1\n"
        "v_add_f32 v8, v8, v9\n"
        "v_smfmac_f32_16x16x64_bf16 v[12:15], v[56:59], v[16:23], v84\n"
        "s_xor_b32 s58, s58, 0x100000\n"
        "ds_read_b128 v[16:19], v86 offset:32768\n"
        "ds_read_b128 v[20:23], v86 offset:33792\n"
        "v_mov_b64 v[34:35], 0\n"
        "v_add_f32 v10, v10, v11\n"
        "v_mul_f32 v8, v8, v76\n"
        "v_mul_f32 v10, v10, v77\n"
        "v_cvt_pk_bf16_f32 v8, v8, v10\n"
        "v_smfmac_f32_16x16x64_bf16 v[0:3], v[52:55], v[232:239], v84\n"
        "s_waitcnt lgkmcnt(4)\n"
        "buffer_store_dword v8, v66, s[40:43], s59 offen sc1\n"
        "v_add_f32 v12, v12, v13\n"
        "v_smfmac_f32_16x16x64_bf16 v[0:3], v[56:59], v[24:31], v84\n"
        "s_xor_b32 s59, s59, 0x100000\n"
        "ds_read_b128 v[24:27], v86 offset:34816\n"
        "ds_read_b128 v[28:31], v86 offset:35840\n"
        "v_mov_b64 v[40:41], 0\n"
        "v_add_f32 v14, v14, v15\n"
        "v_mul_f32 v12, v12, v76\n"
        "v_mul_f32 v14, v14, v77\n"
        "v_cvt_pk_bf16_f32 v12, v12, v14\n"
        "v_smfmac_f32_16x16x64_bf16 v[60:63], v[52:55], v[240:247], v84\n"
        "s_waitcnt lgkmcnt(4)\n"
        "buffer_store_dword v12, v66, s[40:43], s60 offen sc1\n"
        "v_add_f32 v0, v0, v1\n"
        "v_smfmac_f32_16x16x64_bf16 v[60:63], v[56:59], v[68:75], v84\n"
        "s_xor_b32 s60, s60, 0x100000\n"
        "v_mov_b64 v[42:43], 0\n"
        "v_add_f32 v2, v2, v3\n"
        "v_mul_f32 v0, v0, v76\n"
        "v_mul_f32 v2, v2, v77\n"
        "v_cvt_pk_bf16_f32 v0, v0, v2\n"
        "v_smfmac_f32_16x16x64_bf16 v[32:35], v[52:55], v[248:255], v84\n"
        "s_waitcnt lgkmcnt(2)\n"
        "buffer_store_dword v0, v66, s[40:43], s61 offen sc1\n"
        "v_add_f32 v60, v60, v61\n"
        "v_smfmac_f32_16x16x64_bf16 v[32:35], v[56:59], v[16:23], v84\n"
        "s_xor_b32 s61, s61, 0x100000\n"
        "v_add_f32 v62, v62, v63\n"
        "v_mul_f32 v60, v60, v76\n"
        "v_mul_f32 v62, v62, v77\n"
        "v_cvt_pk_bf16_f32 v60, v60, v62\n"
        "v_smfmac_f32_16x16x64_bf16 v[40:43], v[52:55], v[200:207], v84\n"
        "s_waitcnt lgkmcnt(0)\n"
        "buffer_store_dword v60, v66, s[40:43], s62 offen sc1\n"
        "v_add_f32 v32, v32, v33\n"
        "v_smfmac_f32_16x16x64_bf16 v[40:43], v[56:59], v[24:31], v84\n"
        "s_nop 1\n"
        "v_add_f32 v34, v34, v35\n"
        "v_mul_f32 v32, v32, v76\n"
        "v_mul_f32 v34, v34, v77\n"
        "v_cvt_pk_bf16_f32 v32, v32, v34\n"
        "buffer_store_dword v32, v66, s[40:43], s63 offen sc1\n"
        "s_xor_b32 s62, s62, 0x100000\n"
        "v_add_f32 v40, v40, v41\n"
        "v_add_f32 v42, v42, v43\n"
        "v_mul_f32 v40, v40, v76\n"
        "v_mul_f32 v42, v42, v77\n"
        "v_cvt_pk_bf16_f32 v40, v40, v42\n"
        "buffer_store_dword v40, v66, s[40:43], s64 offen sc1\n"
        "s_xor_b32 s63, s63, 0x100000\n"
        "s_lshl1_add_u32 s78, s44, s45\n"
        "s_sub_u32 s79, s48, 1\n"
        "s_or_b32 s79, s79, s30\n"
        "s_cmp_lt_i32 s79, 0\n"
        "s_cselect_b32 s78, s34, s78\n"
        "s_cselect_b32 s79, s26, s74\n"
        "s_sub_u32 s35, s78, s34\n"
        "s_sub_u32 s68, s79, s26\n"
        "s_mov_b32 s34, s78\n"
        "s_mov_b32 s26, s79\n"
        "s_sub_u32 s28, s28, 8\n"
        "s_max_u32 s28, s28, 211\n"
        "s_max_u32 s28, s28, s80\n"
        "s_add_u32 s27, s28, s29\n"
        "s_add_u32 s45, s45, s27\n"
        "s_lshr_b32 s79, s45, 3\n"
        "s_lshl2_add_u32 s44, s79, s44\n"
        "s_and_b32 s45, s45, 7\n"
        "s_add_u32 s49, s49, 32768\n"
        "s_add_u32 s50, s50, 32768\n"
        "s_add_u32 s81, s81, 32768\n"
        "s_add_u32 s52, s52, 262144\n"
        "s_add_u32 s53, s53, 262144\n"
        "s_add_u32 s39, s39, 262144\n"
        "s_add_u32 s48, s48, 1\n"
        "s_xor_b32 s64, s64, 0x100000\n"
        "10:\n"
        "buffer_store_dword v64, v67, s[40:43], s54 offen offset:0 sc1\n"
        "buffer_store_dword v4, v67, s[40:43], s54 offen offset:128 sc1\n"
        "s_mov_b64 exec, s[36:37]\n"
        "buffer_store_dwordx4 v[44:47], v85, s[40:43], s55 offen sc1\n"
        "buffer_store_dwordx4 v[48:51], v85, s[40:43], s55 offen offset:1024 sc1\n"
        "s_mov_b64 exec, -1\n"
        "s_waitcnt vmcnt(13)\n"
        "31:\n"
        "v_max3_u32 v52, v80, v81, v82\n"
        "v_max_u32 v52, v52, v83\n"
        "v_cmp_gt_i32 vcc, 0, v52\n"
        "v_readlane_b32 s78, v52, 0\n"
        "v_readlane_b32 s79, v52, 62\n"
        "s_max_u32 s80, s78, s79\n"
        "s_cbranch_vccnz 30f\n"
        "32:\n"
        "s_barrier\n"
        "s_add_u32 s51, s51, 32768\n"
        "13:\n"
        "s_memrealtime s[76:77]\n"
        "s_mov_b64 s[46:47], %1\n"
        "s_lshl_b32 s78, s45, 2\n"
        "s_sub_u32 s78, 5b-20b, s78\n"
        "s_add_u32 s46, s46, s78\n"
        "s_addc_u32 s47, s47, 0\n"
        "s_add_u32 s55, s55, 65536\n"
        "s_add_u32 s54, s54, 65536\n"
        "s_mov_b32 s78, 64\n"
        "s_waitcnt lgkmcnt(0)\n"
        "s_mov_b32 s27, s76\n"
        "21:\n"
        "s_memrealtime s[76:77]\n"
        "s_cmp_ge_u32 s76, s44\n"
        "s_cbranch_scc1 22f\n"
        "s_memrealtime s[76:77]\n"
        "s_cmp_ge_u32 s76, s44\n"
        "s_cbranch_scc1 22f\n"
        "s_memrealtime s[76:77]\n"
        "s_cmp_ge_u32 s76, s44\n"
        "s_cbranch_scc1 22f\n"
        "s_memrealtime s[76:77]\n"
        "s_cmp_ge_u32 s76, s44\n"
        "s_cbranch_scc1 22f\n"
        "s_memrealtime s[76:77]\n"
        "s_cmp_ge_u32 s76, s44\n"
        "s_cbranch_scc1 22f\n"
        "s_memrealtime s[76:77]\n"
        "s_cmp_ge_u32 s76, s44\n"
        "s_cbranch_scc1 22f\n"
        "s_memrealtime s[76:77]\n"
        "s_cmp_ge_u32 s76, s44\n"
        "s_cbranch_scc1 22f\n"
        "s_memrealtime s[76:77]\n"
        "s_cmp_ge_u32 s76, s44\n"
        "s_cbranch_scc1 22f\n"
        "s_sub_u32 s78, s78, 1\n"
        "s_cbranch_scc0 21b\n"
        "22:\n"
        "s_setpc_b64 s[46:47]\n"
        "2:\n"
        "v_bfe_u32 v0, v85, 2, 4\n"
        "buffer_load_dword v1, v0, s[40:43], s82 offen sc1\n"
        "s_waitcnt vmcnt(0)\n"
        "3:\n"
        "v_cmp_ge_u32 vcc, s48, v1\n"
        "s_cbranch_vccz 4f\n"
        "s_cmp_eq_u32 s67, 0\n"
        "s_cbranch_scc1 4f\n"
        "s_sub_u32 s67, s67, 1\n"
        "s_sleep 2\n"
        "s_branch 2b\n"
        "4:\n"
        "s_mov_b32 exec_hi, 0\n"
        "v_lshlrev_b32 v0, 3, v85\n"
        "s_sub_u32 s78, s39, 262144\n"
        "buffer_load_dword v1, v0, s[40:43], s78 offen sc1\n"
        "s_mov_b32 exec_hi, -1\n"
        "s_cmp_eq_u32 s48, 0\n"
        "s_cbranch_scc0 32b\n"
        "s_sub_u32 s79, s44, 16\n"
        "s_mov_b32 s78, 256\n"
        "14:\n"
        "s_memrealtime s[76:77]\n"
        "s_waitcnt lgkmcnt(0)\n"
        "s_mov_b32 s27, s76\n"
        "s_cmp_ge_u32 s27, s79\n"
        "s_cbranch_scc1 5b\n"
        "s_sub_u32 s78, s78, 1\n"
        "s_cbranch_scc0 14b\n"
        "s_branch 5b\n"
        "30:\n"
        "s_lshl1_add_u32 s78, %5, 1\n"
        "s_bitcmp1_b64 vcc, s78\n"
        "s_cselect_b32 s66, 1, 0\n"
        "s_andn2_b64 vcc, vcc, s[72:73]\n"
        "s_cbranch_scc0 34f\n"
        "v_cmp_eq_u32 s[76:77], -1, v52\n"
        "s_andn2_b64 s[76:77], s[76:77], s[72:73]\n"
        "s_cbranch_scc0 33f\n"
        "s_cmp_eq_u32 s67, 0\n"
        "s_cbranch_scc1 33f\n"
        "s_sub_u32 s67, s67, 1\n"
        "s_sleep 8\n"
        "buffer_load_dwordx4 v[80:83], v5, s[40:43], s51 offen sc1\n"
        "s_waitcnt vmcnt(0)\n"
        "s_branch 31b\n"
        "33:\n"
        "s_sub_u32 s44, s44, 1024\n"
        "v_subrev_u32 v52, s44, v52\n"
        "v_lshlrev_b32 v52, 1, v52\n"
        "v_cndmask_b32 v52, 0, v52, vcc\n"
        "s_nop 1\n"
        "v_max_i32_dpp v52, v52, v52 row_ror:2 row_mask:0xf bank_mask:0xf\n"
        "s_nop 1\n"
        "v_max_i32_dpp v52, v52, v52 row_ror:4 row_mask:0xf bank_mask:0xf\n"
        "s_nop 1\n"
        "v_max_i32_dpp v52, v52, v52 row_ror:8 row_mask:0xf bank_mask:0xf\n"
        "s_nop 0\n"
        "v_readlane_b32 s78, v52, 0\n"
        "v_readlane_b32 s79, v52, 16\n"
        "s_max_i32 s78, s78, s79\n"
        "v_readlane_b32 s79, v52, 32\n"
        "s_max_i32 s78, s78, s79\n"
        "v_readlane_b32 s79, v52, 48\n"
        "s_max_i32 s78, s78, s79\n"
        "s_add_u32 s78, s78, 10\n"
        "s_add_u32 s27, s27, s78\n"
        "s_add_u32 s27, s27, %11\n"
        "s_add_u32 s29, s29, %13\n"
        "s_add_u32 %12, %12, %13\n"
        "s_mov_b32 s45, 7\n"
        "s_mov_b32 s80, s28\n"
        "s_add_u32 s45, s45, s27\n"
        "s_lshr_b32 s78, s45, 3\n"
        "s_lshl_b32 s78, s78, 2\n"
        "s_add_u32 s44, s44, s78\n"
        "s_and_b32 s45, s45, 7\n"
        "34:\n"
        "s_cmp_eq_u32 s66, 0\n"
        "s_cbranch_scc1 32b\n"
        "s_branch 2b\n"
        "8:\n"
        "s_nop 7\n"
        "v_max_u32 v68, v0, v1\n"
        "v_max3_u32 v68, v68, v2, v3\n"
        "v_max3_u32 v68, v68, v4, v5\n"
        "v_max3_u32 v68, v68, v6, v7\n"
        "v_max3_u32 v68, v68, v8, v9\n"
        "v_max3_u32 v68, v68, v10, v11\n"
        "v_max3_u32 v68, v68, v12, v13\n"
        "v_max3_u32 v68, v68, v14, v15\n"
        "v_max3_u32 v68, v68, v16, v17\n"
        "v_max3_u32 v68, v68, v18, v19\n"
        "v_max3_u32 v68, v68, v20, v21\n"
        "v_max3_u32 v68, v68, v22, v23\n"
        "v_max3_u32 v68, v68, v24, v25\n"
        "v_max3_u32 v68, v68, v26, v27\n"
        "v_max3_u32 v68, v68, v28, v29\n"
        "v_max3_u32 v68, v68, v30, v31\n"
        "v_max3_u32 v68, v68, v32, v33\n"
        "v_max3_u32 v68, v68, v34, v35\n"
        "v_cmp_eq_u32 vcc, -1, v68\n"
        "s_cbranch_vccz 7b\n"
        "s_cmp_eq_u32 s67, 0\n"
        "s_cbranch_scc1 7b\n"
        "s_memrealtime s[76:77]\n"
        "s_sub_u32 s67, s67, 1\n"
        "s_add_u32 s69, s69, 1\n"
        "s_waitcnt vmcnt(0) lgkmcnt(0)\n"
        "s_max_u32 s27, s76, s44\n"
        "s_branch 5b\n"
        "11:\n"
        "s_waitcnt lgkmcnt(0)\n"
        "v_max_f32 v12, v8, v10\n"
        "s_nop 1\n"
        "v_max_f32_dpp v12, v12, v12 row_ror:8 row_mask:0xf bank_mask:0xf\n"
        "v_mov_b32 v13, v12\n"
        "s_nop 1\n"
        "v_permlane16_swap_b32 v12, v13\n"
        "v_max_f32 v12, v12, v13\n"
        "v_mov_b32 v13, v12\n"
        "s_nop 1\n"
        "v_permlane32_swap_b32 v12, v13\n"
        "v_max_f32 v12, v12, v13\n"
        "v_and_b32 v13, 0xf0, v85\n"
        "s_lshr_b32 s78, s65, 6\n"
        "v_add_u32 v14, s78, v13\n"
        "ds_write_b32 v14, v12 offset:2048\n"
        "s_waitcnt lgkmcnt(0)\n"
        "s_barrier\n"
        "ds_read_b128 v[36:39], v13 offset:2048\n"
        "s_waitcnt lgkmcnt(0)\n"
        "v_max3_f32 v12, v36, v37, v38\n"
        "v_max_f32 v12, v12, v39\n"
        "v_sub_f32 v8, v8, v12\n"
        "v_sub_f32 v10, v10, v12\n"
        "v_mul_f32 v9, 0x3e0293ee, v8\n"
        "v_mul_f32 v11, 0x3e0293ee, v10\n"
        "v_exp_f32 v9, v9\n"
        "v_exp_f32 v11, v11\n"
        "s_nop 0\n"
        "v_cvt_pk_bf16_f32 v9, v9, v11\n"
        "ds_write_b32 v65, v9\n"
        "s_waitcnt lgkmcnt(0)\n"
        "s_barrier\n"
        "ds_read_b128 v[36:39], v85 offset:4096\n"
        "v_mov_b64 v[52:53], 0\n"
        "v_mov_b64 v[54:55], 0\n"
        "v_mov_b64 v[56:57], 0\n"
        "v_mov_b64 v[58:59], 0\n"
        "v_mov_b64 v[80:81], s[70:71]\n"
        "s_waitcnt lgkmcnt(0)\n"
        "v_smfmac_f32_16x16x64_bf16 v[52:55], v[36:39], v[60:67], v84\n"
        "v_smfmac_f32_16x16x64_bf16 v[56:59], v[36:39], v[0:7], v84\n"
        "v_mfma_f32_16x16x32_bf16 v[76:79], v[36:39], v[80:83], 0\n"
        "s_nop 0\n"
        "v_add_f32 v52, v52, v53\n"
        "v_add_f32 v54, v54, v55\n"
        "s_nop 0\n"
        "v_add_f32 v56, v56, v57\n"
        "v_add_f32 v58, v58, v59\n"
        "v_cvt_pk_bf16_f32 v52, v52, v56\n"
        "v_cvt_pk_bf16_f32 v54, v54, v58\n"
        "ds_write2_b32 v87, v52, v54 offset1:8\n"
        "v_add_f32 v80, v76, v77\n"
        "v_add_f32 v81, v78, v79\n"
        "v_rcp_f32 v76, v80\n"
        "v_rcp_f32 v77, v81\n"
        "s_mov_b64 vcc, 0\n"
        "s_branch 12b\n"
        "9:\n"
        "buffer_store_dword v64, v67, s[40:43], s54 offen offset:0 sc1\n"
        "buffer_store_dword v4, v67, s[40:43], s54 offen offset:128 sc1\n"
        "s_mov_b64 exec, s[36:37]\n"
        "buffer_store_dwordx4 v[44:47], v85, s[40:43], s55 offen sc1\n"
        "buffer_store_dwordx4 v[48:51], v85, s[40:43], s55 offen offset:1024 sc1\n"
        "s_mov_b64 exec, -1\n"
        "v_mov_b32 v40, 64\n"
        "s_lshl2_add_u32 s78, %6, s82\n"
        "s_add_u32 s78, s78, 0x401000-0x400000\n"
        "s_waitcnt vmcnt(0) lgkmcnt(0)\n"
        "s_mov_b64 exec, 1\n"
        "buffer_store_dword v40, off, s[40:43], s78 sc1\n"
        ".Lcode_to:\n"
        "s_endpgm\n"
        :: "s"(ws), "s"(Wq), "s"(Wk), "s"(Wv), "s"(Wo), "s"(hd), "s"(w), "s"(blk), "s"(hop), "s"(off), "s"(cyc), "s"(extra), "s"(bias), "s"(bump)
        : "v0", "v1", "v2", "v3", "v4", "v5", "v6", "v7", "v8", "v9", "v10", "v11", "v12", "v13", "v14", "v15", "v16", "v17", "v18", "v19", "v20", "v21", "v22", "v23", "v24", "v25", "v26", "v27", "v28", "v29", "v30", "v31", "v32", "v33", "v34", "v35", "v36", "v37", "v38", "v39", "v40", "v41", "v42", "v43", "v44", "v45", "v46", "v47", "v48", "v49", "v50", "v51", "v52", "v53", "v54", "v55", "v56", "v57", "v58", "v59", "v60", "v61", "v62", "v63", "v64", "v65", "v66", "v67", "v68", "v69", "v70", "v71", "v72", "v73", "v74", "v75", "v76", "v77", "v78", "v79", "v80", "v81", "v82", "v83", "v84", "v85", "v86", "v87", "v88", "v89", "v90", "v91", "v92", "v93", "v94", "v95", "v96", "v97", "v98", "v99", "v100", "v101", "v102", "v103", "v104", "v105", "v106", "v107", "v108", "v109", "v110", "v111", "v112", "v113", "v114", "v115", "v116", "v117", "v118", "v119", "v120", "v121", "v122", "v123", "v124", "v125", "v126", "v127", "v128", "v129", "v130", "v131", "v132", "v133", "v134", "v135", "v136", "v137", "v138", "v139", "v140", "v141", "v142", "v143", "v144", "v145", "v146", "v147", "v148", "v149", "v150", "v151", "v152", "v153", "v154", "v155", "v156", "v157", "v158", "v159", "v160", "v161", "v162", "v163", "v164", "v165", "v166", "v167", "v168", "v169", "v170", "v171", "v172", "v173", "v174", "v175", "v176", "v177", "v178", "v179", "v180", "v181", "v182", "v183", "v184", "v185", "v186", "v187", "v188", "v189", "v190", "v191", "v192", "v193", "v194", "v195", "v196", "v197", "v198", "v199", "v200", "v201", "v202", "v203", "v204", "v205", "v206", "v207", "v208", "v209", "v210", "v211", "v212", "v213", "v214", "v215", "v216", "v217", "v218", "v219", "v220", "v221", "v222", "v223", "v224", "v225", "v226", "v227", "v228", "v229", "v230", "v231", "v232", "v233", "v234", "v235", "v236", "v237", "v238", "v239", "v240", "v241", "v242", "v243", "v244", "v245", "v246", "v247", "v248", "v249", "v250", "v251", "v252", "v253", "v254", "v255", "a0", "a1", "a2", "a3", "a4", "a5", "a6", "a7", "a8", "a9", "a10", "a11", "a12", "a13", "a14", "a15", "a16", "a17", "a18", "a19", "a20", "a21", "a22", "a23", "a24", "a25", "a26", "a27", "a28", "a29", "a30", "a31", "a32", "a33", "a34", "a35", "a36", "a37", "a38", "a39", "a40", "a41", "a42", "a43", "a44", "a45", "a46", "a47", "a48", "a49", "a50", "a51", "a52", "a53", "a54", "a55", "a56", "a57", "a58", "a59", "a60", "a61", "a62", "a63", "a64", "a65", "a66", "a67", "a68", "a69", "a70", "a71", "a72", "a73", "a74", "a75", "a76", "a77", "a78", "a79", "a80", "a81", "a82", "a83", "a84", "a85", "a86", "a87", "a88", "a89", "a90", "a91", "a92", "a93", "a94", "a95", "a96", "a97", "a98", "a99", "a100", "a101", "a102", "a103", "a104", "a105", "a106", "a107", "a108", "a109", "a110", "a111", "a112", "a113", "a114", "a115", "a116", "a117", "a118", "a119", "a120", "a121", "a122", "a123", "a124", "a125", "a126", "a127", "a128", "a129", "a130", "a131", "a132", "a133", "a134", "a135", "a136", "a137", "a138", "a139", "a140", "a141", "a142", "a143", "a144", "a145", "a146", "a147", "a148", "a149", "a150", "a151", "a152", "a153", "a154", "a155", "a156", "a157", "a158", "a159", "a160", "a161", "a162", "a163", "a164", "a165", "a166", "a167", "a168", "a169", "a170", "a171", "a172", "a173", "a174", "a175", "a176", "a177", "a178", "a179", "a180", "a181", "a182", "a183", "a184", "a185", "a186", "a187", "a188", "a189", "a190", "a191", "a192", "a193", "a194", "a195", "a196", "a197", "a198", "a199", "a200", "a201", "a202", "a203", "a204", "a205", "a206", "a207", "a208", "a209", "a210", "a211", "a212", "a213", "a214", "a215", "a216", "a217", "a218", "a219", "a220", "a221", "a222", "a223", "a224", "a225", "a226", "a227", "a228", "a229", "a230", "a231", "a232", "a233", "a234", "a235", "a236", "a237", "a238", "a239", "a240", "a241", "a242", "a243", "a244", "a245", "a246", "a247", "a248", "a249", "a250", "a251", "a252", "a253", "a254", "a255", "s26", "s27", "s28", "s29", "s30", "s31", "s34", "s35", "s36", "s37", "s38", "s39", "s40", "s41", "s42", "s43", "s44", "s45", "s46", "s47", "s48", "s49", "s50", "s51", "s52", "s53", "s54", "s55", "s56", "s57", "s58", "s59", "s60", "s61", "s62", "s63", "s64", "s65", "s66", "s67", "s68", "s69", "s70", "s71", "s72", "s73", "s74", "s75", "s76", "s77", "s78", "s79", "s80", "s81", "s82", "s83", "s84", "s85", "s86", "s87", "vcc", "scc", "memory");
    __builtin_unreachable();
}
__device__ __forceinline__ void pull_weights(const u16* Wq, const u16* Wk, const u16* Wv, const u16* Wo, int xc, int j, int lane) {
    constexpr int order[37] = {1, 3, 5, 7, 8, 48, 49, 50, 51, 52, 17, 18, 19, 20, 21, 22, 23, 24, 32, 33, 34, 35, 36, 37, 38, 39, 40, 0, 16, 2, 4, 6, 53, 54, 55, 56, 57};
#pragma unroll
    for (int p = HS; p < 37; ++p) {
        const int m = order[p] >> 4, c = order[p] & 15;
        const rsrc_t rw = __builtin_amdgcn_make_buffer_rsrc((void*)(m == 0 ? Wq : m == 1 ? Wk : m == 2 ? Wv : Wo), 0, 4718592, 0x00020000);
        const int off = m < 3 ? (c * 64 + j) * 8192 + xc * 4096 : (xc * 128 + j * 2 + c / 5) * 18432 + c % 5 * 4096;
        asm volatile("s_mov_b32 m0, 0\n\ts_waitcnt vmcnt(%3)\n\tbuffer_load_dwordx4 %0, %1, %2 offen lds\n\tbuffer_load_dwordx4 %0, %1, %2 offen offset:1024 lds"
                     :: "v"(lane * 16), "s"(rw), "s"(off), "n"(HD) : "memory");
        if (m < 3 || c % 5 < 4)
            asm volatile("buffer_load_dwordx4 %0, %1, %2 offen offset:2048 lds\n\tbuffer_load_dwordx4 %0, %1, %2 offen offset:3072 lds" :: "v"(lane * 16), "s"(rw), "s"(off) : "memory");
    }
    WAIT_VM(0);
}

__device__ __forceinline__ void reducer(int u, unsigned char* ws, unsigned par, unsigned start, int lane, int hop, int off, int skew, int extra, int bump) {
    const int tau = u >> 1, beta = u & 1;
    const int m = lane & 15;
    const int jsel = m < 8 ? 2 * (m & 3) + (m >> 2) : -1;
    v4u sel;
#pragma unroll
    for (int j = 0; j < 4; ++j) sel[j] = (jsel == 2 * j ? 0x3f80u : 0u) | (jsel == 2 * j + 1 ? 0x3f800000u : 0u);
    const int src = OFF_PART + tau * 16384 + (par >> 1) * 8192 + beta * 4096;
    const int dst = OFF_MEM + 32768 + ((tau >> 2) >> 3) * 16384 + (par & 1) * 8192 + ((tau >> 2) & 7) * 1024 + (tau & 3) * 256 + beta * 128;
    const int dlane = (2 * ((m >> 2) & 1) + (lane >> 4)) * 32 + (m & 3) * 8;
    asm volatile(
        "s_mov_b64 s[40:41], %0\n"
        "s_mov_b32 s42, 0x6800000\n"
        "s_mov_b32 s43, 0x20000\n"
        "s_memrealtime s[52:53]\n"
        "s_mov_b32 s44, 0\n"
        "s_mov_b32 s67, %7\n"
        "s_mov_b32 s66, %8\n"
        "s_mov_b32 s45, %1\n"
        "s_mov_b32 s46, %2\n"
        "s_mov_b32 s57, %3\n"
        "s_mov_b32 s47, 0x10000\n"
        "s_mov_b32 s54, 0\n"
        "s_mov_b32 s69, 8\n"
        "s_mov_b32 s55, 0\n"
        "s_mov_b32 s56, -1\n"
        "v_mbcnt_lo_u32_b32 v24, -1, 0\n"
        "v_mbcnt_hi_u32_b32 v24, -1, v24\n"
        "v_and_b32 v33, 2, v24\n"
        "v_lshlrev_b32 v32, 5, v33\n"
        "v_lshrrev_b32 v33, 2, v24\n"
        "v_lshl_add_u32 v32, v33, 8, v32\n"
        "v_lshlrev_b32 v24, 4, v24\n"
        "v_mov_b32 v20, -1\n"
        "v_mov_b32 v21, -1\n"
        "v_mov_b32 v22, -1\n"
        "v_mov_b32 v23, -1\n"
        "s_waitcnt lgkmcnt(0)\n"
        "s_sub_u32 s48, %12, s52\n"
        "s_and_b32 s61, s48, 3\n"
        "s_sub_u32 s60, %12, s61\n"
        "s_lshl_b32 s61, s61, 1\n"
        "s_branch 8f\n"
        "1:\n"
        "s_add_u32 s49, s66, %9\n"
        "s_min_i32 s48, s69, 0\n"
        "s_max_i32 s48, s48, 0\n"
        "s_sub_u32 s49, s49, s48\n"
        "s_sub_u32 s69, s69, 4\n"
        "s_add_u32 s63, s61, s49\n"
        "s_lshr_b32 s48, s63, 3\n"
        "s_lshl_b32 s48, s48, 2\n"
        "s_add_u32 s62, s60, s48\n"
        "s_and_b32 s63, s63, 7\n"
        "7:\n"
        "s_memrealtime s[52:53]\n"
        "s_getpc_b64 s[64:65]\n"
        "20:\n"
        "s_lshl_b32 s48, s63, 2\n"
        "s_sub_u32 s48, 23f-20b, s48\n"
        "s_add_u32 s64, s64, s48\n"
        "s_addc_u32 s65, s65, 0\n"
        "s_mov_b32 s48, 256\n"
        "s_waitcnt lgkmcnt(0)\n"
        "21:\n"
        "s_memrealtime s[52:53]\n"
        "s_cmp_ge_u32 s52, s62\n"
        "s_cbranch_scc1 22f\n"
        "s_memrealtime s[52:53]\n"
        "s_cmp_ge_u32 s52, s62\n"
        "s_cbranch_scc1 22f\n"
        "s_memrealtime s[52:53]\n"
        "s_cmp_ge_u32 s52, s62\n"
        "s_cbranch_scc1 22f\n"
        "s_memrealtime s[52:53]\n"
        "s_cmp_ge_u32 s52, s62\n"
        "s_cbranch_scc1 22f\n"
        "s_memrealtime s[52:53]\n"
        "s_cmp_ge_u32 s52, s62\n"
        "s_cbranch_scc1 22f\n"
        "s_memrealtime s[52:53]\n"
        "s_cmp_ge_u32 s52, s62\n"
        "s_cbranch_scc1 22f\n"
        "s_memrealtime s[52:53]\n"
        "s_cmp_ge_u32 s52, s62\n"
        "s_cbranch_scc1 22f\n"
        "s_memrealtime s[52:53]\n"
        "s_cmp_ge_u32 s52, s62\n"
        "s_cbranch_scc1 22f\n"
        "s_sub_u32 s48, s48, 1\n"
        "s_cbranch_scc0 21b\n"
        "22:\n"
        "s_setpc_b64 s[64:65]\n"
        "s_nop 2\n"
        "s_nop 2\n"
        "s_nop 2\n"
        "s_nop 2\n"
        "s_nop 2\n"
        "s_nop 2\n"
        "s_nop 2\n"
        "23:\n"
        "8:\n"
        "buffer_load_dwordx4 v[0:3], v24, s[40:43], s45 offen offset:0 sc1\n"
        "buffer_load_dwordx4 v[4:7], v24, s[40:43], s45 offen offset:1024 sc1\n"
        "buffer_load_dwordx4 v[8:11], v24, s[40:43], s45 offen offset:2048 sc1\n"
        "buffer_load_dwordx4 v[12:15], v24, s[40:43], s45 offen offset:3072 sc1\n"
        "buffer_load_dwordx4 v[36:39], v32, s[40:43], s57 offen sc1\n"
        "s_waitcnt vmcnt(4)\n"
        "v_mfma_f32_16x16x32_bf16 v[16:19], %4, v[0:3], 0\n"
        "s_waitcnt vmcnt(3)\n"
        "v_mfma_f32_16x16x32_bf16 v[16:19], %4, v[4:7], v[16:19]\n"
        "s_waitcnt vmcnt(2)\n"
        "v_mfma_f32_16x16x32_bf16 v[16:19], %4, v[8:11], v[16:19]\n"
        "s_waitcnt vmcnt(1)\n"
        "v_mfma_f32_16x16x32_bf16 v[16:19], %4, v[12:15], v[16:19]\n"
        "s_nop 0\n"
        "s_nop 6\n"
        "v_add_f32_dpp v16, v16, v16 row_ror:8 row_mask:0xf bank_mask:0xf\n"
        "v_add_f32_dpp v17, v17, v17 row_ror:8 row_mask:0xf bank_mask:0xf\n"
        "v_add_f32_dpp v18, v18, v18 row_ror:8 row_mask:0xf bank_mask:0xf\n"
        "v_add_f32_dpp v19, v19, v19 row_ror:8 row_mask:0xf bank_mask:0xf\n"
        "s_mov_b64 exec, s[54:55]\n"
        "v_cmpx_o_f32 vcc, v16, v16\n"
        "v_cvt_pk_bf16_f32 v26, v16, v17\n"
        "v_cvt_pk_bf16_f32 v27, v18, v19\n"
        "buffer_store_dwordx2 v[26:27], %5, s[40:43], s46 offen sc1\n"
        "s_mov_b64 exec, -1\n"
        "s_cmp_eq_u32 vcc_lo, s54\n"
        "s_cbranch_scc1 4f\n"
        "v_max_u32 v25, v0, v1\n"
        "v_max3_u32 v25, v25, v2, v3\n"
        "v_max3_u32 v25, v25, v4, v5\n"
        "v_max3_u32 v25, v25, v6, v7\n"
        "v_max3_u32 v25, v25, v8, v9\n"
        "v_max3_u32 v25, v25, v10, v11\n"
        "v_max3_u32 v25, v25, v12, v13\n"
        "v_max3_u32 v25, v25, v14, v15\n"
        "v_cmp_eq_u32 vcc, -1, v25\n"
        "s_cbranch_vccz 3f\n"
        "s_cmp_eq_u32 s47, 0\n"
        "s_cbranch_scc1 3f\n"
        "s_sub_u32 s47, s47, 1\n"
        "s_waitcnt vmcnt(1)\n"
        "s_branch 31f\n"
        "30:\n"
        "buffer_load_dwordx4 v[36:39], v32, s[40:43], s57 offen sc1\n"
        "s_waitcnt vmcnt(0)\n"
        "31:\n"
        "v_max3_u32 v34, v36, v37, v38\n"
        "v_max_u32 v34, v34, v39\n"
        "v_cmp_gt_i32 vcc, 0, v34\n"
        "v_cmp_eq_u32 s[64:65], -1, v34\n"
        "s_cmp_eq_u64 s[64:65], 0\n"
        "s_cbranch_scc1 32f\n"
        "s_cmp_eq_u32 s47, 0\n"
        "s_cbranch_scc1 32f\n"
        "s_sub_u32 s47, s47, 1\n"
        "s_sleep 4\n"
        "s_branch 30b\n"
        "32:\n"
        "v_subrev_u32 v34, s60, v34\n"
        "v_lshlrev_b32 v34, 1, v34\n"
        "v_cndmask_b32 v34, 0, v34, vcc\n"
        "s_nop 1\n"
        "v_max_i32_dpp v34, v34, v34 row_ror:2 row_mask:0xf bank_mask:0xf\n"
        "s_nop 1\n"
        "v_max_i32_dpp v34, v34, v34 row_ror:4 row_mask:0xf bank_mask:0xf\n"
        "s_nop 1\n"
        "v_max_i32_dpp v34, v34, v34 row_ror:8 row_mask:0xf bank_mask:0xf\n"
        "s_nop 0\n"
        "v_readlane_b32 s48, v34, 0\n"
        "v_readlane_b32 s49, v34, 16\n"
        "s_max_i32 s48, s48, s49\n"
        "v_readlane_b32 s49, v34, 32\n"
        "s_max_i32 s48, s48, s49\n"
        "v_readlane_b32 s49, v34, 48\n"
        "s_max_i32 s48, s48, s49\n"
        "s_add_u32 s48, s48, 10\n"
        "s_lshr_b32 s49, %10, 1\n"
        "s_add_u32 s48, s48, s49\n"
        "s_add_u32 s49, s66, %9\n"
        "s_add_u32 s49, s49, s48\n"
        "s_add_u32 s63, s61, s49\n"
        "s_lshr_b32 s48, s63, 3\n"
        "s_lshl_b32 s48, s48, 2\n"
        "s_add_u32 s62, s60, s48\n"
        "s_and_b32 s63, s63, 7\n"
        "s_branch 7b\n"
        "3:\n"
        "v_cvt_pk_bf16_f32 v26, v16, v17\n"
        "v_cvt_pk_bf16_f32 v27, v18, v19\n"
        "s_mov_b64 exec, s[54:55]\n"
        "buffer_store_dwordx2 v[26:27], %5, s[40:43], s46 offen sc1\n"
        "s_mov_b64 exec, -1\n"
        "4:\n"
        "s_cmp_eq_u32 s54, 0\n"
        "s_mov_b32 s54, 0x00ff00ff\n"
        "s_cbranch_scc1 1b\n"
        "buffer_store_dwordx4 v[20:23], v24, s[40:43], s45 offen offset:0 sc1\n"
        "buffer_store_dwordx4 v[20:23], v24, s[40:43], s45 offen offset:1024 sc1\n"
        "buffer_store_dwordx4 v[20:23], v24, s[40:43], s45 offen offset:2048 sc1\n"
        "buffer_store_dwordx4 v[20:23], v24, s[40:43], s45 offen offset:3072 sc1\n"
        "s_waitcnt vmcnt(0)\n"
        "s_branch 41f\n"
        "40:\n"
        "buffer_load_dwordx4 v[36:39], v32, s[40:43], s57 offen sc1\n"
        "s_waitcnt vmcnt(0)\n"
        "41:\n"
        "v_max3_u32 v34, v36, v37, v38\n"
        "v_max_u32 v34, v34, v39\n"
        "v_cmp_gt_i32 vcc, 0, v34\n"
        "v_cmp_eq_u32 s[64:65], -1, v34\n"
        "s_cmp_eq_u64 s[64:65], 0\n"
        "s_cbranch_scc1 42f\n"
        "s_cmp_eq_u32 s47, 0\n"
        "s_cbranch_scc1 42f\n"
        "s_sub_u32 s47, s47, 1\n"
        "s_sleep 4\n"
        "s_branch 40b\n"
        "42:\n"
        "s_add_u32 s68, s66, s67\n"
        "s_cbranch_vccnz 5f\n"
        "v_readlane_b32 s48, v34, 0\n"
        "v_readlane_b32 s49, v34, 62\n"
        "s_max_u32 s48, s48, s49\n"
        "s_sub_u32 s66, s66, 8\n"
        "s_max_u32 s66, s66, 211\n"
        "s_max_u32 s66, s66, s48\n"
        "s_branch 6f\n"
        "5:\n"
        "v_subrev_u32 v34, s60, v34\n"
        "v_lshlrev_b32 v34, 1, v34\n"
        "v_cndmask_b32 v34, 0, v34, vcc\n"
        "s_nop 1\n"
        "v_max_i32_dpp v34, v34, v34 row_ror:2 row_mask:0xf bank_mask:0xf\n"
        "s_nop 1\n"
        "v_max_i32_dpp v34, v34, v34 row_ror:4 row_mask:0xf bank_mask:0xf\n"
        "s_nop 1\n"
        "v_max_i32_dpp v34, v34, v34 row_ror:8 row_mask:0xf bank_mask:0xf\n"
        "s_nop 0\n"
        "v_readlane_b32 s48, v34, 0\n"
        "v_readlane_b32 s49, v34, 16\n"
        "s_max_i32 s48, s48, s49\n"
        "v_readlane_b32 s49, v34, 32\n"
        "s_max_i32 s48, s48, s49\n"
        "v_readlane_b32 s49, v34, 48\n"
        "s_max_i32 s48, s48, s49\n"
        "s_add_u32 s48, s48, 10\n"
        "s_add_u32 s68, s68, s48\n"
        "s_add_u32 s68, s68, %10\n"
        "s_add_u32 s67, s67, %11\n"
        "s_mov_b32 s61, 7\n"
        "6:\n"
        "s_add_u32 s61, s61, s68\n"
        "s_lshr_b32 s48, s61, 3\n"
        "s_lshl_b32 s48, s48, 2\n"
        "s_add_u32 s60, s60, s48\n"
        "s_and_b32 s61, s61, 7\n"
        "s_add_u32 s44, s44, 1\n"
        "s_xor_b32 s45, s45, 0x100000\n"
        "s_add_u32 s46, s46, 32768\n"
        "s_add_u32 s57, s57, 32768\n"
        "s_cmp_eq_u32 s44, 63\n"
        "s_cbranch_scc0 1b\n"
        "s_waitcnt vmcnt(0) lgkmcnt(0)\n"
        :: "s"(ws), "s"(src), "s"(dst), "s"(OFF_MEM + 20480 + (par & 1) * 8192), "v"(sel), "v"(dlane), "s"(OFF_DBG + (1 << 20) + u * 16), "s"(hop), "s"(off), "s"(skew), "s"(extra), "s"(bump), "s"(start)
        : "v0", "v1", "v2", "v3", "v4", "v5", "v6", "v7", "v8", "v9", "v10", "v11", "v12", "v13", "v14", "v15", "v16", "v17", "v18", "v19", "v20", "v21", "v22", "v23", "v24", "v25", "v26", "v27", "v28", "v29", "v30", "v31", "v32", "v33", "v34", "v35", "v36", "v37", "v38", "v39", "s40", "s41", "s42", "s43", "s44", "s45", "s46", "s47", "s48", "s49", "s50", "s51", "s52", "s53", "s54", "s55", "s56", "s57", "s58", "s59", "s60", "s61", "s62", "s63", "s64", "s65", "s66", "s67", "s68", "s69", "vcc", "scc", "memory");
}

static_assert(FL_ACK == 4347904 && FL_ALL == 4352000 && OFF_DUMMY == 4718592 && OFF_GO == 4456448 && OFF_XF == 5242880 && SPIN_MAX == 65536 && WS_SPAN == 109051904, "constants of the assembly block");
__device__ __forceinline__ void proj_in(unsigned char* ws, const u16* W, int ldw, int out, int stride, int flag, int w, int kind, int flag1, int blk) {
    asm volatile(
        "s_mov_b64 s[40:41], %0\n"
        "s_mov_b32 s42, 0x6800000\n"
        "s_mov_b32 s43, 0x20000\n"
        "s_mov_b64 s[44:45], %1\n"
        "s_mul_i32 s46, %2, 576\n"
        "s_mov_b32 s47, 0x20000\n"
        "s_mul_i32 s56, %6, 12288\n"
        "s_add_u32 s56, s56, 110592\n"
        "s_lshl_b32 s57, %2, 3\n"
        "s_mul_i32 s55, %6, 9216\n"
        "s_add_u32 s49, s55, 0x500000\n"
        "v_mbcnt_lo_u32_b32 v0, -1, 0\n"
        "v_mbcnt_hi_u32_b32 v0, -1, v0\n"
        "v_lshlrev_b32 v120, 4, v0\n"
        "v_lshrrev_b32 v1, 3, v0\n"
        "v_and_b32 v2, 0x70, v120\n"
        "v_mad_u32_u24 v1, v1, %2, v2\n"
        "v_lshrrev_b32 v2, 5, v0\n"
        "v_bfe_u32 v3, v0, 2, 2\n"
        "v_lshl_add_u32 v2, v2, 3, v3\n"
        "v_and_b32 v3, 16, v0\n"
        "v_lshlrev_b32 v3, 1, v3\n"
        "v_lshl_add_u32 v2, v2, 7, v3\n"
        "v_and_b32 v3, 3, v0\n"
        "v_lshl_add_u32 v2, v3, 3, v2\n"
        "v_add_u32 v4, s55, v2\n"
        "v_add_u32 v5, 73728, v4\n"
        "v_add_u32 v2, s56, v2\n"
        "s_cmp_eq_u32 %7, 2\n"
        "s_cselect_b32 s52, 1024, 0\n"
        "s_cselect_b32 s53, 1, 32\n"
        "s_lshl_b32 s53, s53, 10\n"
        "v_lshl_add_u32 v123, v0, 13, %8\n"
        "v_writelane_b32 v123, %5, 0\n"
        "v_mov_b32 v124, s53\n"
        "v_writelane_b32 v124, s52, 0\n"
        "v_and_b32 v125, 0xf0, v120\n"
        "v_lshrrev_b32 v3, 5, v0\n"
        "v_lshl_add_u32 v125, v3, 8, v125\n"
        "v_bfe_u32 v3, v0, 4, 1\n"
        "v_mul_u32_u24 v3, 147456, v3\n"
        "v_add_u32 v125, v3, v125\n"
        "s_mov_b32 s52, 0\n"
        "v_lshrrev_b32 v7, 1, v0\n"
        "v_and_b32 v6, 1, v0\n"
        "v_lshl_add_u32 v7, v7, 3, v6\n"
        "v_lshlrev_b32 v7, 7, v7\n"
        "v_add_u32 v7, 33024, v7\n"
        "s_lshl_b32 s53, %9, 7\n"
        "v_writelane_b32 v7, s53, 26\n"
        "s_mov_b32 s64, 0x440000\n"
        "buffer_load_dword v6, v7, s[40:43], s64 offen sc1\n"
        "s_cmp_eq_u32 %7, 2\n"
        "s_cbranch_scc0 50f\n"
        "50:\n"
        "s_add_u32 m0, s55, 0\n"
        "s_nop 0\n"
        "buffer_load_dwordx4 v1, s[44:47], s52 offen lds\n"
        "s_add_u32 s52, s52, s57\n"
        "s_add_u32 m0, s55, 1024\n"
        "s_nop 0\n"
        "buffer_load_dwordx4 v1, s[44:47], s52 offen lds\n"
        "s_add_u32 s52, s52, s57\n"
        "s_add_u32 m0, s55, 2048\n"
        "s_nop 0\n"
        "buffer_load_dwordx4 v1, s[44:47], s52 offen lds\n"
        "s_add_u32 s52, s52, s57\n"
        "s_add_u32 m0, s55, 3072\n"
        "s_nop 0\n"
        "buffer_load_dwordx4 v1, s[44:47], s52 offen lds\n"
        "s_add_u32 s52, s52, s57\n"
        "s_add_u32 m0, s55, 4096\n"
        "s_nop 0\n"
        "buffer_load_dwordx4 v1, s[44:47], s52 offen lds\n"
        "s_add_u32 s52, s52, s57\n"
        "s_add_u32 m0, s55, 5120\n"
        "s_nop 0\n"
        "buffer_load_dwordx4 v1, s[44:47], s52 offen lds\n"
        "s_add_u32 s52, s52, s57\n"
        "s_add_u32 m0, s55, 6144\n"
        "s_nop 0\n"
        "buffer_load_dwordx4 v1, s[44:47], s52 offen lds\n"
        "s_add_u32 s52, s52, s57\n"
        "s_add_u32 m0, s55, 7168\n"
        "s_nop 0\n"
        "buffer_load_dwordx4 v1, s[44:47], s52 offen lds\n"
        "s_add_u32 s52, s52, s57\n"
        "s_add_u32 m0, s55, 36864\n"
        "s_nop 0\n"
        "buffer_load_dwordx4 v1, s[44:47], s52 offen lds\n"
        "s_add_u32 s52, s52, s57\n"
        "s_add_u32 m0, s55, 37888\n"
        "s_nop 0\n"
        "buffer_load_dwordx4 v1, s[44:47], s52 offen lds\n"
        "s_add_u32 s52, s52, s57\n"
        "s_add_u32 m0, s55, 38912\n"
        "s_nop 0\n"
        "buffer_load_dwordx4 v1, s[44:47], s52 offen lds\n"
        "s_add_u32 s52, s52, s57\n"
        "s_add_u32 m0, s55, 39936\n"
        "s_nop 0\n"
        "buffer_load_dwordx4 v1, s[44:47], s52 offen lds\n"
        "s_add_u32 s52, s52, s57\n"
        "s_add_u32 m0, s55, 40960\n"
        "s_nop 0\n"
        "buffer_load_dwordx4 v1, s[44:47], s52 offen lds\n"
        "s_add_u32 s52, s52, s57\n"
        "s_add_u32 m0, s55, 41984\n"
        "s_nop 0\n"
        "buffer_load_dwordx4 v1, s[44:47], s52 offen lds\n"
        "s_add_u32 s52, s52, s57\n"
        "s_add_u32 m0, s55, 43008\n"
        "s_nop 0\n"
        "buffer_load_dwordx4 v1, s[44:47], s52 offen lds\n"
        "s_add_u32 s52, s52, s57\n"
        "s_add_u32 m0, s55, 44032\n"
        "s_nop 0\n"
        "buffer_load_dwordx4 v1, s[44:47], s52 offen lds\n"
        "s_add_u32 s52, s52, s57\n"
        "s_add_u32 m0, s55, 73728\n"
        "s_nop 0\n"
        "buffer_load_dwordx4 v1, s[44:47], s52 offen lds\n"
        "s_add_u32 s52, s52, s57\n"
        "s_add_u32 m0, s55, 74752\n"
        "s_nop 0\n"
        "buffer_load_dwordx4 v1, s[44:47], s52 offen lds\n"
        "s_add_u32 s52, s52, s57\n"
        "s_add_u32 m0, s55, 75776\n"
        "s_nop 0\n"
        "buffer_load_dwordx4 v1, s[44:47], s52 offen lds\n"
        "s_add_u32 s52, s52, s57\n"
        "s_add_u32 m0, s55, 76800\n"
        "s_nop 0\n"
        "buffer_load_dwordx4 v1, s[44:47], s52 offen lds\n"
        "s_add_u32 s52, s52, s57\n"
        "s_add_u32 m0, s55, 77824\n"
        "s_nop 0\n"
        "buffer_load_dwordx4 v1, s[44:47], s52 offen lds\n"
        "s_add_u32 s52, s52, s57\n"
        "s_add_u32 m0, s55, 78848\n"
        "s_nop 0\n"
        "buffer_load_dwordx4 v1, s[44:47], s52 offen lds\n"
        "s_add_u32 s52, s52, s57\n"
        "s_add_u32 m0, s55, 79872\n"
        "s_nop 0\n"
        "buffer_load_dwordx4 v1, s[44:47], s52 offen lds\n"
        "s_add_u32 s52, s52, s57\n"
        "s_add_u32 m0, s55, 80896\n"
        "s_nop 0\n"
        "buffer_load_dwordx4 v1, s[44:47], s52 offen lds\n"
        "s_add_u32 s52, s52, s57\n"
        "s_add_u32 m0, s56, 0\n"
        "s_nop 0\n"
        "buffer_load_dwordx4 v1, s[44:47], s52 offen lds\n"
        "s_add_u32 s52, s52, s57\n"
        "s_add_u32 m0, s56, 1024\n"
        "s_nop 0\n"
        "buffer_load_dwordx4 v1, s[44:47], s52 offen lds\n"
        "s_add_u32 s52, s52, s57\n"
        "s_add_u32 m0, s56, 2048\n"
        "s_nop 0\n"
        "buffer_load_dwordx4 v1, s[44:47], s52 offen lds\n"
        "s_add_u32 s52, s52, s57\n"
        "s_add_u32 m0, s56, 3072\n"
        "s_nop 0\n"
        "buffer_load_dwordx4 v1, s[44:47], s52 offen lds\n"
        "s_add_u32 s52, s52, s57\n"
        "s_add_u32 m0, s56, 4096\n"
        "s_nop 0\n"
        "buffer_load_dwordx4 v1, s[44:47], s52 offen lds\n"
        "s_add_u32 s52, s52, s57\n"
        "s_add_u32 m0, s56, 5120\n"
        "s_nop 0\n"
        "buffer_load_dwordx4 v1, s[44:47], s52 offen lds\n"
        "s_add_u32 s52, s52, s57\n"
        "s_add_u32 m0, s56, 6144\n"
        "s_nop 0\n"
        "buffer_load_dwordx4 v1, s[44:47], s52 offen lds\n"
        "s_add_u32 s52, s52, s57\n"
        "s_add_u32 m0, s56, 7168\n"
        "s_nop 0\n"
        "buffer_load_dwordx4 v1, s[44:47], s52 offen lds\n"
        "s_add_u32 s52, s52, s57\n"
        "s_add_u32 m0, s56, 8192\n"
        "s_nop 0\n"
        "buffer_load_dwordx4 v1, s[44:47], s52 offen lds\n"
        "s_add_u32 s52, s52, s57\n"
        "s_add_u32 m0, s56, 9216\n"
        "s_nop 0\n"
        "buffer_load_dwordx4 v1, s[44:47], s52 offen lds\n"
        "s_add_u32 s52, s52, s57\n"
        "s_add_u32 m0, s56, 10240\n"
        "s_nop 0\n"
        "buffer_load_dwordx4 v1, s[44:47], s52 offen lds\n"
        "s_add_u32 s52, s52, s57\n"
        "s_add_u32 m0, s56, 11264\n"
        "s_nop 0\n"
        "buffer_load_dwordx4 v1, s[44:47], s52 offen lds\n"
        "s_add_u32 s52, s52, s57\n"
        "s_waitcnt vmcnt(34)\n"
        "ds_read_b64_tr_b16 a[0:1], v4 offset:0\n"
        "ds_read_b64_tr_b16 a[2:3], v4 offset:512\n"
        "ds_read_b64_tr_b16 a[4:5], v4 offset:64\n"
        "ds_read_b64_tr_b16 a[6:7], v4 offset:576\n"
        "s_waitcnt lgkmcnt(0)\n"
        "s_add_u32 m0, s55, 0\n"
        "s_nop 0\n"
        "buffer_load_dwordx4 v1, s[44:47], s52 offen lds\n"
        "s_add_u32 s52, s52, s57\n"
        "s_add_u32 m0, s55, 1024\n"
        "s_nop 0\n"
        "buffer_load_dwordx4 v1, s[44:47], s52 offen lds\n"
        "s_add_u32 s52, s52, s57\n"
        "s_waitcnt vmcnt(34)\n"
        "ds_read_b64_tr_b16 a[8:9], v4 offset:2048\n"
        "ds_read_b64_tr_b16 a[10:11], v4 offset:2560\n"
        "ds_read_b64_tr_b16 a[12:13], v4 offset:2112\n"
        "ds_read_b64_tr_b16 a[14:15], v4 offset:2624\n"
        "s_waitcnt lgkmcnt(0)\n"
        "s_add_u32 m0, s55, 2048\n"
        "s_nop 0\n"
        "buffer_load_dwordx4 v1, s[44:47], s52 offen lds\n"
        "s_add_u32 s52, s52, s57\n"
        "s_add_u32 m0, s55, 3072\n"
        "s_nop 0\n"
        "buffer_load_dwordx4 v1, s[44:47], s52 offen lds\n"
        "s_add_u32 s52, s52, s57\n"
        "s_waitcnt vmcnt(34)\n"
        "ds_read_b64_tr_b16 a[16:17], v4 offset:4096\n"
        "ds_read_b64_tr_b16 a[18:19], v4 offset:4608\n"
        "ds_read_b64_tr_b16 a[20:21], v4 offset:4160\n"
        "ds_read_b64_tr_b16 a[22:23], v4 offset:4672\n"
        "s_waitcnt lgkmcnt(0)\n"
        "s_add_u32 m0, s55, 4096\n"
        "s_nop 0\n"
        "buffer_load_dwordx4 v1, s[44:47], s52 offen lds\n"
        "s_add_u32 s52, s52, s57\n"
        "s_add_u32 m0, s55, 5120\n"
        "s_nop 0\n"
        "buffer_load_dwordx4 v1, s[44:47], s52 offen lds\n"
        "s_add_u32 s52, s52, s57\n"
        "s_waitcnt vmcnt(34)\n"
        "ds_read_b64_tr_b16 a[24:25], v4 offset:6144\n"
        "ds_read_b64_tr_b16 a[26:27], v4 offset:6656\n"
        "ds_read_b64_tr_b16 a[28:29], v4 offset:6208\n"
        "ds_read_b64_tr_b16 a[30:31], v4 offset:6720\n"
        "s_waitcnt lgkmcnt(0)\n"
        "s_add_u32 m0, s55, 6144\n"
        "s_nop 0\n"
        "buffer_load_dwordx4 v1, s[44:47], s52 offen lds\n"
        "s_add_u32 s52, s52, s57\n"
        "s_add_u32 m0, s55, 7168\n"
        "s_nop 0\n"
        "buffer_load_dwordx4 v1, s[44:47], s52 offen lds\n"
        "s_add_u32 s52, s52, s57\n"
        "s_waitcnt vmcnt(34)\n"
        "ds_read_b64_tr_b16 a[32:33], v4 offset:36864\n"
        "ds_read_b64_tr_b16 a[34:35], v4 offset:37376\n"
        "ds_read_b64_tr_b16 a[36:37], v4 offset:36928\n"
        "ds_read_b64_tr_b16 a[38:39], v4 offset:37440\n"
        "s_waitcnt lgkmcnt(0)\n"
        "s_add_u32 m0, s55, 36864\n"
        "s_nop 0\n"
        "buffer_load_dwordx4 v1, s[44:47], s52 offen lds\n"
        "s_add_u32 s52, s52, s57\n"
        "s_add_u32 m0, s55, 37888\n"
        "s_nop 0\n"
        "buffer_load_dwordx4 v1, s[44:47], s52 offen lds\n"
        "s_add_u32 s52, s52, s57\n"
        "s_waitcnt vmcnt(34)\n"
        "ds_read_b64_tr_b16 a[40:41], v4 offset:38912\n"
        "ds_read_b64_tr_b16 a[42:43], v4 offset:39424\n"
        "ds_read_b64_tr_b16 a[44:45], v4 offset:38976\n"
        "ds_read_b64_tr_b16 a[46:47], v4 offset:39488\n"
        "s_waitcnt lgkmcnt(0)\n"
        "s_add_u32 m0, s55, 38912\n"
        "s_nop 0\n"
        "buffer_load_dwordx4 v1, s[44:47], s52 offen lds\n"
        "s_add_u32 s52, s52, s57\n"
        "s_add_u32 m0, s55, 39936\n"
        "s_nop 0\n"
        "buffer_load_dwordx4 v1, s[44:47], s52 offen lds\n"
        "s_add_u32 s52, s52, s57\n"
        "s_waitcnt vmcnt(34)\n"
        "ds_read_b64_tr_b16 a[48:49], v4 offset:40960\n"
        "ds_read_b64_tr_b16 a[50:51], v4 offset:41472\n"
        "ds_read_b64_tr_b16 a[52:53], v4 offset:41024\n"
        "ds_read_b64_tr_b16 a[54:55], v4 offset:41536\n"
        "s_waitcnt lgkmcnt(0)\n"
        "s_add_u32 m0, s55, 40960\n"
        "s_nop 0\n"
        "buffer_load_dwordx4 v1, s[44:47], s52 offen lds\n"
        "s_add_u32 s52, s52, s57\n"
        "s_add_u32 m0, s55, 41984\n"
        "s_nop 0\n"
        "buffer_load_dwordx4 v1, s[44:47], s52 offen lds\n"
        "s_add_u32 s52, s52, s57\n"
        "s_waitcnt vmcnt(34)\n"
        "ds_read_b64_tr_b16 a[56:57], v4 offset:43008\n"
        "ds_read_b64_tr_b16 a[58:59], v4 offset:43520\n"
        "ds_read_b64_tr_b16 a[60:61], v4 offset:43072\n"
        "ds_read_b64_tr_b16 a[62:63], v4 offset:43584\n"
        "s_waitcnt lgkmcnt(0)\n"
        "s_add_u32 m0, s55, 43008\n"
        "s_nop 0\n"
        "buffer_load_dwordx4 v1, s[44:47], s52 offen lds\n"
        "s_add_u32 s52, s52, s57\n"
        "s_add_u32 m0, s55, 44032\n"
        "s_nop 0\n"
        "buffer_load_dwordx4 v1, s[44:47], s52 offen lds\n"
        "s_add_u32 s52, s52, s57\n"
        "s_waitcnt vmcnt(34)\n"
        "ds_read_b64_tr_b16 a[64:65], v5 offset:0\n"
        "ds_read_b64_tr_b16 a[66:67], v5 offset:512\n"
        "ds_read_b64_tr_b16 a[68:69], v5 offset:64\n"
        "ds_read_b64_tr_b16 a[70:71], v5 offset:576\n"
        "s_waitcnt lgkmcnt(0)\n"
        "s_add_u32 m0, s55, 73728\n"
        "s_nop 0\n"
        "buffer_load_dwordx4 v1, s[44:47], s52 offen lds\n"
        "s_add_u32 s52, s52, s57\n"
        "s_add_u32 m0, s55, 74752\n"
        "s_nop 0\n"
        "buffer_load_dwordx4 v1, s[44:47], s52 offen lds\n"
        "s_add_u32 s52, s52, s57\n"
        "s_waitcnt vmcnt(34)\n"
        "ds_read_b64_tr_b16 a[72:73], v5 offset:2048\n"
        "ds_read_b64_tr_b16 a[74:75], v5 offset:2560\n"
        "ds_read_b64_tr_b16 a[76:77], v5 offset:2112\n"
        "ds_read_b64_tr_b16 a[78:79], v5 offset:2624\n"
        "s_waitcnt lgkmcnt(0)\n"
        "s_add_u32 m0, s55, 75776\n"
        "s_nop 0\n"
        "buffer_load_dwordx4 v1, s[44:47], s52 offen lds\n"
        "s_add_u32 s52, s52, s57\n"
        "s_add_u32 m0, s55, 76800\n"
        "s_nop 0\n"
        "buffer_load_dwordx4 v1, s[44:47], s52 offen lds\n"
        "s_add_u32 s52, s52, s57\n"
        "s_waitcnt vmcnt(34)\n"
        "ds_read_b64_tr_b16 a[80:81], v5 offset:4096\n"
        "ds_read_b64_tr_b16 a[82:83], v5 offset:4608\n"
        "ds_read_b64_tr_b16 a[84:85], v5 offset:4160\n"
        "ds_read_b64_tr_b16 a[86:87], v5 offset:4672\n"
        "s_waitcnt lgkmcnt(0)\n"
        "s_add_u32 m0, s55, 77824\n"
        "s_nop 0\n"
        "buffer_load_dwordx4 v1, s[44:47], s52 offen lds\n"
        "s_add_u32 s52, s52, s57\n"
        "s_add_u32 m0, s55, 78848\n"
        "s_nop 0\n"
        "buffer_load_dwordx4 v1, s[44:47], s52 offen lds\n"
        "s_add_u32 s52, s52, s57\n"
        "s_waitcnt vmcnt(34)\n"
        "ds_read_b64_tr_b16 a[88:89], v5 offset:6144\n"
        "ds_read_b64_tr_b16 a[90:91], v5 offset:6656\n"
        "ds_read_b64_tr_b16 a[92:93], v5 offset:6208\n"
        "ds_read_b64_tr_b16 a[94:95], v5 offset:6720\n"
        "s_waitcnt lgkmcnt(0)\n"
        "s_add_u32 m0, s55, 79872\n"
        "s_nop 0\n"
        "buffer_load_dwordx4 v1, s[44:47], s52 offen lds\n"
        "s_add_u32 s52, s52, s57\n"
        "s_add_u32 m0, s55, 80896\n"
        "s_nop 0\n"
        "buffer_load_dwordx4 v1, s[44:47], s52 offen lds\n"
        "s_add_u32 s52, s52, s57\n"
        "s_waitcnt vmcnt(34)\n"
        "ds_read_b64_tr_b16 a[96:97], v2 offset:0\n"
        "ds_read_b64_tr_b16 a[98:99], v2 offset:512\n"
        "ds_read_b64_tr_b16 a[100:101], v2 offset:64\n"
        "ds_read_b64_tr_b16 a[102:103], v2 offset:576\n"
        "s_waitcnt lgkmcnt(0)\n"
        "s_add_u32 m0, s56, 0\n"
        "s_nop 0\n"
        "buffer_load_dwordx4 v1, s[44:47], s52 offen lds\n"
        "s_add_u32 s52, s52, s57\n"
        "s_add_u32 m0, s56, 1024\n"
        "s_nop 0\n"
        "buffer_load_dwordx4 v1, s[44:47], s52 offen lds\n"
        "s_add_u32 s52, s52, s57\n"
        "s_waitcnt vmcnt(34)\n"
        "ds_read_b64_tr_b16 a[104:105], v2 offset:2048\n"
        "ds_read_b64_tr_b16 a[106:107], v2 offset:2560\n"
        "ds_read_b64_tr_b16 a[108:109], v2 offset:2112\n"
        "ds_read_b64_tr_b16 a[110:111], v2 offset:2624\n"
        "s_waitcnt lgkmcnt(0)\n"
        "s_add_u32 m0, s56, 2048\n"
        "s_nop 0\n"
        "buffer_load_dwordx4 v1, s[44:47], s52 offen lds\n"
        "s_add_u32 s52, s52, s57\n"
        "s_add_u32 m0, s56, 3072\n"
        "s_nop 0\n"
        "buffer_load_dwordx4 v1, s[44:47], s52 offen lds\n"
        "s_add_u32 s52, s52, s57\n"
        "s_waitcnt vmcnt(34)\n"
        "ds_read_b64_tr_b16 a[112:113], v2 offset:4096\n"
        "ds_read_b64_tr_b16 a[114:115], v2 offset:4608\n"
        "ds_read_b64_tr_b16 a[116:117], v2 offset:4160\n"
        "ds_read_b64_tr_b16 a[118:119], v2 offset:4672\n"
        "s_waitcnt lgkmcnt(0)\n"
        "s_add_u32 m0, s56, 4096\n"
        "s_nop 0\n"
        "buffer_load_dwordx4 v1, s[44:47], s52 offen lds\n"
        "s_add_u32 s52, s52, s57\n"
        "s_add_u32 m0, s56, 5120\n"
        "s_nop 0\n"
        "buffer_load_dwordx4 v1, s[44:47], s52 offen lds\n"
        "s_add_u32 s52, s52, s57\n"
        "s_waitcnt vmcnt(34)\n"
        "ds_read_b64_tr_b16 a[120:121], v2 offset:6144\n"
        "ds_read_b64_tr_b16 a[122:123], v2 offset:6656\n"
        "ds_read_b64_tr_b16 a[124:125], v2 offset:6208\n"
        "ds_read_b64_tr_b16 a[126:127], v2 offset:6720\n"
        "s_waitcnt lgkmcnt(0)\n"
        "s_add_u32 m0, s56, 6144\n"
        "s_nop 0\n"
        "buffer_load_dwordx4 v1, s[44:47], s52 offen lds\n"
        "s_add_u32 s52, s52, s57\n"
        "s_add_u32 m0, s56, 7168\n"
        "s_nop 0\n"
        "buffer_load_dwordx4 v1, s[44:47], s52 offen lds\n"
        "s_add_u32 s52, s52, s57\n"
        "s_waitcnt vmcnt(34)\n"
        "ds_read_b64_tr_b16 a[128:129], v2 offset:8192\n"
        "ds_read_b64_tr_b16 a[130:131], v2 offset:8704\n"
        "ds_read_b64_tr_b16 a[132:133], v2 offset:8256\n"
        "ds_read_b64_tr_b16 a[134:135], v2 offset:8768\n"
        "s_waitcnt lgkmcnt(0)\n"
        "s_add_u32 m0, s56, 8192\n"
        "s_nop 0\n"
        "buffer_load_dwordx4 v1, s[44:47], s52 offen lds\n"
        "s_add_u32 s52, s52, s57\n"
        "s_add_u32 m0, s56, 9216\n"
        "s_nop 0\n"
        "buffer_load_dwordx4 v1, s[44:47], s52 offen lds\n"
        "s_add_u32 s52, s52, s57\n"
        "s_waitcnt vmcnt(34)\n"
        "ds_read_b64_tr_b16 a[136:137], v2 offset:10240\n"
        "ds_read_b64_tr_b16 a[138:139], v2 offset:10752\n"
        "ds_read_b64_tr_b16 a[140:141], v2 offset:10304\n"
        "ds_read_b64_tr_b16 a[142:143], v2 offset:10816\n"
        "s_waitcnt lgkmcnt(0)\n"
        "s_add_u32 m0, s56, 10240\n"
        "s_nop 0\n"
        "buffer_load_dwordx4 v1, s[44:47], s52 offen lds\n"
        "s_add_u32 s52, s52, s57\n"
        "s_add_u32 m0, s56, 11264\n"
        "s_nop 0\n"
        "buffer_load_dwordx4 v1, s[44:47], s52 offen lds\n"
        "s_add_u32 s52, s52, s57\n"
        "s_waitcnt vmcnt(34)\n"
        "ds_read_b64_tr_b16 a[144:145], v4 offset:0\n"
        "ds_read_b64_tr_b16 a[146:147], v4 offset:512\n"
        "ds_read_b64_tr_b16 a[148:149], v4 offset:64\n"
        "ds_read_b64_tr_b16 a[150:151], v4 offset:576\n"
        "s_waitcnt lgkmcnt(0)\n"
        "s_waitcnt vmcnt(32)\n"
        "ds_read_b64_tr_b16 a[152:153], v4 offset:2048\n"
        "ds_read_b64_tr_b16 a[154:155], v4 offset:2560\n"
        "ds_read_b64_tr_b16 a[156:157], v4 offset:2112\n"
        "ds_read_b64_tr_b16 a[158:159], v4 offset:2624\n"
        "s_waitcnt lgkmcnt(0)\n"
        "s_waitcnt vmcnt(30)\n"
        "ds_read_b64_tr_b16 a[160:161], v4 offset:4096\n"
        "ds_read_b64_tr_b16 a[162:163], v4 offset:4608\n"
        "ds_read_b64_tr_b16 a[164:165], v4 offset:4160\n"
        "ds_read_b64_tr_b16 a[166:167], v4 offset:4672\n"
        "s_waitcnt lgkmcnt(0)\n"
        "s_waitcnt vmcnt(28)\n"
        "ds_read_b64_tr_b16 a[168:169], v4 offset:6144\n"
        "ds_read_b64_tr_b16 a[170:171], v4 offset:6656\n"
        "ds_read_b64_tr_b16 a[172:173], v4 offset:6208\n"
        "ds_read_b64_tr_b16 a[174:175], v4 offset:6720\n"
        "s_waitcnt lgkmcnt(0)\n"
        "s_waitcnt vmcnt(26)\n"
        "ds_read_b64_tr_b16 a[176:177], v4 offset:36864\n"
        "ds_read_b64_tr_b16 a[178:179], v4 offset:37376\n"
        "ds_read_b64_tr_b16 a[180:181], v4 offset:36928\n"
        "ds_read_b64_tr_b16 a[182:183], v4 offset:37440\n"
        "s_waitcnt lgkmcnt(0)\n"
        "s_waitcnt vmcnt(24)\n"
        "ds_read_b64_tr_b16 a[184:185], v4 offset:38912\n"
        "ds_read_b64_tr_b16 a[186:187], v4 offset:39424\n"
        "ds_read_b64_tr_b16 a[188:189], v4 offset:38976\n"
        "ds_read_b64_tr_b16 a[190:191], v4 offset:39488\n"
        "s_waitcnt lgkmcnt(0)\n"
        "s_waitcnt vmcnt(22)\n"
        "ds_read_b64_tr_b16 a[192:193], v4 offset:40960\n"
        "ds_read_b64_tr_b16 a[194:195], v4 offset:41472\n"
        "ds_read_b64_tr_b16 a[196:197], v4 offset:41024\n"
        "ds_read_b64_tr_b16 a[198:199], v4 offset:41536\n"
        "s_waitcnt lgkmcnt(0)\n"
        "s_waitcnt vmcnt(20)\n"
        "ds_read_b64_tr_b16 a[200:201], v4 offset:43008\n"
        "ds_read_b64_tr_b16 a[202:203], v4 offset:43520\n"
        "ds_read_b64_tr_b16 a[204:205], v4 offset:43072\n"
        "ds_read_b64_tr_b16 a[206:207], v4 offset:43584\n"
        "s_waitcnt lgkmcnt(0)\n"
        "s_waitcnt vmcnt(18)\n"
        "ds_read_b64_tr_b16 a[208:209], v5 offset:0\n"
        "ds_read_b64_tr_b16 a[210:211], v5 offset:512\n"
        "ds_read_b64_tr_b16 a[212:213], v5 offset:64\n"
        "ds_read_b64_tr_b16 a[214:215], v5 offset:576\n"
        "s_waitcnt lgkmcnt(0)\n"
        "s_waitcnt vmcnt(16)\n"
        "ds_read_b64_tr_b16 a[216:217], v5 offset:2048\n"
        "ds_read_b64_tr_b16 a[218:219], v5 offset:2560\n"
        "ds_read_b64_tr_b16 a[220:221], v5 offset:2112\n"
        "ds_read_b64_tr_b16 a[222:223], v5 offset:2624\n"
        "s_waitcnt lgkmcnt(0)\n"
        "s_waitcnt vmcnt(14)\n"
        "ds_read_b64_tr_b16 a[224:225], v5 offset:4096\n"
        "ds_read_b64_tr_b16 a[226:227], v5 offset:4608\n"
        "ds_read_b64_tr_b16 a[228:229], v5 offset:4160\n"
        "ds_read_b64_tr_b16 a[230:231], v5 offset:4672\n"
        "s_waitcnt lgkmcnt(0)\n"
        "s_waitcnt vmcnt(12)\n"
        "ds_read_b64_tr_b16 a[232:233], v5 offset:6144\n"
        "ds_read_b64_tr_b16 a[234:235], v5 offset:6656\n"
        "ds_read_b64_tr_b16 a[236:237], v5 offset:6208\n"
        "ds_read_b64_tr_b16 a[238:239], v5 offset:6720\n"
        "s_waitcnt lgkmcnt(0)\n"
        "s_waitcnt vmcnt(10)\n"
        "ds_read_b64_tr_b16 a[240:241], v2 offset:0\n"
        "ds_read_b64_tr_b16 a[242:243], v2 offset:512\n"
        "ds_read_b64_tr_b16 a[244:245], v2 offset:64\n"
        "ds_read_b64_tr_b16 a[246:247], v2 offset:576\n"
        "s_waitcnt lgkmcnt(0)\n"
        "s_mov_b32 s53, 0x10000\n"
        "40:\n"
        "buffer_load_dword v8, v7, s[40:43], s64 offen sc1\n"
        "s_waitcnt vmcnt(0)\n"
        "v_cmp_ne_u32 vcc, v6, v8\n"
        "s_bitcmp1_b32 vcc_lo, 26\n"
        "s_cbranch_scc1 41f\n"
        "s_bitset1_b32 vcc_lo, 26\n"
        "s_cmp_eq_u64 vcc, -1\n"
        "s_cbranch_scc1 41f\n"
        "s_sub_u32 s53, s53, 1\n"
        "s_cbranch_scc0 40b\n"
        "41:\n"
        "s_lshl2_add_u32 s52, %9, %6\n"
        "s_lshl2_add_u32 s52, s52, 0x425800\n"
        "s_memrealtime vcc\n"
        "s_waitcnt lgkmcnt(0)\n"
        "v_or_b32 v6, vcc_lo, 1\n"
        "buffer_store_dword v6, off, s[40:43], s52 sc1\n"
        "s_add_u32 m0, s55, 0\n"
        "s_add_u32 s53, s49, 0\n"
        "buffer_load_dwordx4 v120, s[40:43], s53 offen offset:0 sc1 lds\n"
        "buffer_load_dwordx4 v120, s[40:43], s53 offen offset:1024 sc1 lds\n"
        "buffer_load_dwordx4 v120, s[40:43], s53 offen offset:2048 sc1 lds\n"
        "s_waitcnt vmcnt(11)\n"
        "ds_read_b64_tr_b16 a[248:249], v2 offset:2048\n"
        "ds_read_b64_tr_b16 a[250:251], v2 offset:2560\n"
        "ds_read_b64_tr_b16 a[252:253], v2 offset:2112\n"
        "ds_read_b64_tr_b16 a[254:255], v2 offset:2624\n"
        "s_waitcnt lgkmcnt(0)\n"
        "buffer_load_dwordx4 v120, s[40:43], s53 offen offset:3072 sc1 lds\n"
        "s_add_u32 m0, s55, 4096\n"
        "s_add_u32 s53, s49, 4096\n"
        "buffer_load_dwordx4 v120, s[40:43], s53 offen offset:0 sc1 lds\n"
        "buffer_load_dwordx4 v120, s[40:43], s53 offen offset:1024 sc1 lds\n"
        "s_waitcnt vmcnt(12)\n"
        "ds_read_b64_tr_b16 v[224:225], v2 offset:4096\n"
        "ds_read_b64_tr_b16 v[226:227], v2 offset:4608\n"
        "ds_read_b64_tr_b16 v[228:229], v2 offset:4160\n"
        "ds_read_b64_tr_b16 v[230:231], v2 offset:4672\n"
        "s_waitcnt lgkmcnt(0)\n"
        "buffer_load_dwordx4 v120, s[40:43], s53 offen offset:2048 sc1 lds\n"
        "buffer_load_dwordx4 v120, s[40:43], s53 offen offset:3072 sc1 lds\n"
        "s_add_u32 m0, s55, 8192\n"
        "s_add_u32 s53, s49, 8192\n"
        "buffer_load_dwordx4 v120, s[40:43], s53 offen offset:0 sc1 lds\n"
        "s_add_u32 s49, s49, 36864\n"
        "s_add_u32 s55, s55, 36864\n"
        "s_cmp_ge_u32 s55, 110592\n"
        "s_cselect_b32 s53, 110592, 0\n"
        "s_sub_u32 s55, s55, s53\n"
        "s_waitcnt vmcnt(13)\n"
        "ds_read_b64_tr_b16 v[232:233], v2 offset:6144\n"
        "ds_read_b64_tr_b16 v[234:235], v2 offset:6656\n"
        "ds_read_b64_tr_b16 v[236:237], v2 offset:6208\n"
        "ds_read_b64_tr_b16 v[238:239], v2 offset:6720\n"
        "s_waitcnt lgkmcnt(0)\n"
        "s_add_u32 m0, s55, 0\n"
        "s_add_u32 s53, s49, 0\n"
        "buffer_load_dwordx4 v120, s[40:43], s53 offen offset:0 sc1 lds\n"
        "buffer_load_dwordx4 v120, s[40:43], s53 offen offset:1024 sc1 lds\n"
        "buffer_load_dwordx4 v120, s[40:43], s53 offen offset:2048 sc1 lds\n"
        "s_waitcnt vmcnt(14)\n"
        "ds_read_b64_tr_b16 v[240:241], v2 offset:8192\n"
        "ds_read_b64_tr_b16 v[242:243], v2 offset:8704\n"
        "ds_read_b64_tr_b16 v[244:245], v2 offset:8256\n"
        "ds_read_b64_tr_b16 v[246:247], v2 offset:8768\n"
        "s_waitcnt lgkmcnt(0)\n"
        "buffer_load_dwordx4 v120, s[40:43], s53 offen offset:3072 sc1 lds\n"
        "s_add_u32 m0, s55, 4096\n"
        "s_add_u32 s53, s49, 4096\n"
        "buffer_load_dwordx4 v120, s[40:43], s53 offen offset:0 sc1 lds\n"
        "buffer_load_dwordx4 v120, s[40:43], s53 offen offset:1024 sc1 lds\n"
        "s_waitcnt vmcnt(15)\n"
        "ds_read_b64_tr_b16 v[248:249], v2 offset:10240\n"
        "ds_read_b64_tr_b16 v[250:251], v2 offset:10752\n"
        "ds_read_b64_tr_b16 v[252:253], v2 offset:10304\n"
        "ds_read_b64_tr_b16 v[254:255], v2 offset:10816\n"
        "s_waitcnt lgkmcnt(0)\n"
        "buffer_load_dwordx4 v120, s[40:43], s53 offen offset:2048 sc1 lds\n"
        "buffer_load_dwordx4 v120, s[40:43], s53 offen offset:3072 sc1 lds\n"
        "s_add_u32 m0, s55, 8192\n"
        "s_add_u32 s53, s49, 8192\n"
        "buffer_load_dwordx4 v120, s[40:43], s53 offen offset:0 sc1 lds\n"
        "s_add_u32 s49, s49, 36864\n"
        "s_add_u32 s55, s55, 36864\n"
        "s_cmp_ge_u32 s55, 110592\n"
        "s_cselect_b32 s53, 110592, 0\n"
        "s_sub_u32 s55, s55, s53\n"
        "s_add_u32 m0, s55, 0\n"
        "s_add_u32 s53, s49, 0\n"
        "buffer_load_dwordx4 v120, s[40:43], s53 offen offset:0 sc1 lds\n"
        "buffer_load_dwordx4 v120, s[40:43], s53 offen offset:1024 sc1 lds\n"
        "s_waitcnt vmcnt(11)\n"
        "s_barrier\n"
        "v_mov_b32 v121, v120\n"
        "s_mov_b32 s54, 0\n"
        "s_mov_b32 s48, 0\n"
        "s_mov_b32 s50, 0x480000\n"
        "s_mov_b32 s51, %3\n"
        "ds_read_b128 v[64:67], v121 offset:0\n"
        "ds_read_b128 v[68:71], v121 offset:1024\n"
        "ds_read_b128 v[72:75], v121 offset:2048\n"
        "s_cmp_eq_u32 %7, 1\n"
        "s_cbranch_scc1 11f\n"
        "s_cmp_eq_u32 %7, 2\n"
        "s_cbranch_scc1 12f\n"
        "10:\n"
        "s_waitcnt lgkmcnt(2)\n"
        "v_mfma_f32_32x32x16_bf16 v[0:15], a[0:3], v[64:67], 0\n"
        "ds_read_b128 v[76:79], v121 offset:3072\n"
        "buffer_load_dwordx4 v120, s[40:43], s53 offen offset:2048 sc1 lds\n"
        "v_mfma_f32_32x32x16_bf16 v[16:31], a[4:7], v[64:67], 0\n"
        "s_waitcnt lgkmcnt(2)\n"
        "v_cvt_pk_bf16_f32 v80, v32, v36\n"
        "v_mfma_f32_32x32x16_bf16 v[0:15], a[8:11], v[68:71], v[0:15]\n"
        "ds_read_b128 v[64:67], v121 offset:4096\n"
        "v_cvt_pk_bf16_f32 v81, v33, v37\n"
        "v_mfma_f32_32x32x16_bf16 v[16:31], a[12:15], v[68:71], v[16:31]\n"
        "s_waitcnt lgkmcnt(2)\n"
        "v_cvt_pk_bf16_f32 v82, v34, v38\n"
        "v_mfma_f32_32x32x16_bf16 v[0:15], a[16:19], v[72:75], v[0:15]\n"
        "ds_read_b128 v[68:71], v121 offset:5120\n"
        "v_cvt_pk_bf16_f32 v83, v35, v39\n"
        "v_mfma_f32_32x32x16_bf16 v[16:31], a[20:23], v[72:75], v[16:31]\n"
        "buffer_store_dwordx4 v[80:83], v120, s[40:43], s50 offen offset:0 sc1\n"
        "s_waitcnt lgkmcnt(2)\n"
        "v_mfma_f32_32x32x16_bf16 v[0:15], a[24:27], v[76:79], v[0:15]\n"
        "ds_read_b128 v[72:75], v121 offset:6144\n"
        "v_cvt_pk_bf16_f32 v84, v40, v44\n"
        "v_mfma_f32_32x32x16_bf16 v[16:31], a[28:31], v[76:79], v[16:31]\n"
        "s_waitcnt lgkmcnt(2)\n"
        "v_cvt_pk_bf16_f32 v85, v41, v45\n"
        "v_mfma_f32_32x32x16_bf16 v[0:15], a[32:35], v[64:67], v[0:15]\n"
        "ds_read_b128 v[76:79], v121 offset:7168\n"
        "v_cvt_pk_bf16_f32 v86, v42, v46\n"
        "v_mfma_f32_32x32x16_bf16 v[16:31], a[36:39], v[64:67], v[16:31]\n"
        "s_waitcnt lgkmcnt(2)\n"
        "v_cvt_pk_bf16_f32 v87, v43, v47\n"
        "v_mfma_f32_32x32x16_bf16 v[0:15], a[40:43], v[68:71], v[0:15]\n"
        "ds_read_b128 v[64:67], v121 offset:8192\n"
        "buffer_store_dwordx4 v[84:87], v120, s[40:43], s50 offen offset:1024 sc1\n"
        "v_mfma_f32_32x32x16_bf16 v[16:31], a[44:47], v[68:71], v[16:31]\n"
        "s_waitcnt lgkmcnt(2)\n"
        "v_cvt_pk_bf16_f32 v88, v48, v52\n"
        "v_mfma_f32_32x32x16_bf16 v[0:15], a[48:51], v[72:75], v[0:15]\n"
        "ds_read_b128 v[68:71], v121 offset:9216\n"
        "v_cvt_pk_bf16_f32 v89, v49, v53\n"
        "v_mfma_f32_32x32x16_bf16 v[16:31], a[52:55], v[72:75], v[16:31]\n"
        "s_waitcnt lgkmcnt(2)\n"
        "v_cvt_pk_bf16_f32 v90, v50, v54\n"
        "v_mfma_f32_32x32x16_bf16 v[0:15], a[56:59], v[76:79], v[0:15]\n"
        "ds_read_b128 v[72:75], v121 offset:10240\n"
        "v_cvt_pk_bf16_f32 v91, v51, v55\n"
        "v_mfma_f32_32x32x16_bf16 v[16:31], a[60:63], v[76:79], v[16:31]\n"
        "buffer_store_dwordx4 v[88:91], v120, s[40:43], s50 offen offset:2048 sc1\n"
        "s_waitcnt lgkmcnt(2)\n"
        "v_mfma_f32_32x32x16_bf16 v[0:15], a[64:67], v[64:67], v[0:15]\n"
        "ds_read_b128 v[76:79], v121 offset:11264\n"
        "v_cvt_pk_bf16_f32 v92, v56, v60\n"
        "v_mfma_f32_32x32x16_bf16 v[16:31], a[68:71], v[64:67], v[16:31]\n"
        "s_add_u32 s54, s54, 36864\n"
        "v_cvt_pk_bf16_f32 v93, v57, v61\n"
        "s_waitcnt lgkmcnt(2)\n"
        "v_mfma_f32_32x32x16_bf16 v[0:15], a[72:75], v[68:71], v[0:15]\n"
        "ds_read_b128 v[64:67], v121 offset:12288\n"
        "v_cvt_pk_bf16_f32 v94, v58, v62\n"
        "v_mfma_f32_32x32x16_bf16 v[16:31], a[76:79], v[68:71], v[16:31]\n"
        "s_cmp_ge_u32 s54, 110592\n"
        "v_cvt_pk_bf16_f32 v95, v59, v63\n"
        "s_waitcnt lgkmcnt(2)\n"
        "v_mfma_f32_32x32x16_bf16 v[0:15], a[80:83], v[72:75], v[0:15]\n"
        "ds_read_b128 v[68:71], v121 offset:13312\n"
        "buffer_store_dwordx4 v[92:95], v120, s[40:43], s50 offen offset:3072 sc1\n"
        "v_mfma_f32_32x32x16_bf16 v[16:31], a[84:87], v[72:75], v[16:31]\n"
        "s_cselect_b32 s52, 110592, 0\n"
        "s_waitcnt lgkmcnt(2)\n"
        "v_mfma_f32_32x32x16_bf16 v[0:15], a[88:91], v[76:79], v[0:15]\n"
        "ds_read_b128 v[72:75], v121 offset:14336\n"
        "v_mfma_f32_32x32x16_bf16 v[16:31], a[92:95], v[76:79], v[16:31]\n"
        "s_sub_u32 s54, s54, s52\n"
        "s_waitcnt lgkmcnt(2)\n"
        "v_mfma_f32_32x32x16_bf16 v[0:15], a[96:99], v[64:67], v[0:15]\n"
        "ds_read_b128 v[76:79], v121 offset:15360\n"
        "v_mfma_f32_32x32x16_bf16 v[16:31], a[100:103], v[64:67], v[16:31]\n"
        "s_waitcnt lgkmcnt(2)\n"
        "v_mfma_f32_32x32x16_bf16 v[0:15], a[104:107], v[68:71], v[0:15]\n"
        "ds_read_b128 v[64:67], v121 offset:16384\n"
        "buffer_load_dwordx4 v120, s[40:43], s53 offen offset:3072 sc1 lds\n"
        "v_mfma_f32_32x32x16_bf16 v[16:31], a[108:111], v[68:71], v[16:31]\n"
        "s_waitcnt lgkmcnt(2)\n"
        "v_mfma_f32_32x32x16_bf16 v[0:15], a[112:115], v[72:75], v[0:15]\n"
        "ds_read_b128 v[68:71], v121 offset:17408\n"
        "v_mfma_f32_32x32x16_bf16 v[16:31], a[116:119], v[72:75], v[16:31]\n"
        "s_waitcnt lgkmcnt(2)\n"
        "v_mfma_f32_32x32x16_bf16 v[0:15], a[120:123], v[76:79], v[0:15]\n"
        "ds_read_b128 v[72:75], v121 offset:18432\n"
        "v_mfma_f32_32x32x16_bf16 v[16:31], a[124:127], v[76:79], v[16:31]\n"
        "s_waitcnt lgkmcnt(2)\n"
        "v_mfma_f32_32x32x16_bf16 v[0:15], a[128:131], v[64:67], v[0:15]\n"
        "ds_read_b128 v[76:79], v121 offset:19456\n"
        "s_add_u32 m0, s55, 4096\n"
        "s_add_u32 s53, s49, 4096\n"
        "buffer_load_dwordx4 v120, s[40:43], s53 offen offset:0 sc1 lds\n"
        "v_mfma_f32_32x32x16_bf16 v[16:31], a[132:135], v[64:67], v[16:31]\n"
        "s_waitcnt lgkmcnt(2)\n"
        "v_mfma_f32_32x32x16_bf16 v[0:15], a[136:139], v[68:71], v[0:15]\n"
        "ds_read_b128 v[64:67], v121 offset:20480\n"
        "v_mfma_f32_32x32x16_bf16 v[16:31], a[140:143], v[68:71], v[16:31]\n"
        "s_waitcnt lgkmcnt(2)\n"
        "v_mfma_f32_32x32x16_bf16 v[0:15], a[144:147], v[72:75], v[0:15]\n"
        "ds_read_b128 v[68:71], v121 offset:21504\n"
        "v_mfma_f32_32x32x16_bf16 v[16:31], a[148:151], v[72:75], v[16:31]\n"
        "s_waitcnt lgkmcnt(2)\n"
        "v_mfma_f32_32x32x16_bf16 v[0:15], a[152:155], v[76:79], v[0:15]\n"
        "ds_read_b128 v[72:75], v121 offset:22528\n"
        "buffer_load_dwordx4 v120, s[40:43], s53 offen offset:1024 sc1 lds\n"
        "v_mfma_f32_32x32x16_bf16 v[16:31], a[156:159], v[76:79], v[16:31]\n"
        "s_waitcnt lgkmcnt(2)\n"
        "v_mfma_f32_32x32x16_bf16 v[0:15], a[160:163], v[64:67], v[0:15]\n"
        "ds_read_b128 v[76:79], v121 offset:23552\n"
        "v_mfma_f32_32x32x16_bf16 v[16:31], a[164:167], v[64:67], v[16:31]\n"
        "s_waitcnt lgkmcnt(2)\n"
        "v_mfma_f32_32x32x16_bf16 v[0:15], a[168:171], v[68:71], v[0:15]\n"
        "ds_read_b128 v[64:67], v121 offset:24576\n"
        "v_mfma_f32_32x32x16_bf16 v[16:31], a[172:175], v[68:71], v[16:31]\n"
        "s_waitcnt lgkmcnt(2)\n"
        "v_mfma_f32_32x32x16_bf16 v[0:15], a[176:179], v[72:75], v[0:15]\n"
        "ds_read_b128 v[68:71], v121 offset:25600\n"
        "buffer_load_dwordx4 v120, s[40:43], s53 offen offset:2048 sc1 lds\n"
        "v_mfma_f32_32x32x16_bf16 v[16:31], a[180:183], v[72:75], v[16:31]\n"
        "s_waitcnt lgkmcnt(2)\n"
        "v_mfma_f32_32x32x16_bf16 v[0:15], a[184:187], v[76:79], v[0:15]\n"
        "ds_read_b128 v[72:75], v121 offset:26624\n"
        "v_mfma_f32_32x32x16_bf16 v[16:31], a[188:191], v[76:79], v[16:31]\n"
        "s_sub_u32 s52, s48, 0\n"
        "s_waitcnt lgkmcnt(2)\n"
        "v_mfma_f32_32x32x16_bf16 v[0:15], a[192:195], v[64:67], v[0:15]\n"
        "ds_read_b128 v[76:79], v121 offset:27648\n"
        "v_mfma_f32_32x32x16_bf16 v[16:31], a[196:199], v[64:67], v[16:31]\n"
        "s_max_i32 s52, s52, 0\n"
        "s_waitcnt lgkmcnt(2)\n"
        "v_mfma_f32_32x32x16_bf16 v[0:15], a[200:203], v[68:71], v[0:15]\n"
        "ds_read_b128 v[64:67], v121 offset:28672\n"
        "buffer_load_dwordx4 v120, s[40:43], s53 offen offset:3072 sc1 lds\n"
        "v_mfma_f32_32x32x16_bf16 v[16:31], a[204:207], v[68:71], v[16:31]\n"
        "s_waitcnt lgkmcnt(2)\n"
        "v_mfma_f32_32x32x16_bf16 v[0:15], a[208:211], v[72:75], v[0:15]\n"
        "ds_read_b128 v[68:71], v121 offset:29696\n"
        "v_mfma_f32_32x32x16_bf16 v[16:31], a[212:215], v[72:75], v[16:31]\n"
        "s_waitcnt lgkmcnt(2)\n"
        "v_mfma_f32_32x32x16_bf16 v[0:15], a[216:219], v[76:79], v[0:15]\n"
        "ds_read_b128 v[72:75], v121 offset:30720\n"
        "s_add_u32 m0, s55, 8192\n"
        "s_add_u32 s53, s49, 8192\n"
        "buffer_load_dwordx4 v120, s[40:43], s53 offen offset:0 sc1 lds\n"
        "v_mfma_f32_32x32x16_bf16 v[16:31], a[220:223], v[76:79], v[16:31]\n"
        "s_waitcnt lgkmcnt(2)\n"
        "v_mfma_f32_32x32x16_bf16 v[0:15], a[224:227], v[64:67], v[0:15]\n"
        "ds_read_b128 v[76:79], v121 offset:31744\n"
        "v_mfma_f32_32x32x16_bf16 v[16:31], a[228:231], v[64:67], v[16:31]\n"
        "s_add_u32 s49, s49, 36864\n"
        "s_waitcnt lgkmcnt(2)\n"
        "v_mfma_f32_32x32x16_bf16 v[0:15], a[232:235], v[68:71], v[0:15]\n"
        "ds_read_b128 v[64:67], v121 offset:32768\n"
        "v_mfma_f32_32x32x16_bf16 v[16:31], a[236:239], v[68:71], v[16:31]\n"
        "s_add_u32 s55, s55, 36864\n"
        "s_waitcnt lgkmcnt(2)\n"
        "v_mfma_f32_32x32x16_bf16 v[0:15], a[240:243], v[72:75], v[0:15]\n"
        "ds_read_b128 v[68:71], v121 offset:33792\n"
        "v_mfma_f32_32x32x16_bf16 v[16:31], a[244:247], v[72:75], v[16:31]\n"
        "s_cmp_ge_u32 s55, 110592\n"
        "s_waitcnt lgkmcnt(2)\n"
        "v_mfma_f32_32x32x16_bf16 v[0:15], a[248:251], v[76:79], v[0:15]\n"
        "ds_read_b128 v[72:75], v121 offset:34816\n"
        "v_mfma_f32_32x32x16_bf16 v[16:31], a[252:255], v[76:79], v[16:31]\n"
        "s_cselect_b32 s53, 110592, 0\n"
        "s_waitcnt lgkmcnt(2)\n"
        "v_mfma_f32_32x32x16_bf16 v[0:15], v[224:227], v[64:67], v[0:15]\n"
        "ds_read_b128 v[76:79], v121 offset:35840\n"
        "v_mfma_f32_32x32x16_bf16 v[16:31], v[228:231], v[64:67], v[16:31]\n"
        "s_sub_u32 s55, s55, s53\n"
        "s_waitcnt lgkmcnt(2)\n"
        "v_mfma_f32_32x32x16_bf16 v[0:15], v[232:235], v[68:71], v[0:15]\n"
        "v_mov_b32 v122, s52\n"
        "v_add_u32 v123, v124, v123\n"
        "v_add_u32 v121, s54, v120\n"
        "s_mov_b32 s50, s51\n"
        "s_add_u32 s51, s51, %4\n"
        "s_add_u32 s48, s48, 1\n"
        "s_cmp_ge_u32 s48, 3\n"
        "v_mfma_f32_32x32x16_bf16 v[16:31], v[236:239], v[68:71], v[16:31]\n"
        "s_cselect_b64 exec, 7, 1\n"
        "s_waitcnt vmcnt(6) lgkmcnt(0)\n"
        "buffer_store_dword v122, v123, s[40:43], 0 offen sc1\n"
        "s_mov_b64 exec, -1\n"
        "s_barrier\n"
        "v_mfma_f32_32x32x16_bf16 v[0:15], v[240:243], v[72:75], v[0:15]\n"
        "ds_read_b128 v[64:67], v121 offset:0\n"
        "s_add_u32 m0, s55, 0\n"
        "s_add_u32 s53, s49, 0\n"
        "buffer_load_dwordx4 v120, s[40:43], s53 offen offset:0 sc1 lds\n"
        "v_mfma_f32_32x32x16_bf16 v[16:31], v[244:247], v[72:75], v[16:31]\n"
        "ds_read_b128 v[68:71], v121 offset:1024\n"
        "v_mfma_f32_32x32x16_bf16 v[0:15], v[248:251], v[76:79], v[0:15]\n"
        "ds_read_b128 v[72:75], v121 offset:2048\n"
        "buffer_load_dwordx4 v120, s[40:43], s53 offen offset:1024 sc1 lds\n"
        "v_mfma_f32_32x32x16_bf16 v[16:31], v[252:255], v[76:79], v[16:31]\n"
        "s_cmp_eq_u32 s48, 65\n"
        "s_cbranch_scc1 20f\n"
        "s_waitcnt lgkmcnt(2)\n"
        "v_mfma_f32_32x32x16_bf16 v[32:47], a[0:3], v[64:67], 0\n"
        "ds_read_b128 v[76:79], v121 offset:3072\n"
        "buffer_load_dwordx4 v120, s[40:43], s53 offen offset:2048 sc1 lds\n"
        "v_mfma_f32_32x32x16_bf16 v[48:63], a[4:7], v[64:67], 0\n"
        "s_waitcnt lgkmcnt(2)\n"
        "v_cvt_pk_bf16_f32 v80, v0, v4\n"
        "v_mfma_f32_32x32x16_bf16 v[32:47], a[8:11], v[68:71], v[32:47]\n"
        "ds_read_b128 v[64:67], v121 offset:4096\n"
        "v_cvt_pk_bf16_f32 v81, v1, v5\n"
        "v_mfma_f32_32x32x16_bf16 v[48:63], a[12:15], v[68:71], v[48:63]\n"
        "s_waitcnt lgkmcnt(2)\n"
        "v_cvt_pk_bf16_f32 v82, v2, v6\n"
        "v_mfma_f32_32x32x16_bf16 v[32:47], a[16:19], v[72:75], v[32:47]\n"
        "ds_read_b128 v[68:71], v121 offset:5120\n"
        "v_cvt_pk_bf16_f32 v83, v3, v7\n"
        "v_mfma_f32_32x32x16_bf16 v[48:63], a[20:23], v[72:75], v[48:63]\n"
        "buffer_store_dwordx4 v[80:83], v120, s[40:43], s50 offen offset:0 sc1\n"
        "s_waitcnt lgkmcnt(2)\n"
        "v_mfma_f32_32x32x16_bf16 v[32:47], a[24:27], v[76:79], v[32:47]\n"
        "ds_read_b128 v[72:75], v121 offset:6144\n"
        "v_cvt_pk_bf16_f32 v84, v8, v12\n"
        "v_mfma_f32_32x32x16_bf16 v[48:63], a[28:31], v[76:79], v[48:63]\n"
        "s_waitcnt lgkmcnt(2)\n"
        "v_cvt_pk_bf16_f32 v85, v9, v13\n"
        "v_mfma_f32_32x32x16_bf16 v[32:47], a[32:35], v[64:67], v[32:47]\n"
        "ds_read_b128 v[76:79], v121 offset:7168\n"
        "v_cvt_pk_bf16_f32 v86, v10, v14\n"
        "v_mfma_f32_32x32x16_bf16 v[48:63], a[36:39], v[64:67], v[48:63]\n"
        "s_waitcnt lgkmcnt(2)\n"
        "v_cvt_pk_bf16_f32 v87, v11, v15\n"
        "v_mfma_f32_32x32x16_bf16 v[32:47], a[40:43], v[68:71], v[32:47]\n"
        "ds_read_b128 v[64:67], v121 offset:8192\n"
        "buffer_store_dwordx4 v[84:87], v120, s[40:43], s50 offen offset:1024 sc1\n"
        "v_mfma_f32_32x32x16_bf16 v[48:63], a[44:47], v[68:71], v[48:63]\n"
        "s_waitcnt lgkmcnt(2)\n"
        "v_cvt_pk_bf16_f32 v88, v16, v20\n"
        "v_mfma_f32_32x32x16_bf16 v[32:47], a[48:51], v[72:75], v[32:47]\n"
        "ds_read_b128 v[68:71], v121 offset:9216\n"
        "v_cvt_pk_bf16_f32 v89, v17, v21\n"
        "v_mfma_f32_32x32x16_bf16 v[48:63], a[52:55], v[72:75], v[48:63]\n"
        "s_waitcnt lgkmcnt(2)\n"
        "v_cvt_pk_bf16_f32 v90, v18, v22\n"
        "v_mfma_f32_32x32x16_bf16 v[32:47], a[56:59], v[76:79], v[32:47]\n"
        "ds_read_b128 v[72:75], v121 offset:10240\n"
        "v_cvt_pk_bf16_f32 v91, v19, v23\n"
        "v_mfma_f32_32x32x16_bf16 v[48:63], a[60:63], v[76:79], v[48:63]\n"
        "buffer_store_dwordx4 v[88:91], v120, s[40:43], s50 offen offset:2048 sc1\n"
        "s_waitcnt lgkmcnt(2)\n"
        "v_mfma_f32_32x32x16_bf16 v[32:47], a[64:67], v[64:67], v[32:47]\n"
        "ds_read_b128 v[76:79], v121 offset:11264\n"
        "v_cvt_pk_bf16_f32 v92, v24, v28\n"
        "v_mfma_f32_32x32x16_bf16 v[48:63], a[68:71], v[64:67], v[48:63]\n"
        "s_add_u32 s54, s54, 36864\n"
        "v_cvt_pk_bf16_f32 v93, v25, v29\n"
        "s_waitcnt lgkmcnt(2)\n"
        "v_mfma_f32_32x32x16_bf16 v[32:47], a[72:75], v[68:71], v[32:47]\n"
        "ds_read_b128 v[64:67], v121 offset:12288\n"
        "v_cvt_pk_bf16_f32 v94, v26, v30\n"
        "v_mfma_f32_32x32x16_bf16 v[48:63], a[76:79], v[68:71], v[48:63]\n"
        "s_cmp_ge_u32 s54, 110592\n"
        "v_cvt_pk_bf16_f32 v95, v27, v31\n"
        "s_waitcnt lgkmcnt(2)\n"
        "v_mfma_f32_32x32x16_bf16 v[32:47], a[80:83], v[72:75], v[32:47]\n"
        "ds_read_b128 v[68:71], v121 offset:13312\n"
        "buffer_store_dwordx4 v[92:95], v120, s[40:43], s50 offen offset:3072 sc1\n"
        "v_mfma_f32_32x32x16_bf16 v[48:63], a[84:87], v[72:75], v[48:63]\n"
        "s_cselect_b32 s52, 110592, 0\n"
        "s_waitcnt lgkmcnt(2)\n"
        "v_mfma_f32_32x32x16_bf16 v[32:47], a[88:91], v[76:79], v[32:47]\n"
        "ds_read_b128 v[72:75], v121 offset:14336\n"
        "v_mfma_f32_32x32x16_bf16 v[48:63], a[92:95], v[76:79], v[48:63]\n"
        "s_sub_u32 s54, s54, s52\n"
        "s_waitcnt lgkmcnt(2)\n"
        "v_mfma_f32_32x32x16_bf16 v[32:47], a[96:99], v[64:67], v[32:47]\n"
        "ds_read_b128 v[76:79], v121 offset:15360\n"
        "v_mfma_f32_32x32x16_bf16 v[48:63], a[100:103], v[64:67], v[48:63]\n"
        "s_waitcnt lgkmcnt(2)\n"
        "v_mfma_f32_32x32x16_bf16 v[32:47], a[104:107], v[68:71], v[32:47]\n"
        "ds_read_b128 v[64:67], v121 offset:16384\n"
        "buffer_load_dwordx4 v120, s[40:43], s53 offen offset:3072 sc1 lds\n"
        "v_mfma_f32_32x32x16_bf16 v[48:63], a[108:111], v[68:71], v[48:63]\n"
        "s_waitcnt lgkmcnt(2)\n"
        "v_mfma_f32_32x32x16_bf16 v[32:47], a[112:115], v[72:75], v[32:47]\n"
        "ds_read_b128 v[68:71], v121 offset:17408\n"
        "v_mfma_f32_32x32x16_bf16 v[48:63], a[116:119], v[72:75], v[48:63]\n"
        "s_waitcnt lgkmcnt(2)\n"
        "v_mfma_f32_32x32x16_bf16 v[32:47], a[120:123], v[76:79], v[32:47]\n"
        "ds_read_b128 v[72:75], v121 offset:18432\n"
        "v_mfma_f32_32x32x16_bf16 v[48:63], a[124:127], v[76:79], v[48:63]\n"
        "s_waitcnt lgkmcnt(2)\n"
        "v_mfma_f32_32x32x16_bf16 v[32:47], a[128:131], v[64:67], v[32:47]\n"
        "ds_read_b128 v[76:79], v121 offset:19456\n"
        "s_add_u32 m0, s55, 4096\n"
        "s_add_u32 s53, s49, 4096\n"
        "buffer_load_dwordx4 v120, s[40:43], s53 offen offset:0 sc1 lds\n"
        "v_mfma_f32_32x32x16_bf16 v[48:63], a[132:135], v[64:67], v[48:63]\n"
        "s_waitcnt lgkmcnt(2)\n"
        "v_mfma_f32_32x32x16_bf16 v[32:47], a[136:139], v[68:71], v[32:47]\n"
        "ds_read_b128 v[64:67], v121 offset:20480\n"
        "v_mfma_f32_32x32x16_bf16 v[48:63], a[140:143], v[68:71], v[48:63]\n"
        "s_waitcnt lgkmcnt(2)\n"
        "v_mfma_f32_32x32x16_bf16 v[32:47], a[144:147], v[72:75], v[32:47]\n"
        "ds_read_b128 v[68:71], v121 offset:21504\n"
        "v_mfma_f32_32x32x16_bf16 v[48:63], a[148:151], v[72:75], v[48:63]\n"
        "s_waitcnt lgkmcnt(2)\n"
        "v_mfma_f32_32x32x16_bf16 v[32:47], a[152:155], v[76:79], v[32:47]\n"
        "ds_read_b128 v[72:75], v121 offset:22528\n"
        "buffer_load_dwordx4 v120, s[40:43], s53 offen offset:1024 sc1 lds\n"
        "v_mfma_f32_32x32x16_bf16 v[48:63], a[156:159], v[76:79], v[48:63]\n"
        "s_waitcnt lgkmcnt(2)\n"
        "v_mfma_f32_32x32x16_bf16 v[32:47], a[160:163], v[64:67], v[32:47]\n"
        "ds_read_b128 v[76:79], v121 offset:23552\n"
        "v_mfma_f32_32x32x16_bf16 v[48:63], a[164:167], v[64:67], v[48:63]\n"
        "s_waitcnt lgkmcnt(2)\n"
        "v_mfma_f32_32x32x16_bf16 v[32:47], a[168:171], v[68:71], v[32:47]\n"
        "ds_read_b128 v[64:67], v121 offset:24576\n"
        "v_mfma_f32_32x32x16_bf16 v[48:63], a[172:175], v[68:71], v[48:63]\n"
        "s_waitcnt lgkmcnt(2)\n"
        "v_mfma_f32_32x32x16_bf16 v[32:47], a[176:179], v[72:75], v[32:47]\n"
        "ds_read_b128 v[68:71], v121 offset:25600\n"
        "buffer_load_dwordx4 v120, s[40:43], s53 offen offset:2048 sc1 lds\n"
        "v_mfma_f32_32x32x16_bf16 v[48:63], a[180:183], v[72:75], v[48:63]\n"
        "s_waitcnt lgkmcnt(2)\n"
        "v_mfma_f32_32x32x16_bf16 v[32:47], a[184:187], v[76:79], v[32:47]\n"
        "ds_read_b128 v[72:75], v121 offset:26624\n"
        "v_mfma_f32_32x32x16_bf16 v[48:63], a[188:191], v[76:79], v[48:63]\n"
        "s_sub_u32 s52, s48, 0\n"
        "s_waitcnt lgkmcnt(2)\n"
        "v_mfma_f32_32x32x16_bf16 v[32:47], a[192:195], v[64:67], v[32:47]\n"
        "ds_read_b128 v[76:79], v121 offset:27648\n"
        "v_mfma_f32_32x32x16_bf16 v[48:63], a[196:199], v[64:67], v[48:63]\n"
        "s_max_i32 s52, s52, 0\n"
        "s_waitcnt lgkmcnt(2)\n"
        "v_mfma_f32_32x32x16_bf16 v[32:47], a[200:203], v[68:71], v[32:47]\n"
        "ds_read_b128 v[64:67], v121 offset:28672\n"
        "buffer_load_dwordx4 v120, s[40:43], s53 offen offset:3072 sc1 lds\n"
        "v_mfma_f32_32x32x16_bf16 v[48:63], a[204:207], v[68:71], v[48:63]\n"
        "s_waitcnt lgkmcnt(2)\n"
        "v_mfma_f32_32x32x16_bf16 v[32:47], a[208:211], v[72:75], v[32:47]\n"
        "ds_read_b128 v[68:71], v121 offset:29696\n"
        "v_mfma_f32_32x32x16_bf16 v[48:63], a[212:215], v[72:75], v[48:63]\n"
        "s_waitcnt lgkmcnt(2)\n"
        "v_mfma_f32_32x32x16_bf16 v[32:47], a[216:219], v[76:79], v[32:47]\n"
        "ds_read_b128 v[72:75], v121 offset:30720\n"
        "s_add_u32 m0, s55, 8192\n"
        "s_add_u32 s53, s49, 8192\n"
        "buffer_load_dwordx4 v120, s[40:43], s53 offen offset:0 sc1 lds\n"
        "v_mfma_f32_32x32x16_bf16 v[48:63], a[220:223], v[76:79], v[48:63]\n"
        "s_waitcnt lgkmcnt(2)\n"
        "v_mfma_f32_32x32x16_bf16 v[32:47], a[224:227], v[64:67], v[32:47]\n"
        "ds_read_b128 v[76:79], v121 offset:31744\n"
        "v_mfma_f32_32x32x16_bf16 v[48:63], a[228:231], v[64:67], v[48:63]\n"
        "s_add_u32 s49, s49, 36864\n"
        "s_waitcnt lgkmcnt(2)\n"
        "v_mfma_f32_32x32x16_bf16 v[32:47], a[232:235], v[68:71], v[32:47]\n"
        "ds_read_b128 v[64:67], v121 offset:32768\n"
        "v_mfma_f32_32x32x16_bf16 v[48:63], a[236:239], v[68:71], v[48:63]\n"
        "s_add_u32 s55, s55, 36864\n"
        "s_waitcnt lgkmcnt(2)\n"
        "v_mfma_f32_32x32x16_bf16 v[32:47], a[240:243], v[72:75], v[32:47]\n"
        "ds_read_b128 v[68:71], v121 offset:33792\n"
        "v_mfma_f32_32x32x16_bf16 v[48:63], a[244:247], v[72:75], v[48:63]\n"
        "s_cmp_ge_u32 s55, 110592\n"
        "s_waitcnt lgkmcnt(2)\n"
        "v_mfma_f32_32x32x16_bf16 v[32:47], a[248:251], v[76:79], v[32:47]\n"
        "ds_read_b128 v[72:75], v121 offset:34816\n"
        "v_mfma_f32_32x32x16_bf16 v[48:63], a[252:255], v[76:79], v[48:63]\n"
        "s_cselect_b32 s53, 110592, 0\n"
        "s_waitcnt lgkmcnt(2)\n"
        "v_mfma_f32_32x32x16_bf16 v[32:47], v[224:227], v[64:67], v[32:47]\n"
        "ds_read_b128 v[76:79], v121 offset:35840\n"
        "v_mfma_f32_32x32x16_bf16 v[48:63], v[228:231], v[64:67], v[48:63]\n"
        "s_sub_u32 s55, s55, s53\n"
        "s_waitcnt lgkmcnt(2)\n"
        "v_mfma_f32_32x32x16_bf16 v[32:47], v[232:235], v[68:71], v[32:47]\n"
        "v_mov_b32 v122, s52\n"
        "v_add_u32 v123, v124, v123\n"
        "v_add_u32 v121, s54, v120\n"
        "s_mov_b32 s50, s51\n"
        "s_add_u32 s51, s51, %4\n"
        "s_add_u32 s48, s48, 1\n"
        "s_cmp_ge_u32 s48, 3\n"
        "v_mfma_f32_32x32x16_bf16 v[48:63], v[236:239], v[68:71], v[48:63]\n"
        "s_cselect_b64 exec, 7, 1\n"
        "s_waitcnt vmcnt(6) lgkmcnt(0)\n"
        "buffer_store_dword v122, v123, s[40:43], 0 offen sc1\n"
        "s_mov_b64 exec, -1\n"
        "s_barrier\n"
        "v_mfma_f32_32x32x16_bf16 v[32:47], v[240:243], v[72:75], v[32:47]\n"
        "ds_read_b128 v[64:67], v121 offset:0\n"
        "s_add_u32 m0, s55, 0\n"
        "s_add_u32 s53, s49, 0\n"
        "buffer_load_dwordx4 v120, s[40:43], s53 offen offset:0 sc1 lds\n"
        "v_mfma_f32_32x32x16_bf16 v[48:63], v[244:247], v[72:75], v[48:63]\n"
        "ds_read_b128 v[68:71], v121 offset:1024\n"
        "v_mfma_f32_32x32x16_bf16 v[32:47], v[248:251], v[76:79], v[32:47]\n"
        "ds_read_b128 v[72:75], v121 offset:2048\n"
        "buffer_load_dwordx4 v120, s[40:43], s53 offen offset:1024 sc1 lds\n"
        "v_mfma_f32_32x32x16_bf16 v[48:63], v[252:255], v[76:79], v[48:63]\n"
        "s_cmp_eq_u32 s48, 65\n"
        "s_cmp_eq_u32 s48, 12\n"
        "s_cbranch_scc0 10b\n"
        "s_lshl4_add_u32 s52, %9, 0x426800\n"
        "s_mov_b32 s64, 0x10000\n"
        "30:\n"
        "buffer_load_dword v122, off, s[40:43], s52 sc1\n"
        "s_waitcnt vmcnt(0)\n"
        "v_cmp_eq_u32 vcc, 0, v122\n"
        "s_cbranch_vccz 10b\n"
        "s_sub_u32 s64, s64, 1\n"
        "s_cbranch_scc0 30b\n"
        "s_branch 10b\n"
        "11:\n"
        "s_waitcnt lgkmcnt(2)\n"
        "v_mfma_f32_32x32x16_bf16 v[0:15], v[64:67], a[0:3], 0\n"
        "ds_read_b128 v[76:79], v121 offset:3072\n"
        "buffer_load_dwordx4 v120, s[40:43], s53 offen offset:2048 sc1 lds\n"
        "v_mfma_f32_32x32x16_bf16 v[16:31], v[64:67], a[4:7], 0\n"
        "s_waitcnt lgkmcnt(2)\n"
        "v_cvt_pk_bf16_f32 v80, v32, v33\n"
        "v_mfma_f32_32x32x16_bf16 v[0:15], v[68:71], a[8:11], v[0:15]\n"
        "ds_read_b128 v[64:67], v121 offset:4096\n"
        "v_cvt_pk_bf16_f32 v81, v34, v35\n"
        "v_mfma_f32_32x32x16_bf16 v[16:31], v[68:71], a[12:15], v[16:31]\n"
        "s_waitcnt lgkmcnt(2)\n"
        "v_cvt_pk_bf16_f32 v82, v36, v37\n"
        "v_mfma_f32_32x32x16_bf16 v[0:15], v[72:75], a[16:19], v[0:15]\n"
        "ds_read_b128 v[68:71], v121 offset:5120\n"
        "v_cvt_pk_bf16_f32 v83, v38, v39\n"
        "v_mfma_f32_32x32x16_bf16 v[16:31], v[72:75], a[20:23], v[16:31]\n"
        "buffer_store_dwordx4 v[80:83], v120, s[40:43], s50 offen offset:0 sc1\n"
        "s_waitcnt lgkmcnt(2)\n"
        "v_mfma_f32_32x32x16_bf16 v[0:15], v[76:79], a[24:27], v[0:15]\n"
        "ds_read_b128 v[72:75], v121 offset:6144\n"
        "v_cvt_pk_bf16_f32 v84, v40, v41\n"
        "v_mfma_f32_32x32x16_bf16 v[16:31], v[76:79], a[28:31], v[16:31]\n"
        "s_waitcnt lgkmcnt(2)\n"
        "v_cvt_pk_bf16_f32 v85, v42, v43\n"
        "v_mfma_f32_32x32x16_bf16 v[0:15], v[64:67], a[32:35], v[0:15]\n"
        "ds_read_b128 v[76:79], v121 offset:7168\n"
        "v_cvt_pk_bf16_f32 v86, v44, v45\n"
        "v_mfma_f32_32x32x16_bf16 v[16:31], v[64:67], a[36:39], v[16:31]\n"
        "s_waitcnt lgkmcnt(2)\n"
        "v_cvt_pk_bf16_f32 v87, v46, v47\n"
        "v_mfma_f32_32x32x16_bf16 v[0:15], v[68:71], a[40:43], v[0:15]\n"
        "ds_read_b128 v[64:67], v121 offset:8192\n"
        "buffer_store_dwordx4 v[84:87], v120, s[40:43], s50 offen offset:1024 sc1\n"
        "v_mfma_f32_32x32x16_bf16 v[16:31], v[68:71], a[44:47], v[16:31]\n"
        "s_waitcnt lgkmcnt(2)\n"
        "v_cvt_pk_bf16_f32 v88, v48, v49\n"
        "v_mfma_f32_32x32x16_bf16 v[0:15], v[72:75], a[48:51], v[0:15]\n"
        "ds_read_b128 v[68:71], v121 offset:9216\n"
        "v_cvt_pk_bf16_f32 v89, v50, v51\n"
        "v_mfma_f32_32x32x16_bf16 v[16:31], v[72:75], a[52:55], v[16:31]\n"
        "s_waitcnt lgkmcnt(2)\n"
        "v_cvt_pk_bf16_f32 v90, v52, v53\n"
        "v_mfma_f32_32x32x16_bf16 v[0:15], v[76:79], a[56:59], v[0:15]\n"
        "ds_read_b128 v[72:75], v121 offset:10240\n"
        "v_cvt_pk_bf16_f32 v91, v54, v55\n"
        "v_mfma_f32_32x32x16_bf16 v[16:31], v[76:79], a[60:63], v[16:31]\n"
        "buffer_store_dwordx4 v[88:91], v120, s[40:43], s50 offen offset:2048 sc1\n"
        "s_waitcnt lgkmcnt(2)\n"
        "v_mfma_f32_32x32x16_bf16 v[0:15], v[64:67], a[64:67], v[0:15]\n"
        "ds_read_b128 v[76:79], v121 offset:11264\n"
        "v_cvt_pk_bf16_f32 v92, v56, v57\n"
        "v_mfma_f32_32x32x16_bf16 v[16:31], v[64:67], a[68:71], v[16:31]\n"
        "s_add_u32 s54, s54, 36864\n"
        "v_cvt_pk_bf16_f32 v93, v58, v59\n"
        "s_waitcnt lgkmcnt(2)\n"
        "v_mfma_f32_32x32x16_bf16 v[0:15], v[68:71], a[72:75], v[0:15]\n"
        "ds_read_b128 v[64:67], v121 offset:12288\n"
        "v_cvt_pk_bf16_f32 v94, v60, v61\n"
        "v_mfma_f32_32x32x16_bf16 v[16:31], v[68:71], a[76:79], v[16:31]\n"
        "s_cmp_ge_u32 s54, 110592\n"
        "v_cvt_pk_bf16_f32 v95, v62, v63\n"
        "s_waitcnt lgkmcnt(2)\n"
        "v_mfma_f32_32x32x16_bf16 v[0:15], v[72:75], a[80:83], v[0:15]\n"
        "ds_read_b128 v[68:71], v121 offset:13312\n"
        "buffer_store_dwordx4 v[92:95], v120, s[40:43], s50 offen offset:3072 sc1\n"
        "v_mfma_f32_32x32x16_bf16 v[16:31], v[72:75], a[84:87], v[16:31]\n"
        "s_cselect_b32 s52, 110592, 0\n"
        "s_waitcnt lgkmcnt(2)\n"
        "v_mfma_f32_32x32x16_bf16 v[0:15], v[76:79], a[88:91], v[0:15]\n"
        "ds_read_b128 v[72:75], v121 offset:14336\n"
        "v_mfma_f32_32x32x16_bf16 v[16:31], v[76:79], a[92:95], v[16:31]\n"
        "s_sub_u32 s54, s54, s52\n"
        "s_waitcnt lgkmcnt(2)\n"
        "v_mfma_f32_32x32x16_bf16 v[0:15], v[64:67], a[96:99], v[0:15]\n"
        "ds_read_b128 v[76:79], v121 offset:15360\n"
        "v_mfma_f32_32x32x16_bf16 v[16:31], v[64:67], a[100:103], v[16:31]\n"
        "s_waitcnt lgkmcnt(2)\n"
        "v_mfma_f32_32x32x16_bf16 v[0:15], v[68:71], a[104:107], v[0:15]\n"
        "ds_read_b128 v[64:67], v121 offset:16384\n"
        "buffer_load_dwordx4 v120, s[40:43], s53 offen offset:3072 sc1 lds\n"
        "v_mfma_f32_32x32x16_bf16 v[16:31], v[68:71], a[108:111], v[16:31]\n"
        "s_waitcnt lgkmcnt(2)\n"
        "v_mfma_f32_32x32x16_bf16 v[0:15], v[72:75], a[112:115], v[0:15]\n"
        "ds_read_b128 v[68:71], v121 offset:17408\n"
        "v_mfma_f32_32x32x16_bf16 v[16:31], v[72:75], a[116:119], v[16:31]\n"
        "s_waitcnt lgkmcnt(2)\n"
        "v_mfma_f32_32x32x16_bf16 v[0:15], v[76:79], a[120:123], v[0:15]\n"
        "ds_read_b128 v[72:75], v121 offset:18432\n"
        "v_mfma_f32_32x32x16_bf16 v[16:31], v[76:79], a[124:127], v[16:31]\n"
        "s_waitcnt lgkmcnt(2)\n"
        "v_mfma_f32_32x32x16_bf16 v[0:15], v[64:67], a[128:131], v[0:15]\n"
        "ds_read_b128 v[76:79], v121 offset:19456\n"
        "s_add_u32 m0, s55, 4096\n"
        "s_add_u32 s53, s49, 4096\n"
        "buffer_load_dwordx4 v120, s[40:43], s53 offen offset:0 sc1 lds\n"
        "v_mfma_f32_32x32x16_bf16 v[16:31], v[64:67], a[132:135], v[16:31]\n"
        "s_waitcnt lgkmcnt(2)\n"
        "v_mfma_f32_32x32x16_bf16 v[0:15], v[68:71], a[136:139], v[0:15]\n"
        "ds_read_b128 v[64:67], v121 offset:20480\n"
        "v_mfma_f32_32x32x16_bf16 v[16:31], v[68:71], a[140:143], v[16:31]\n"
        "s_waitcnt lgkmcnt(2)\n"
        "v_mfma_f32_32x32x16_bf16 v[0:15], v[72:75], a[144:147], v[0:15]\n"
        "ds_read_b128 v[68:71], v121 offset:21504\n"
        "v_mfma_f32_32x32x16_bf16 v[16:31], v[72:75], a[148:151], v[16:31]\n"
        "s_waitcnt lgkmcnt(2)\n"
        "v_mfma_f32_32x32x16_bf16 v[0:15], v[76:79], a[152:155], v[0:15]\n"
        "ds_read_b128 v[72:75], v121 offset:22528\n"
        "buffer_load_dwordx4 v120, s[40:43], s53 offen offset:1024 sc1 lds\n"
        "v_mfma_f32_32x32x16_bf16 v[16:31], v[76:79], a[156:159], v[16:31]\n"
        "s_waitcnt lgkmcnt(2)\n"
        "v_mfma_f32_32x32x16_bf16 v[0:15], v[64:67], a[160:163], v[0:15]\n"
        "ds_read_b128 v[76:79], v121 offset:23552\n"
        "v_mfma_f32_32x32x16_bf16 v[16:31], v[64:67], a[164:167], v[16:31]\n"
        "s_waitcnt lgkmcnt(2)\n"
        "v_mfma_f32_32x32x16_bf16 v[0:15], v[68:71], a[168:171], v[0:15]\n"
        "ds_read_b128 v[64:67], v121 offset:24576\n"
        "v_mfma_f32_32x32x16_bf16 v[16:31], v[68:71], a[172:175], v[16:31]\n"
        "s_waitcnt lgkmcnt(2)\n"
        "v_mfma_f32_32x32x16_bf16 v[0:15], v[72:75], a[176:179], v[0:15]\n"
        "ds_read_b128 v[68:71], v121 offset:25600\n"
        "buffer_load_dwordx4 v120, s[40:43], s53 offen offset:2048 sc1 lds\n"
        "v_mfma_f32_32x32x16_bf16 v[16:31], v[72:75], a[180:183], v[16:31]\n"
        "s_waitcnt lgkmcnt(2)\n"
        "v_mfma_f32_32x32x16_bf16 v[0:15], v[76:79], a[184:187], v[0:15]\n"
        "ds_read_b128 v[72:75], v121 offset:26624\n"
        "v_mfma_f32_32x32x16_bf16 v[16:31], v[76:79], a[188:191], v[16:31]\n"
        "s_sub_u32 s52, s48, 0\n"
        "s_waitcnt lgkmcnt(2)\n"
        "v_mfma_f32_32x32x16_bf16 v[0:15], v[64:67], a[192:195], v[0:15]\n"
        "ds_read_b128 v[76:79], v121 offset:27648\n"
        "v_mfma_f32_32x32x16_bf16 v[16:31], v[64:67], a[196:199], v[16:31]\n"
        "s_max_i32 s52, s52, 0\n"
        "s_waitcnt lgkmcnt(2)\n"
        "v_mfma_f32_32x32x16_bf16 v[0:15], v[68:71], a[200:203], v[0:15]\n"
        "ds_read_b128 v[64:67], v121 offset:28672\n"
        "buffer_load_dwordx4 v120, s[40:43], s53 offen offset:3072 sc1 lds\n"
        "v_mfma_f32_32x32x16_bf16 v[16:31], v[68:71], a[204:207], v[16:31]\n"
        "s_waitcnt lgkmcnt(2)\n"
        "v_mfma_f32_32x32x16_bf16 v[0:15], v[72:75], a[208:211], v[0:15]\n"
        "ds_read_b128 v[68:71], v121 offset:29696\n"
        "v_mfma_f32_32x32x16_bf16 v[16:31], v[72:75], a[212:215], v[16:31]\n"
        "s_waitcnt lgkmcnt(2)\n"
        "v_mfma_f32_32x32x16_bf16 v[0:15], v[76:79], a[216:219], v[0:15]\n"
        "ds_read_b128 v[72:75], v121 offset:30720\n"
        "s_add_u32 m0, s55, 8192\n"
        "s_add_u32 s53, s49, 8192\n"
        "buffer_load_dwordx4 v120, s[40:43], s53 offen offset:0 sc1 lds\n"
        "v_mfma_f32_32x32x16_bf16 v[16:31], v[76:79], a[220:223], v[16:31]\n"
        "s_waitcnt lgkmcnt(2)\n"
        "v_mfma_f32_32x32x16_bf16 v[0:15], v[64:67], a[224:227], v[0:15]\n"
        "ds_read_b128 v[76:79], v121 offset:31744\n"
        "v_mfma_f32_32x32x16_bf16 v[16:31], v[64:67], a[228:231], v[16:31]\n"
        "s_add_u32 s49, s49, 36864\n"
        "s_waitcnt lgkmcnt(2)\n"
        "v_mfma_f32_32x32x16_bf16 v[0:15], v[68:71], a[232:235], v[0:15]\n"
        "ds_read_b128 v[64:67], v121 offset:32768\n"
        "v_mfma_f32_32x32x16_bf16 v[16:31], v[68:71], a[236:239], v[16:31]\n"
        "s_add_u32 s55, s55, 36864\n"
        "s_waitcnt lgkmcnt(2)\n"
        "v_mfma_f32_32x32x16_bf16 v[0:15], v[72:75], a[240:243], v[0:15]\n"
        "ds_read_b128 v[68:71], v121 offset:33792\n"
        "v_mfma_f32_32x32x16_bf16 v[16:31], v[72:75], a[244:247], v[16:31]\n"
        "s_cmp_ge_u32 s55, 110592\n"
        "s_waitcnt lgkmcnt(2)\n"
        "v_mfma_f32_32x32x16_bf16 v[0:15], v[76:79], a[248:251], v[0:15]\n"
        "ds_read_b128 v[72:75], v121 offset:34816\n"
        "v_mfma_f32_32x32x16_bf16 v[16:31], v[76:79], a[252:255], v[16:31]\n"
        "s_cselect_b32 s53, 110592, 0\n"
        "s_waitcnt lgkmcnt(2)\n"
        "v_mfma_f32_32x32x16_bf16 v[0:15], v[64:67], v[224:227], v[0:15]\n"
        "ds_read_b128 v[76:79], v121 offset:35840\n"
        "v_mfma_f32_32x32x16_bf16 v[16:31], v[64:67], v[228:231], v[16:31]\n"
        "s_sub_u32 s55, s55, s53\n"
        "s_waitcnt lgkmcnt(2)\n"
        "v_mfma_f32_32x32x16_bf16 v[0:15], v[68:71], v[232:235], v[0:15]\n"
        "v_mov_b32 v122, s52\n"
        "v_add_u32 v123, v124, v123\n"
        "v_add_u32 v121, s54, v120\n"
        "s_mov_b32 s50, s51\n"
        "s_add_u32 s51, s51, %4\n"
        "s_add_u32 s48, s48, 1\n"
        "s_cmp_ge_u32 s48, 3\n"
        "v_mfma_f32_32x32x16_bf16 v[16:31], v[68:71], v[236:239], v[16:31]\n"
        "s_cselect_b64 exec, 7, 1\n"
        "s_waitcnt vmcnt(6) lgkmcnt(0)\n"
        "buffer_store_dword v122, v123, s[40:43], 0 offen sc1\n"
        "s_mov_b64 exec, -1\n"
        "s_barrier\n"
        "v_mfma_f32_32x32x16_bf16 v[0:15], v[72:75], v[240:243], v[0:15]\n"
        "ds_read_b128 v[64:67], v121 offset:0\n"
        "s_add_u32 m0, s55, 0\n"
        "s_add_u32 s53, s49, 0\n"
        "buffer_load_dwordx4 v120, s[40:43], s53 offen offset:0 sc1 lds\n"
        "v_mfma_f32_32x32x16_bf16 v[16:31], v[72:75], v[244:247], v[16:31]\n"
        "ds_read_b128 v[68:71], v121 offset:1024\n"
        "v_mfma_f32_32x32x16_bf16 v[0:15], v[76:79], v[248:251], v[0:15]\n"
        "ds_read_b128 v[72:75], v121 offset:2048\n"
        "buffer_load_dwordx4 v120, s[40:43], s53 offen offset:1024 sc1 lds\n"
        "v_mfma_f32_32x32x16_bf16 v[16:31], v[76:79], v[252:255], v[16:31]\n"
        "s_cmp_eq_u32 s48, 65\n"
        "s_cbranch_scc1 20f\n"
        "s_waitcnt lgkmcnt(2)\n"
        "v_mfma_f32_32x32x16_bf16 v[32:47], v[64:67], a[0:3], 0\n"
        "ds_read_b128 v[76:79], v121 offset:3072\n"
        "buffer_load_dwordx4 v120, s[40:43], s53 offen offset:2048 sc1 lds\n"
        "v_mfma_f32_32x32x16_bf16 v[48:63], v[64:67], a[4:7], 0\n"
        "s_waitcnt lgkmcnt(2)\n"
        "v_cvt_pk_bf16_f32 v80, v0, v1\n"
        "v_mfma_f32_32x32x16_bf16 v[32:47], v[68:71], a[8:11], v[32:47]\n"
        "ds_read_b128 v[64:67], v121 offset:4096\n"
        "v_cvt_pk_bf16_f32 v81, v2, v3\n"
        "v_mfma_f32_32x32x16_bf16 v[48:63], v[68:71], a[12:15], v[48:63]\n"
        "s_waitcnt lgkmcnt(2)\n"
        "v_cvt_pk_bf16_f32 v82, v4, v5\n"
        "v_mfma_f32_32x32x16_bf16 v[32:47], v[72:75], a[16:19], v[32:47]\n"
        "ds_read_b128 v[68:71], v121 offset:5120\n"
        "v_cvt_pk_bf16_f32 v83, v6, v7\n"
        "v_mfma_f32_32x32x16_bf16 v[48:63], v[72:75], a[20:23], v[48:63]\n"
        "buffer_store_dwordx4 v[80:83], v120, s[40:43], s50 offen offset:0 sc1\n"
        "s_waitcnt lgkmcnt(2)\n"
        "v_mfma_f32_32x32x16_bf16 v[32:47], v[76:79], a[24:27], v[32:47]\n"
        "ds_read_b128 v[72:75], v121 offset:6144\n"
        "v_cvt_pk_bf16_f32 v84, v8, v9\n"
        "v_mfma_f32_32x32x16_bf16 v[48:63], v[76:79], a[28:31], v[48:63]\n"
        "s_waitcnt lgkmcnt(2)\n"
        "v_cvt_pk_bf16_f32 v85, v10, v11\n"
        "v_mfma_f32_32x32x16_bf16 v[32:47], v[64:67], a[32:35], v[32:47]\n"
        "ds_read_b128 v[76:79], v121 offset:7168\n"
        "v_cvt_pk_bf16_f32 v86, v12, v13\n"
        "v_mfma_f32_32x32x16_bf16 v[48:63], v[64:67], a[36:39], v[48:63]\n"
        "s_waitcnt lgkmcnt(2)\n"
        "v_cvt_pk_bf16_f32 v87, v14, v15\n"
        "v_mfma_f32_32x32x16_bf16 v[32:47], v[68:71], a[40:43], v[32:47]\n"
        "ds_read_b128 v[64:67], v121 offset:8192\n"
        "buffer_store_dwordx4 v[84:87], v120, s[40:43], s50 offen offset:1024 sc1\n"
        "v_mfma_f32_32x32x16_bf16 v[48:63], v[68:71], a[44:47], v[48:63]\n"
        "s_waitcnt lgkmcnt(2)\n"
        "v_cvt_pk_bf16_f32 v88, v16, v17\n"
        "v_mfma_f32_32x32x16_bf16 v[32:47], v[72:75], a[48:51], v[32:47]\n"
        "ds_read_b128 v[68:71], v121 offset:9216\n"
        "v_cvt_pk_bf16_f32 v89, v18, v19\n"
        "v_mfma_f32_32x32x16_bf16 v[48:63], v[72:75], a[52:55], v[48:63]\n"
        "s_waitcnt lgkmcnt(2)\n"
        "v_cvt_pk_bf16_f32 v90, v20, v21\n"
        "v_mfma_f32_32x32x16_bf16 v[32:47], v[76:79], a[56:59], v[32:47]\n"
        "ds_read_b128 v[72:75], v121 offset:10240\n"
        "v_cvt_pk_bf16_f32 v91, v22, v23\n"
        "v_mfma_f32_32x32x16_bf16 v[48:63], v[76:79], a[60:63], v[48:63]\n"
        "buffer_store_dwordx4 v[88:91], v120, s[40:43], s50 offen offset:2048 sc1\n"
        "s_waitcnt lgkmcnt(2)\n"
        "v_mfma_f32_32x32x16_bf16 v[32:47], v[64:67], a[64:67], v[32:47]\n"
        "ds_read_b128 v[76:79], v121 offset:11264\n"
        "v_cvt_pk_bf16_f32 v92, v24, v25\n"
        "v_mfma_f32_32x32x16_bf16 v[48:63], v[64:67], a[68:71], v[48:63]\n"
        "s_add_u32 s54, s54, 36864\n"
        "v_cvt_pk_bf16_f32 v93, v26, v27\n"
        "s_waitcnt lgkmcnt(2)\n"
        "v_mfma_f32_32x32x16_bf16 v[32:47], v[68:71], a[72:75], v[32:47]\n"
        "ds_read_b128 v[64:67], v121 offset:12288\n"
        "v_cvt_pk_bf16_f32 v94, v28, v29\n"
        "v_mfma_f32_32x32x16_bf16 v[48:63], v[68:71], a[76:79], v[48:63]\n"
        "s_cmp_ge_u32 s54, 110592\n"
        "v_cvt_pk_bf16_f32 v95, v30, v31\n"
        "s_waitcnt lgkmcnt(2)\n"
        "v_mfma_f32_32x32x16_bf16 v[32:47], v[72:75], a[80:83], v[32:47]\n"
        "ds_read_b128 v[68:71], v121 offset:13312\n"
        "buffer_store_dwordx4 v[92:95], v120, s[40:43], s50 offen offset:3072 sc1\n"
        "v_mfma_f32_32x32x16_bf16 v[48:63], v[72:75], a[84:87], v[48:63]\n"
        "s_cselect_b32 s52, 110592, 0\n"
        "s_waitcnt lgkmcnt(2)\n"
        "v_mfma_f32_32x32x16_bf16 v[32:47], v[76:79], a[88:91], v[32:47]\n"
        "ds_read_b128 v[72:75], v121 offset:14336\n"
        "v_mfma_f32_32x32x16_bf16 v[48:63], v[76:79], a[92:95], v[48:63]\n"
        "s_sub_u32 s54, s54, s52\n"
        "s_waitcnt lgkmcnt(2)\n"
        "v_mfma_f32_32x32x16_bf16 v[32:47], v[64:67], a[96:99], v[32:47]\n"
        "ds_read_b128 v[76:79], v121 offset:15360\n"
        "v_mfma_f32_32x32x16_bf16 v[48:63], v[64:67], a[100:103], v[48:63]\n"
        "s_waitcnt lgkmcnt(2)\n"
        "v_mfma_f32_32x32x16_bf16 v[32:47], v[68:71], a[104:107], v[32:47]\n"
        "ds_read_b128 v[64:67], v121 offset:16384\n"
        "buffer_load_dwordx4 v120, s[40:43], s53 offen offset:3072 sc1 lds\n"
        "v_mfma_f32_32x32x16_bf16 v[48:63], v[68:71], a[108:111], v[48:63]\n"
        "s_waitcnt lgkmcnt(2)\n"
        "v_mfma_f32_32x32x16_bf16 v[32:47], v[72:75], a[112:115], v[32:47]\n"
        "ds_read_b128 v[68:71], v121 offset:17408\n"
        "v_mfma_f32_32x32x16_bf16 v[48:63], v[72:75], a[116:119], v[48:63]\n"
        "s_waitcnt lgkmcnt(2)\n"
        "v_mfma_f32_32x32x16_bf16 v[32:47], v[76:79], a[120:123], v[32:47]\n"
        "ds_read_b128 v[72:75], v121 offset:18432\n"
        "v_mfma_f32_32x32x16_bf16 v[48:63], v[76:79], a[124:127], v[48:63]\n"
        "s_waitcnt lgkmcnt(2)\n"
        "v_mfma_f32_32x32x16_bf16 v[32:47], v[64:67], a[128:131], v[32:47]\n"
        "ds_read_b128 v[76:79], v121 offset:19456\n"
        "s_add_u32 m0, s55, 4096\n"
        "s_add_u32 s53, s49, 4096\n"
        "buffer_load_dwordx4 v120, s[40:43], s53 offen offset:0 sc1 lds\n"
        "v_mfma_f32_32x32x16_bf16 v[48:63], v[64:67], a[132:135], v[48:63]\n"
        "s_waitcnt lgkmcnt(2)\n"
        "v_mfma_f32_32x32x16_bf16 v[32:47], v[68:71], a[136:139], v[32:47]\n"
        "ds_read_b128 v[64:67], v121 offset:20480\n"
        "v_mfma_f32_32x32x16_bf16 v[48:63], v[68:71], a[140:143], v[48:63]\n"
        "s_waitcnt lgkmcnt(2)\n"
        "v_mfma_f32_32x32x16_bf16 v[32:47], v[72:75], a[144:147], v[32:47]\n"
        "ds_read_b128 v[68:71], v121 offset:21504\n"
        "v_mfma_f32_32x32x16_bf16 v[48:63], v[72:75], a[148:151], v[48:63]\n"
        "s_waitcnt lgkmcnt(2)\n"
        "v_mfma_f32_32x32x16_bf16 v[32:47], v[76:79], a[152:155], v[32:47]\n"
        "ds_read_b128 v[72:75], v121 offset:22528\n"
        "buffer_load_dwordx4 v120, s[40:43], s53 offen offset:1024 sc1 lds\n"
        "v_mfma_f32_32x32x16_bf16 v[48:63], v[76:79], a[156:159], v[48:63]\n"
        "s_waitcnt lgkmcnt(2)\n"
        "v_mfma_f32_32x32x16_bf16 v[32:47], v[64:67], a[160:163], v[32:47]\n"
        "ds_read_b128 v[76:79], v121 offset:23552\n"
        "v_mfma_f32_32x32x16_bf16 v[48:63], v[64:67], a[164:167], v[48:63]\n"
        "s_waitcnt lgkmcnt(2)\n"
        "v_mfma_f32_32x32x16_bf16 v[32:47], v[68:71], a[168:171], v[32:47]\n"
        "ds_read_b128 v[64:67], v121 offset:24576\n"
        "v_mfma_f32_32x32x16_bf16 v[48:63], v[68:71], a[172:175], v[48:63]\n"
        "s_waitcnt lgkmcnt(2)\n"
        "v_mfma_f32_32x32x16_bf16 v[32:47], v[72:75], a[176:179], v[32:47]\n"
        "ds_read_b128 v[68:71], v121 offset:25600\n"
        "buffer_load_dwordx4 v120, s[40:43], s53 offen offset:2048 sc1 lds\n"
        "v_mfma_f32_32x32x16_bf16 v[48:63], v[72:75], a[180:183], v[48:63]\n"
        "s_waitcnt lgkmcnt(2)\n"
        "v_mfma_f32_32x32x16_bf16 v[32:47], v[76:79], a[184:187], v[32:47]\n"
        "ds_read_b128 v[72:75], v121 offset:26624\n"
        "v_mfma_f32_32x32x16_bf16 v[48:63], v[76:79], a[188:191], v[48:63]\n"
        "s_sub_u32 s52, s48, 0\n"
        "s_waitcnt lgkmcnt(2)\n"
        "v_mfma_f32_32x32x16_bf16 v[32:47], v[64:67], a[192:195], v[32:47]\n"
        "ds_read_b128 v[76:79], v121 offset:27648\n"
        "v_mfma_f32_32x32x16_bf16 v[48:63], v[64:67], a[196:199], v[48:63]\n"
        "s_max_i32 s52, s52, 0\n"
        "s_waitcnt lgkmcnt(2)\n"
        "v_mfma_f32_32x32x16_bf16 v[32:47], v[68:71], a[200:203], v[32:47]\n"
        "ds_read_b128 v[64:67], v121 offset:28672\n"
        "buffer_load_dwordx4 v120, s[40:43], s53 offen offset:3072 sc1 lds\n"
        "v_mfma_f32_32x32x16_bf16 v[48:63], v[68:71], a[204:207], v[48:63]\n"
        "s_waitcnt lgkmcnt(2)\n"
        "v_mfma_f32_32x32x16_bf16 v[32:47], v[72:75], a[208:211], v[32:47]\n"
        "ds_read_b128 v[68:71], v121 offset:29696\n"
        "v_mfma_f32_32x32x16_bf16 v[48:63], v[72:75], a[212:215], v[48:63]\n"
        "s_waitcnt lgkmcnt(2)\n"
        "v_mfma_f32_32x32x16_bf16 v[32:47], v[76:79], a[216:219], v[32:47]\n"
        "ds_read_b128 v[72:75], v121 offset:30720\n"
        "s_add_u32 m0, s55, 8192\n"
        "s_add_u32 s53, s49, 8192\n"
        "buffer_load_dwordx4 v120, s[40:43], s53 offen offset:0 sc1 lds\n"
        "v_mfma_f32_32x32x16_bf16 v[48:63], v[76:79], a[220:223], v[48:63]\n"
        "s_waitcnt lgkmcnt(2)\n"
        "v_mfma_f32_32x32x16_bf16 v[32:47], v[64:67], a[224:227], v[32:47]\n"
        "ds_read_b128 v[76:79], v121 offset:31744\n"
        "v_mfma_f32_32x32x16_bf16 v[48:63], v[64:67], a[228:231], v[48:63]\n"
        "s_add_u32 s49, s49, 36864\n"
        "s_waitcnt lgkmcnt(2)\n"
        "v_mfma_f32_32x32x16_bf16 v[32:47], v[68:71], a[232:235], v[32:47]\n"
        "ds_read_b128 v[64:67], v121 offset:32768\n"
        "v_mfma_f32_32x32x16_bf16 v[48:63], v[68:71], a[236:239], v[48:63]\n"
        "s_add_u32 s55, s55, 36864\n"
        "s_waitcnt lgkmcnt(2)\n"
        "v_mfma_f32_32x32x16_bf16 v[32:47], v[72:75], a[240:243], v[32:47]\n"
        "ds_read_b128 v[68:71], v121 offset:33792\n"
        "v_mfma_f32_32x32x16_bf16 v[48:63], v[72:75], a[244:247], v[48:63]\n"
        "s_cmp_ge_u32 s55, 110592\n"
        "s_waitcnt lgkmcnt(2)\n"
        "v_mfma_f32_32x32x16_bf16 v[32:47], v[76:79], a[248:251], v[32:47]\n"
        "ds_read_b128 v[72:75], v121 offset:34816\n"
        "v_mfma_f32_32x32x16_bf16 v[48:63], v[76:79], a[252:255], v[48:63]\n"
        "s_cselect_b32 s53, 110592, 0\n"
        "s_waitcnt lgkmcnt(2)\n"
        "v_mfma_f32_32x32x16_bf16 v[32:47], v[64:67], v[224:227], v[32:47]\n"
        "ds_read_b128 v[76:79], v121 offset:35840\n"
        "v_mfma_f32_32x32x16_bf16 v[48:63], v[64:67], v[228:231], v[48:63]\n"
        "s_sub_u32 s55, s55, s53\n"
        "s_waitcnt lgkmcnt(2)\n"
        "v_mfma_f32_32x32x16_bf16 v[32:47], v[68:71], v[232:235], v[32:47]\n"
        "v_mov_b32 v122, s52\n"
        "v_add_u32 v123, v124, v123\n"
        "v_add_u32 v121, s54, v120\n"
        "s_mov_b32 s50, s51\n"
        "s_add_u32 s51, s51, %4\n"
        "s_add_u32 s48, s48, 1\n"
        "s_cmp_ge_u32 s48, 3\n"
        "v_mfma_f32_32x32x16_bf16 v[48:63], v[68:71], v[236:239], v[48:63]\n"
        "s_cselect_b64 exec, 7, 1\n"
        "s_waitcnt vmcnt(6) lgkmcnt(0)\n"
        "buffer_store_dword v122, v123, s[40:43], 0 offen sc1\n"
        "s_mov_b64 exec, -1\n"
        "s_barrier\n"
        "v_mfma_f32_32x32x16_bf16 v[32:47], v[72:75], v[240:243], v[32:47]\n"
        "ds_read_b128 v[64:67], v121 offset:0\n"
        "s_add_u32 m0, s55, 0\n"
        "s_add_u32 s53, s49, 0\n"
        "buffer_load_dwordx4 v120, s[40:43], s53 offen offset:0 sc1 lds\n"
        "v_mfma_f32_32x32x16_bf16 v[48:63], v[72:75], v[244:247], v[48:63]\n"
        "ds_read_b128 v[68:71], v121 offset:1024\n"
        "v_mfma_f32_32x32x16_bf16 v[32:47], v[76:79], v[248:251], v[32:47]\n"
        "ds_read_b128 v[72:75], v121 offset:2048\n"
        "buffer_load_dwordx4 v120, s[40:43], s53 offen offset:1024 sc1 lds\n"
        "v_mfma_f32_32x32x16_bf16 v[48:63], v[76:79], v[252:255], v[48:63]\n"
        "s_cmp_eq_u32 s48, 65\n"
        "s_cmp_eq_u32 s48, 12\n"
        "s_cbranch_scc0 11b\n"
        "s_lshl4_add_u32 s52, %9, 0x426800\n"
        "s_mov_b32 s64, 0x10000\n"
        "30:\n"
        "buffer_load_dword v122, off, s[40:43], s52 sc1\n"
        "s_waitcnt vmcnt(0)\n"
        "v_cmp_eq_u32 vcc, 0, v122\n"
        "s_cbranch_vccz 11b\n"
        "s_sub_u32 s64, s64, 1\n"
        "s_cbranch_scc0 30b\n"
        "s_branch 11b\n"
        "12:\n"
        "s_waitcnt lgkmcnt(2)\n"
        "v_mfma_f32_32x32x16_bf16 v[0:15], a[0:3], v[64:67], 0\n"
        "ds_read_b128 v[76:79], v121 offset:3072\n"
        "buffer_load_dwordx4 v120, s[40:43], s53 offen offset:2048 sc1 lds\n"
        "v_mfma_f32_32x32x16_bf16 v[16:31], a[4:7], v[64:67], 0\n"
        "v_mul_f32 v96, 0xbfb8aa3b, v32\n"
        "v_mul_f32 v97, 0xbfb8aa3b, v33\n"
        "v_exp_f32 v96, v96\n"
        "s_waitcnt lgkmcnt(2)\n"
        "v_mfma_f32_32x32x16_bf16 v[0:15], a[8:11], v[68:71], v[0:15]\n"
        "ds_read_b128 v[64:67], v121 offset:4096\n"
        "v_exp_f32 v97, v97\n"
        "v_add_f32 v96, 1.0, v96\n"
        "v_add_f32 v97, 1.0, v97\n"
        "v_mfma_f32_32x32x16_bf16 v[16:31], a[12:15], v[68:71], v[16:31]\n"
        "v_rcp_f32 v96, v96\n"
        "v_rcp_f32 v97, v97\n"
        "v_mul_f32 v32, v32, v96\n"
        "s_waitcnt lgkmcnt(2)\n"
        "v_mfma_f32_32x32x16_bf16 v[0:15], a[16:19], v[72:75], v[0:15]\n"
        "ds_read_b128 v[68:71], v121 offset:5120\n"
        "v_mul_f32 v33, v33, v97\n"
        "v_cvt_pk_bf16_f32 v80, v32, v33\n"
        "v_mul_f32 v98, 0xbfb8aa3b, v34\n"
        "v_mul_f32 v99, 0xbfb8aa3b, v35\n"
        "v_mfma_f32_32x32x16_bf16 v[16:31], a[20:23], v[72:75], v[16:31]\n"
        "v_exp_f32 v98, v98\n"
        "v_exp_f32 v99, v99\n"
        "v_add_f32 v98, 1.0, v98\n"
        "s_waitcnt lgkmcnt(2)\n"
        "v_mfma_f32_32x32x16_bf16 v[0:15], a[24:27], v[76:79], v[0:15]\n"
        "ds_read_b128 v[72:75], v121 offset:6144\n"
        "buffer_load_dwordx4 v120, s[40:43], s53 offen offset:3072 sc1 lds\n"
        "v_mfma_f32_32x32x16_bf16 v[16:31], a[28:31], v[76:79], v[16:31]\n"
        "v_add_f32 v99, 1.0, v99\n"
        "v_rcp_f32 v98, v98\n"
        "v_rcp_f32 v99, v99\n"
        "s_waitcnt lgkmcnt(2)\n"
        "v_mfma_f32_32x32x16_bf16 v[0:15], a[32:35], v[64:67], v[0:15]\n"
        "ds_read_b128 v[76:79], v121 offset:7168\n"
        "v_mul_f32 v34, v34, v98\n"
        "v_mul_f32 v35, v35, v99\n"
        "v_cvt_pk_bf16_f32 v81, v34, v35\n"
        "v_mul_f32 v96, 0xbfb8aa3b, v36\n"
        "v_mul_f32 v97, 0xbfb8aa3b, v37\n"
        "v_mfma_f32_32x32x16_bf16 v[16:31], a[36:39], v[64:67], v[16:31]\n"
        "v_exp_f32 v96, v96\n"
        "v_exp_f32 v97, v97\n"
        "v_add_f32 v96, 1.0, v96\n"
        "s_waitcnt lgkmcnt(2)\n"
        "v_mfma_f32_32x32x16_bf16 v[0:15], a[40:43], v[68:71], v[0:15]\n"
        "ds_read_b128 v[64:67], v121 offset:8192\n"
        "v_add_f32 v97, 1.0, v97\n"
        "v_rcp_f32 v96, v96\n"
        "v_rcp_f32 v97, v97\n"
        "v_mfma_f32_32x32x16_bf16 v[16:31], a[44:47], v[68:71], v[16:31]\n"
        "v_mul_f32 v36, v36, v96\n"
        "v_mul_f32 v37, v37, v97\n"
        "v_cvt_pk_bf16_f32 v82, v36, v37\n"
        "v_mul_f32 v98, 0xbfb8aa3b, v38\n"
        "v_mul_f32 v99, 0xbfb8aa3b, v39\n"
        "s_waitcnt lgkmcnt(2)\n"
        "v_mfma_f32_32x32x16_bf16 v[0:15], a[48:51], v[72:75], v[0:15]\n"
        "ds_read_b128 v[68:71], v121 offset:9216\n"
        "s_add_u32 m0, s55, 4096\n"
        "s_add_u32 s53, s49, 4096\n"
        "buffer_load_dwordx4 v120, s[40:43], s53 offen offset:0 sc1 lds\n"
        "v_mfma_f32_32x32x16_bf16 v[16:31], a[52:55], v[72:75], v[16:31]\n"
        "v_exp_f32 v98, v98\n"
        "v_exp_f32 v99, v99\n"
        "v_add_f32 v98, 1.0, v98\n"
        "s_waitcnt lgkmcnt(2)\n"
        "v_mfma_f32_32x32x16_bf16 v[0:15], a[56:59], v[76:79], v[0:15]\n"
        "ds_read_b128 v[72:75], v121 offset:10240\n"
        "v_add_f32 v99, 1.0, v99\n"
        "v_rcp_f32 v98, v98\n"
        "v_rcp_f32 v99, v99\n"
        "v_mfma_f32_32x32x16_bf16 v[16:31], a[60:63], v[76:79], v[16:31]\n"
        "v_mul_f32 v38, v38, v98\n"
        "v_mul_f32 v39, v39, v99\n"
        "v_cvt_pk_bf16_f32 v83, v38, v39\n"
        "s_waitcnt lgkmcnt(2)\n"
        "v_mfma_f32_32x32x16_bf16 v[0:15], a[64:67], v[64:67], v[0:15]\n"
        "ds_read_b128 v[76:79], v121 offset:11264\n"
        "buffer_store_dwordx4 v[80:83], v125, s[40:43], s50 offen offset:0 sc1\n"
        "v_mfma_f32_32x32x16_bf16 v[16:31], a[68:71], v[64:67], v[16:31]\n"
        "s_add_u32 s54, s54, 36864\n"
        "v_mul_f32 v96, 0xbfb8aa3b, v40\n"
        "v_mul_f32 v97, 0xbfb8aa3b, v41\n"
        "v_exp_f32 v96, v96\n"
        "s_waitcnt lgkmcnt(2)\n"
        "v_mfma_f32_32x32x16_bf16 v[0:15], a[72:75], v[68:71], v[0:15]\n"
        "ds_read_b128 v[64:67], v121 offset:12288\n"
        "v_exp_f32 v97, v97\n"
        "v_add_f32 v96, 1.0, v96\n"
        "v_add_f32 v97, 1.0, v97\n"
        "v_mfma_f32_32x32x16_bf16 v[16:31], a[76:79], v[68:71], v[16:31]\n"
        "s_cmp_ge_u32 s54, 110592\n"
        "v_rcp_f32 v96, v96\n"
        "v_rcp_f32 v97, v97\n"
        "v_mul_f32 v40, v40, v96\n"
        "s_waitcnt lgkmcnt(2)\n"
        "v_mfma_f32_32x32x16_bf16 v[0:15], a[80:83], v[72:75], v[0:15]\n"
        "ds_read_b128 v[68:71], v121 offset:13312\n"
        "v_mul_f32 v41, v41, v97\n"
        "v_cvt_pk_bf16_f32 v84, v40, v41\n"
        "v_mul_f32 v98, 0xbfb8aa3b, v42\n"
        "v_mul_f32 v99, 0xbfb8aa3b, v43\n"
        "v_mfma_f32_32x32x16_bf16 v[16:31], a[84:87], v[72:75], v[16:31]\n"
        "s_cselect_b32 s52, 110592, 0\n"
        "v_exp_f32 v98, v98\n"
        "v_exp_f32 v99, v99\n"
        "v_add_f32 v98, 1.0, v98\n"
        "s_waitcnt lgkmcnt(2)\n"
        "v_mfma_f32_32x32x16_bf16 v[0:15], a[88:91], v[76:79], v[0:15]\n"
        "ds_read_b128 v[72:75], v121 offset:14336\n"
        "v_add_f32 v99, 1.0, v99\n"
        "v_rcp_f32 v98, v98\n"
        "v_rcp_f32 v99, v99\n"
        "v_mfma_f32_32x32x16_bf16 v[16:31], a[92:95], v[76:79], v[16:31]\n"
        "s_sub_u32 s54, s54, s52\n"
        "v_mul_f32 v42, v42, v98\n"
        "v_mul_f32 v43, v43, v99\n"
        "v_cvt_pk_bf16_f32 v85, v42, v43\n"
        "v_mul_f32 v96, 0xbfb8aa3b, v44\n"
        "v_mul_f32 v97, 0xbfb8aa3b, v45\n"
        "s_waitcnt lgkmcnt(2)\n"
        "v_mfma_f32_32x32x16_bf16 v[0:15], a[96:99], v[64:67], v[0:15]\n"
        "ds_read_b128 v[76:79], v121 offset:15360\n"
        "buffer_load_dwordx4 v120, s[40:43], s53 offen offset:1024 sc1 lds\n"
        "v_mfma_f32_32x32x16_bf16 v[16:31], a[100:103], v[64:67], v[16:31]\n"
        "v_exp_f32 v96, v96\n"
        "v_exp_f32 v97, v97\n"
        "v_add_f32 v96, 1.0, v96\n"
        "s_waitcnt lgkmcnt(2)\n"
        "v_mfma_f32_32x32x16_bf16 v[0:15], a[104:107], v[68:71], v[0:15]\n"
        "ds_read_b128 v[64:67], v121 offset:16384\n"
        "v_add_f32 v97, 1.0, v97\n"
        "v_rcp_f32 v96, v96\n"
        "v_rcp_f32 v97, v97\n"
        "v_mfma_f32_32x32x16_bf16 v[16:31], a[108:111], v[68:71], v[16:31]\n"
        "v_mul_f32 v44, v44, v96\n"
        "v_mul_f32 v45, v45, v97\n"
        "v_cvt_pk_bf16_f32 v86, v44, v45\n"
        "v_mul_f32 v98, 0xbfb8aa3b, v46\n"
        "v_mul_f32 v99, 0xbfb8aa3b, v47\n"
        "s_waitcnt lgkmcnt(2)\n"
        "v_mfma_f32_32x32x16_bf16 v[0:15], a[112:115], v[72:75], v[0:15]\n"
        "ds_read_b128 v[68:71], v121 offset:17408\n"
        "v_exp_f32 v98, v98\n"
        "v_exp_f32 v99, v99\n"
        "v_add_f32 v98, 1.0, v98\n"
        "v_mfma_f32_32x32x16_bf16 v[16:31], a[116:119], v[72:75], v[16:31]\n"
        "v_add_f32 v99, 1.0, v99\n"
        "v_rcp_f32 v98, v98\n"
        "v_rcp_f32 v99, v99\n"
        "s_waitcnt lgkmcnt(2)\n"
        "v_mfma_f32_32x32x16_bf16 v[0:15], a[120:123], v[76:79], v[0:15]\n"
        "ds_read_b128 v[72:75], v121 offset:18432\n"
        "buffer_load_dwordx4 v120, s[40:43], s53 offen offset:2048 sc1 lds\n"
        "v_mfma_f32_32x32x16_bf16 v[16:31], a[124:127], v[76:79], v[16:31]\n"
        "v_mul_f32 v46, v46, v98\n"
        "v_mul_f32 v47, v47, v99\n"
        "v_cvt_pk_bf16_f32 v87, v46, v47\n"
        "s_waitcnt lgkmcnt(2)\n"
        "v_mfma_f32_32x32x16_bf16 v[0:15], a[128:131], v[64:67], v[0:15]\n"
        "ds_read_b128 v[76:79], v121 offset:19456\n"
        "buffer_store_dwordx4 v[84:87], v125, s[40:43], s50 offen offset:512 sc1\n"
        "v_mfma_f32_32x32x16_bf16 v[16:31], a[132:135], v[64:67], v[16:31]\n"
        "v_mul_f32 v96, 0xbfb8aa3b, v48\n"
        "v_mul_f32 v97, 0xbfb8aa3b, v49\n"
        "v_exp_f32 v96, v96\n"
        "s_waitcnt lgkmcnt(2)\n"
        "v_mfma_f32_32x32x16_bf16 v[0:15], a[136:139], v[68:71], v[0:15]\n"
        "ds_read_b128 v[64:67], v121 offset:20480\n"
        "v_exp_f32 v97, v97\n"
        "v_add_f32 v96, 1.0, v96\n"
        "v_add_f32 v97, 1.0, v97\n"
        "v_mfma_f32_32x32x16_bf16 v[16:31], a[140:143], v[68:71], v[16:31]\n"
        "v_rcp_f32 v96, v96\n"
        "v_rcp_f32 v97, v97\n"
        "v_mul_f32 v48, v48, v96\n"
        "s_waitcnt lgkmcnt(2)\n"
        "v_mfma_f32_32x32x16_bf16 v[0:15], a[144:147], v[72:75], v[0:15]\n"
        "ds_read_b128 v[68:71], v121 offset:21504\n"
        "v_mul_f32 v49, v49, v97\n"
        "v_cvt_pk_bf16_f32 v88, v48, v49\n"
        "v_mul_f32 v98, 0xbfb8aa3b, v50\n"
        "v_mul_f32 v99, 0xbfb8aa3b, v51\n"
        "v_mfma_f32_32x32x16_bf16 v[16:31], a[148:151], v[72:75], v[16:31]\n"
        "v_exp_f32 v98, v98\n"
        "v_exp_f32 v99, v99\n"
        "v_add_f32 v98, 1.0, v98\n"
        "s_waitcnt lgkmcnt(2)\n"
        "v_mfma_f32_32x32x16_bf16 v[0:15], a[152:155], v[76:79], v[0:15]\n"
        "ds_read_b128 v[72:75], v121 offset:22528\n"
        "v_add_f32 v99, 1.0, v99\n"
        "v_rcp_f32 v98, v98\n"
        "v_rcp_f32 v99, v99\n"
        "v_mfma_f32_32x32x16_bf16 v[16:31], a[156:159], v[76:79], v[16:31]\n"
        "v_mul_f32 v50, v50, v98\n"
        "v_mul_f32 v51, v51, v99\n"
        "v_cvt_pk_bf16_f32 v89, v50, v51\n"
        "v_mul_f32 v96, 0xbfb8aa3b, v52\n"
        "v_mul_f32 v97, 0xbfb8aa3b, v53\n"
        "s_waitcnt lgkmcnt(2)\n"
        "v_mfma_f32_32x32x16_bf16 v[0:15], a[160:163], v[64:67], v[0:15]\n"
        "ds_read_b128 v[76:79], v121 offset:23552\n"
        "v_exp_f32 v96, v96\n"
        "v_exp_f32 v97, v97\n"
        "v_add_f32 v96, 1.0, v96\n"
        "v_mfma_f32_32x32x16_bf16 v[16:31], a[164:167], v[64:67], v[16:31]\n"
        "v_add_f32 v97, 1.0, v97\n"
        "v_rcp_f32 v96, v96\n"
        "v_rcp_f32 v97, v97\n"
        "s_waitcnt lgkmcnt(2)\n"
        "v_mfma_f32_32x32x16_bf16 v[0:15], a[168:171], v[68:71], v[0:15]\n"
        "ds_read_b128 v[64:67], v121 offset:24576\n"
        "buffer_load_dwordx4 v120, s[40:43], s53 offen offset:3072 sc1 lds\n"
        "v_mfma_f32_32x32x16_bf16 v[16:31], a[172:175], v[68:71], v[16:31]\n"
        "v_mul_f32 v52, v52, v96\n"
        "v_mul_f32 v53, v53, v97\n"
        "v_cvt_pk_bf16_f32 v90, v52, v53\n"
        "v_mul_f32 v98, 0xbfb8aa3b, v54\n"
        "v_mul_f32 v99, 0xbfb8aa3b, v55\n"
        "s_waitcnt lgkmcnt(2)\n"
        "v_mfma_f32_32x32x16_bf16 v[0:15], a[176:179], v[72:75], v[0:15]\n"
        "ds_read_b128 v[68:71], v121 offset:25600\n"
        "v_exp_f32 v98, v98\n"
        "v_exp_f32 v99, v99\n"
        "v_add_f32 v98, 1.0, v98\n"
        "v_mfma_f32_32x32x16_bf16 v[16:31], a[180:183], v[72:75], v[16:31]\n"
        "v_add_f32 v99, 1.0, v99\n"
        "v_rcp_f32 v98, v98\n"
        "v_rcp_f32 v99, v99\n"
        "s_waitcnt lgkmcnt(2)\n"
        "v_mfma_f32_32x32x16_bf16 v[0:15], a[184:187], v[76:79], v[0:15]\n"
        "ds_read_b128 v[72:75], v121 offset:26624\n"
        "v_mul_f32 v54, v54, v98\n"
        "v_mul_f32 v55, v55, v99\n"
        "v_cvt_pk_bf16_f32 v91, v54, v55\n"
        "v_mfma_f32_32x32x16_bf16 v[16:31], a[188:191], v[76:79], v[16:31]\n"
        "s_sub_u32 s52, s48, 1\n"
        "buffer_store_dwordx4 v[88:91], v125, s[40:43], s50 offen offset:1024 sc1\n"
        "s_waitcnt lgkmcnt(2)\n"
        "v_mfma_f32_32x32x16_bf16 v[0:15], a[192:195], v[64:67], v[0:15]\n"
        "ds_read_b128 v[76:79], v121 offset:27648\n"
        "v_mul_f32 v96, 0xbfb8aa3b, v56\n"
        "v_mul_f32 v97, 0xbfb8aa3b, v57\n"
        "v_exp_f32 v96, v96\n"
        "v_mfma_f32_32x32x16_bf16 v[16:31], a[196:199], v[64:67], v[16:31]\n"
        "s_max_i32 s52, s52, 0\n"
        "v_exp_f32 v97, v97\n"
        "v_add_f32 v96, 1.0, v96\n"
        "v_add_f32 v97, 1.0, v97\n"
        "s_waitcnt lgkmcnt(2)\n"
        "v_mfma_f32_32x32x16_bf16 v[0:15], a[200:203], v[68:71], v[0:15]\n"
        "ds_read_b128 v[64:67], v121 offset:28672\n"
        "v_rcp_f32 v96, v96\n"
        "v_rcp_f32 v97, v97\n"
        "v_mul_f32 v56, v56, v96\n"
        "v_mfma_f32_32x32x16_bf16 v[16:31], a[204:207], v[68:71], v[16:31]\n"
        "v_mul_f32 v57, v57, v97\n"
        "v_cvt_pk_bf16_f32 v92, v56, v57\n"
        "v_mul_f32 v98, 0xbfb8aa3b, v58\n"
        "v_mul_f32 v99, 0xbfb8aa3b, v59\n"
        "s_waitcnt lgkmcnt(2)\n"
        "v_mfma_f32_32x32x16_bf16 v[0:15], a[208:211], v[72:75], v[0:15]\n"
        "ds_read_b128 v[68:71], v121 offset:29696\n"
        "v_exp_f32 v98, v98\n"
        "v_exp_f32 v99, v99\n"
        "v_add_f32 v98, 1.0, v98\n"
        "v_mfma_f32_32x32x16_bf16 v[16:31], a[212:215], v[72:75], v[16:31]\n"
        "v_add_f32 v99, 1.0, v99\n"
        "v_rcp_f32 v98, v98\n"
        "v_rcp_f32 v99, v99\n"
        "s_waitcnt lgkmcnt(2)\n"
        "v_mfma_f32_32x32x16_bf16 v[0:15], a[216:219], v[76:79], v[0:15]\n"
        "ds_read_b128 v[72:75], v121 offset:30720\n"
        "s_add_u32 m0, s55, 8192\n"
        "s_add_u32 s53, s49, 8192\n"
        "buffer_load_dwordx4 v120, s[40:43], s53 offen offset:0 sc1 lds\n"
        "v_mfma_f32_32x32x16_bf16 v[16:31], a[220:223], v[76:79], v[16:31]\n"
        "v_mul_f32 v58, v58, v98\n"
        "v_mul_f32 v59, v59, v99\n"
        "v_cvt_pk_bf16_f32 v93, v58, v59\n"
        "v_mul_f32 v96, 0xbfb8aa3b, v60\n"
        "v_mul_f32 v97, 0xbfb8aa3b, v61\n"
        "s_waitcnt lgkmcnt(2)\n"
        "v_mfma_f32_32x32x16_bf16 v[0:15], a[224:227], v[64:67], v[0:15]\n"
        "ds_read_b128 v[76:79], v121 offset:31744\n"
        "v_exp_f32 v96, v96\n"
        "v_exp_f32 v97, v97\n"
        "v_add_f32 v96, 1.0, v96\n"
        "v_mfma_f32_32x32x16_bf16 v[16:31], a[228:231], v[64:67], v[16:31]\n"
        "s_add_u32 s49, s49, 36864\n"
        "v_add_f32 v97, 1.0, v97\n"
        "v_rcp_f32 v96, v96\n"
        "v_rcp_f32 v97, v97\n"
        "s_waitcnt lgkmcnt(2)\n"
        "v_mfma_f32_32x32x16_bf16 v[0:15], a[232:235], v[68:71], v[0:15]\n"
        "ds_read_b128 v[64:67], v121 offset:32768\n"
        "v_mul_f32 v60, v60, v96\n"
        "v_mul_f32 v61, v61, v97\n"
        "v_cvt_pk_bf16_f32 v94, v60, v61\n"
        "v_mul_f32 v98, 0xbfb8aa3b, v62\n"
        "v_mul_f32 v99, 0xbfb8aa3b, v63\n"
        "v_mfma_f32_32x32x16_bf16 v[16:31], a[236:239], v[68:71], v[16:31]\n"
        "s_add_u32 s55, s55, 36864\n"
        "v_exp_f32 v98, v98\n"
        "v_exp_f32 v99, v99\n"
        "v_add_f32 v98, 1.0, v98\n"
        "s_waitcnt lgkmcnt(2)\n"
        "v_mfma_f32_32x32x16_bf16 v[0:15], a[240:243], v[72:75], v[0:15]\n"
        "ds_read_b128 v[68:71], v121 offset:33792\n"
        "v_add_f32 v99, 1.0, v99\n"
        "v_rcp_f32 v98, v98\n"
        "v_rcp_f32 v99, v99\n"
        "v_mfma_f32_32x32x16_bf16 v[16:31], a[244:247], v[72:75], v[16:31]\n"
        "s_cmp_ge_u32 s55, 110592\n"
        "v_mul_f32 v62, v62, v98\n"
        "v_mul_f32 v63, v63, v99\n"
        "v_cvt_pk_bf16_f32 v95, v62, v63\n"
        "s_waitcnt lgkmcnt(2)\n"
        "v_mfma_f32_32x32x16_bf16 v[0:15], a[248:251], v[76:79], v[0:15]\n"
        "ds_read_b128 v[72:75], v121 offset:34816\n"
        "buffer_store_dwordx4 v[92:95], v125, s[40:43], s50 offen offset:1536 sc1\n"
        "v_mfma_f32_32x32x16_bf16 v[16:31], a[252:255], v[76:79], v[16:31]\n"
        "s_cselect_b32 s53, 110592, 0\n"
        "s_waitcnt lgkmcnt(2)\n"
        "v_mfma_f32_32x32x16_bf16 v[0:15], v[224:227], v[64:67], v[0:15]\n"
        "ds_read_b128 v[76:79], v121 offset:35840\n"
        "v_mfma_f32_32x32x16_bf16 v[16:31], v[228:231], v[64:67], v[16:31]\n"
        "s_sub_u32 s55, s55, s53\n"
        "s_waitcnt lgkmcnt(2)\n"
        "v_mfma_f32_32x32x16_bf16 v[0:15], v[232:235], v[68:71], v[0:15]\n"
        "v_mov_b32 v122, s52\n"
        "v_add_u32 v123, v124, v123\n"
        "v_add_u32 v121, s54, v120\n"
        "s_mov_b32 s50, s51\n"
        "s_add_u32 s51, s51, %4\n"
        "s_add_u32 s48, s48, 1\n"
        "s_cmp_ge_u32 s48, 3\n"
        "v_mfma_f32_32x32x16_bf16 v[16:31], v[236:239], v[68:71], v[16:31]\n"
        "s_mov_b64 exec, 3\n"
        "s_waitcnt vmcnt(13) lgkmcnt(0)\n"
        "buffer_store_dword v122, v123, s[40:43], 0 offen sc1\n"
        "s_mov_b64 exec, -1\n"
        "s_barrier\n"
        "v_mfma_f32_32x32x16_bf16 v[0:15], v[240:243], v[72:75], v[0:15]\n"
        "ds_read_b128 v[64:67], v121 offset:0\n"
        "s_add_u32 m0, s55, 0\n"
        "s_add_u32 s53, s49, 0\n"
        "buffer_load_dwordx4 v120, s[40:43], s53 offen offset:0 sc1 lds\n"
        "v_mfma_f32_32x32x16_bf16 v[16:31], v[244:247], v[72:75], v[16:31]\n"
        "ds_read_b128 v[68:71], v121 offset:1024\n"
        "v_mfma_f32_32x32x16_bf16 v[0:15], v[248:251], v[76:79], v[0:15]\n"
        "ds_read_b128 v[72:75], v121 offset:2048\n"
        "buffer_load_dwordx4 v120, s[40:43], s53 offen offset:1024 sc1 lds\n"
        "v_mfma_f32_32x32x16_bf16 v[16:31], v[252:255], v[76:79], v[16:31]\n"
        "s_cmp_eq_u32 s48, 65\n"
        "s_cbranch_scc1 20f\n"
        "s_waitcnt lgkmcnt(2)\n"
        "v_mfma_f32_32x32x16_bf16 v[32:47], a[0:3], v[64:67], 0\n"
        "ds_read_b128 v[76:79], v121 offset:3072\n"
        "buffer_load_dwordx4 v120, s[40:43], s53 offen offset:2048 sc1 lds\n"
        "v_mfma_f32_32x32x16_bf16 v[48:63], a[4:7], v[64:67], 0\n"
        "v_mul_f32 v96, 0xbfb8aa3b, v0\n"
        "v_mul_f32 v97, 0xbfb8aa3b, v1\n"
        "v_exp_f32 v96, v96\n"
        "s_waitcnt lgkmcnt(2)\n"
        "v_mfma_f32_32x32x16_bf16 v[32:47], a[8:11], v[68:71], v[32:47]\n"
        "ds_read_b128 v[64:67], v121 offset:4096\n"
        "v_exp_f32 v97, v97\n"
        "v_add_f32 v96, 1.0, v96\n"
        "v_add_f32 v97, 1.0, v97\n"
        "v_mfma_f32_32x32x16_bf16 v[48:63], a[12:15], v[68:71], v[48:63]\n"
        "v_rcp_f32 v96, v96\n"
        "v_rcp_f32 v97, v97\n"
        "v_mul_f32 v0, v0, v96\n"
        "s_waitcnt lgkmcnt(2)\n"
        "v_mfma_f32_32x32x16_bf16 v[32:47], a[16:19], v[72:75], v[32:47]\n"
        "ds_read_b128 v[68:71], v121 offset:5120\n"
        "v_mul_f32 v1, v1, v97\n"
        "v_cvt_pk_bf16_f32 v80, v0, v1\n"
        "v_mul_f32 v98, 0xbfb8aa3b, v2\n"
        "v_mul_f32 v99, 0xbfb8aa3b, v3\n"
        "v_mfma_f32_32x32x16_bf16 v[48:63], a[20:23], v[72:75], v[48:63]\n"
        "v_exp_f32 v98, v98\n"
        "v_exp_f32 v99, v99\n"
        "v_add_f32 v98, 1.0, v98\n"
        "s_waitcnt lgkmcnt(2)\n"
        "v_mfma_f32_32x32x16_bf16 v[32:47], a[24:27], v[76:79], v[32:47]\n"
        "ds_read_b128 v[72:75], v121 offset:6144\n"
        "buffer_load_dwordx4 v120, s[40:43], s53 offen offset:3072 sc1 lds\n"
        "v_mfma_f32_32x32x16_bf16 v[48:63], a[28:31], v[76:79], v[48:63]\n"
        "v_add_f32 v99, 1.0, v99\n"
        "v_rcp_f32 v98, v98\n"
        "v_rcp_f32 v99, v99\n"
        "s_waitcnt lgkmcnt(2)\n"
        "v_mfma_f32_32x32x16_bf16 v[32:47], a[32:35], v[64:67], v[32:47]\n"
        "ds_read_b128 v[76:79], v121 offset:7168\n"
        "v_mul_f32 v2, v2, v98\n"
        "v_mul_f32 v3, v3, v99\n"
        "v_cvt_pk_bf16_f32 v81, v2, v3\n"
        "v_mul_f32 v96, 0xbfb8aa3b, v4\n"
        "v_mul_f32 v97, 0xbfb8aa3b, v5\n"
        "v_mfma_f32_32x32x16_bf16 v[48:63], a[36:39], v[64:67], v[48:63]\n"
        "v_exp_f32 v96, v96\n"
        "v_exp_f32 v97, v97\n"
        "v_add_f32 v96, 1.0, v96\n"
        "s_waitcnt lgkmcnt(2)\n"
        "v_mfma_f32_32x32x16_bf16 v[32:47], a[40:43], v[68:71], v[32:47]\n"
        "ds_read_b128 v[64:67], v121 offset:8192\n"
        "v_add_f32 v97, 1.0, v97\n"
        "v_rcp_f32 v96, v96\n"
        "v_rcp_f32 v97, v97\n"
        "v_mfma_f32_32x32x16_bf16 v[48:63], a[44:47], v[68:71], v[48:63]\n"
        "v_mul_f32 v4, v4, v96\n"
        "v_mul_f32 v5, v5, v97\n"
        "v_cvt_pk_bf16_f32 v82, v4, v5\n"
        "v_mul_f32 v98, 0xbfb8aa3b, v6\n"
        "v_mul_f32 v99, 0xbfb8aa3b, v7\n"
        "s_waitcnt lgkmcnt(2)\n"
        "v_mfma_f32_32x32x16_bf16 v[32:47], a[48:51], v[72:75], v[32:47]\n"
        "ds_read_b128 v[68:71], v121 offset:9216\n"
        "s_add_u32 m0, s55, 4096\n"
        "s_add_u32 s53, s49, 4096\n"
        "buffer_load_dwordx4 v120, s[40:43], s53 offen offset:0 sc1 lds\n"
        "v_mfma_f32_32x32x16_bf16 v[48:63], a[52:55], v[72:75], v[48:63]\n"
        "v_exp_f32 v98, v98\n"
        "v_exp_f32 v99, v99\n"
        "v_add_f32 v98, 1.0, v98\n"
        "s_waitcnt lgkmcnt(2)\n"
        "v_mfma_f32_32x32x16_bf16 v[32:47], a[56:59], v[76:79], v[32:47]\n"
        "ds_read_b128 v[72:75], v121 offset:10240\n"
        "v_add_f32 v99, 1.0, v99\n"
        "v_rcp_f32 v98, v98\n"
        "v_rcp_f32 v99, v99\n"
        "v_mfma_f32_32x32x16_bf16 v[48:63], a[60:63], v[76:79], v[48:63]\n"
        "v_mul_f32 v6, v6, v98\n"
        "v_mul_f32 v7, v7, v99\n"
        "v_cvt_pk_bf16_f32 v83, v6, v7\n"
        "s_waitcnt lgkmcnt(2)\n"
        "v_mfma_f32_32x32x16_bf16 v[32:47], a[64:67], v[64:67], v[32:47]\n"
        "ds_read_b128 v[76:79], v121 offset:11264\n"
        "buffer_store_dwordx4 v[80:83], v125, s[40:43], s50 offen offset:0 sc1\n"
        "v_mfma_f32_32x32x16_bf16 v[48:63], a[68:71], v[64:67], v[48:63]\n"
        "s_add_u32 s54, s54, 36864\n"
        "v_mul_f32 v96, 0xbfb8aa3b, v8\n"
        "v_mul_f32 v97, 0xbfb8aa3b, v9\n"
        "v_exp_f32 v96, v96\n"
        "s_waitcnt lgkmcnt(2)\n"
        "v_mfma_f32_32x32x16_bf16 v[32:47], a[72:75], v[68:71], v[32:47]\n"
        "ds_read_b128 v[64:67], v121 offset:12288\n"
        "v_exp_f32 v97, v97\n"
        "v_add_f32 v96, 1.0, v96\n"
        "v_add_f32 v97, 1.0, v97\n"
        "v_mfma_f32_32x32x16_bf16 v[48:63], a[76:79], v[68:71], v[48:63]\n"
        "s_cmp_ge_u32 s54, 110592\n"
        "v_rcp_f32 v96, v96\n"
        "v_rcp_f32 v97, v97\n"
        "v_mul_f32 v8, v8, v96\n"
        "s_waitcnt lgkmcnt(2)\n"
        "v_mfma_f32_32x32x16_bf16 v[32:47], a[80:83], v[72:75], v[32:47]\n"
        "ds_read_b128 v[68:71], v121 offset:13312\n"
        "v_mul_f32 v9, v9, v97\n"
        "v_cvt_pk_bf16_f32 v84, v8, v9\n"
        "v_mul_f32 v98, 0xbfb8aa3b, v10\n"
        "v_mul_f32 v99, 0xbfb8aa3b, v11\n"
        "v_mfma_f32_32x32x16_bf16 v[48:63], a[84:87], v[72:75], v[48:63]\n"
        "s_cselect_b32 s52, 110592, 0\n"
        "v_exp_f32 v98, v98\n"
        "v_exp_f32 v99, v99\n"
        "v_add_f32 v98, 1.0, v98\n"
        "s_waitcnt lgkmcnt(2)\n"
        "v_mfma_f32_32x32x16_bf16 v[32:47], a[88:91], v[76:79], v[32:47]\n"
        "ds_read_b128 v[72:75], v121 offset:14336\n"
        "v_add_f32 v99, 1.0, v99\n"
        "v_rcp_f32 v98, v98\n"
        "v_rcp_f32 v99, v99\n"
        "v_mfma_f32_32x32x16_bf16 v[48:63], a[92:95], v[76:79], v[48:63]\n"
        "s_sub_u32 s54, s54, s52\n"
        "v_mul_f32 v10, v10, v98\n"
        "v_mul_f32 v11, v11, v99\n"
        "v_cvt_pk_bf16_f32 v85, v10, v11\n"
        "v_mul_f32 v96, 0xbfb8aa3b, v12\n"
        "v_mul_f32 v97, 0xbfb8aa3b, v13\n"
        "s_waitcnt lgkmcnt(2)\n"
        "v_mfma_f32_32x32x16_bf16 v[32:47], a[96:99], v[64:67], v[32:47]\n"
        "ds_read_b128 v[76:79], v121 offset:15360\n"
        "buffer_load_dwordx4 v120, s[40:43], s53 offen offset:1024 sc1 lds\n"
        "v_mfma_f32_32x32x16_bf16 v[48:63], a[100:103], v[64:67], v[48:63]\n"
        "v_exp_f32 v96, v96\n"
        "v_exp_f32 v97, v97\n"
        "v_add_f32 v96, 1.0, v96\n"
        "s_waitcnt lgkmcnt(2)\n"
        "v_mfma_f32_32x32x16_bf16 v[32:47], a[104:107], v[68:71], v[32:47]\n"
        "ds_read_b128 v[64:67], v121 offset:16384\n"
        "v_add_f32 v97, 1.0, v97\n"
        "v_rcp_f32 v96, v96\n"
        "v_rcp_f32 v97, v97\n"
        "v_mfma_f32_32x32x16_bf16 v[48:63], a[108:111], v[68:71], v[48:63]\n"
        "v_mul_f32 v12, v12, v96\n"
        "v_mul_f32 v13, v13, v97\n"
        "v_cvt_pk_bf16_f32 v86, v12, v13\n"
        "v_mul_f32 v98, 0xbfb8aa3b, v14\n"
        "v_mul_f32 v99, 0xbfb8aa3b, v15\n"
        "s_waitcnt lgkmcnt(2)\n"
        "v_mfma_f32_32x32x16_bf16 v[32:47], a[112:115], v[72:75], v[32:47]\n"
        "ds_read_b128 v[68:71], v121 offset:17408\n"
        "v_exp_f32 v98, v98\n"
        "v_exp_f32 v99, v99\n"
        "v_add_f32 v98, 1.0, v98\n"
        "v_mfma_f32_32x32x16_bf16 v[48:63], a[116:119], v[72:75], v[48:63]\n"
        "v_add_f32 v99, 1.0, v99\n"
        "v_rcp_f32 v98, v98\n"
        "v_rcp_f32 v99, v99\n"
        "s_waitcnt lgkmcnt(2)\n"
        "v_mfma_f32_32x32x16_bf16 v[32:47], a[120:123], v[76:79], v[32:47]\n"
        "ds_read_b128 v[72:75], v121 offset:18432\n"
        "buffer_load_dwordx4 v120, s[40:43], s53 offen offset:2048 sc1 lds\n"
        "v_mfma_f32_32x32x16_bf16 v[48:63], a[124:127], v[76:79], v[48:63]\n"
        "v_mul_f32 v14, v14, v98\n"
        "v_mul_f32 v15, v15, v99\n"
        "v_cvt_pk_bf16_f32 v87, v14, v15\n"
        "s_waitcnt lgkmcnt(2)\n"
        "v_mfma_f32_32x32x16_bf16 v[32:47], a[128:131], v[64:67], v[32:47]\n"
        "ds_read_b128 v[76:79], v121 offset:19456\n"
        "buffer_store_dwordx4 v[84:87], v125, s[40:43], s50 offen offset:512 sc1\n"
        "v_mfma_f32_32x32x16_bf16 v[48:63], a[132:135], v[64:67], v[48:63]\n"
        "v_mul_f32 v96, 0xbfb8aa3b, v16\n"
        "v_mul_f32 v97, 0xbfb8aa3b, v17\n"
        "v_exp_f32 v96, v96\n"
        "s_waitcnt lgkmcnt(2)\n"
        "v_mfma_f32_32x32x16_bf16 v[32:47], a[136:139], v[68:71], v[32:47]\n"
        "ds_read_b128 v[64:67], v121 offset:20480\n"
        "v_exp_f32 v97, v97\n"
        "v_add_f32 v96, 1.0, v96\n"
        "v_add_f32 v97, 1.0, v97\n"
        "v_mfma_f32_32x32x16_bf16 v[48:63], a[140:143], v[68:71], v[48:63]\n"
        "v_rcp_f32 v96, v96\n"
        "v_rcp_f32 v97, v97\n"
        "v_mul_f32 v16, v16, v96\n"
        "s_waitcnt lgkmcnt(2)\n"
        "v_mfma_f32_32x32x16_bf16 v[32:47], a[144:147], v[72:75], v[32:47]\n"
        "ds_read_b128 v[68:71], v121 offset:21504\n"
        "v_mul_f32 v17, v17, v97\n"
        "v_cvt_pk_bf16_f32 v88, v16, v17\n"
        "v_mul_f32 v98, 0xbfb8aa3b, v18\n"
        "v_mul_f32 v99, 0xbfb8aa3b, v19\n"
        "v_mfma_f32_32x32x16_bf16 v[48:63], a[148:151], v[72:75], v[48:63]\n"
        "v_exp_f32 v98, v98\n"
        "v_exp_f32 v99, v99\n"
        "v_add_f32 v98, 1.0, v98\n"
        "s_waitcnt lgkmcnt(2)\n"
        "v_mfma_f32_32x32x16_bf16 v[32:47], a[152:155], v[76:79], v[32:47]\n"
        "ds_read_b128 v[72:75], v121 offset:22528\n"
        "v_add_f32 v99, 1.0, v99\n"
        "v_rcp_f32 v98, v98\n"
        "v_rcp_f32 v99, v99\n"
        "v_mfma_f32_32x32x16_bf16 v[48:63], a[156:159], v[76:79], v[48:63]\n"
        "v_mul_f32 v18, v18, v98\n"
        "v_mul_f32 v19, v19, v99\n"
        "v_cvt_pk_bf16_f32 v89, v18, v19\n"
        "v_mul_f32 v96, 0xbfb8aa3b, v20\n"
        "v_mul_f32 v97, 0xbfb8aa3b, v21\n"
        "s_waitcnt lgkmcnt(2)\n"
        "v_mfma_f32_32x32x16_bf16 v[32:47], a[160:163], v[64:67], v[32:47]\n"
        "ds_read_b128 v[76:79], v121 offset:23552\n"
        "v_exp_f32 v96, v96\n"
        "v_exp_f32 v97, v97\n"
        "v_add_f32 v96, 1.0, v96\n"
        "v_mfma_f32_32x32x16_bf16 v[48:63], a[164:167], v[64:67], v[48:63]\n"
        "v_add_f32 v97, 1.0, v97\n"
        "v_rcp_f32 v96, v96\n"
        "v_rcp_f32 v97, v97\n"
        "s_waitcnt lgkmcnt(2)\n"
        "v_mfma_f32_32x32x16_bf16 v[32:47], a[168:171], v[68:71], v[32:47]\n"
        "ds_read_b128 v[64:67], v121 offset:24576\n"
        "buffer_load_dwordx4 v120, s[40:43], s53 offen offset:3072 sc1 lds\n"
        "v_mfma_f32_32x32x16_bf16 v[48:63], a[172:175], v[68:71], v[48:63]\n"
        "v_mul_f32 v20, v20, v96\n"
        "v_mul_f32 v21, v21, v97\n"
        "v_cvt_pk_bf16_f32 v90, v20, v21\n"
        "v_mul_f32 v98, 0xbfb8aa3b, v22\n"
        "v_mul_f32 v99, 0xbfb8aa3b, v23\n"
        "s_waitcnt lgkmcnt(2)\n"
        "v_mfma_f32_32x32x16_bf16 v[32:47], a[176:179], v[72:75], v[32:47]\n"
        "ds_read_b128 v[68:71], v121 offset:25600\n"
        "v_exp_f32 v98, v98\n"
        "v_exp_f32 v99, v99\n"
        "v_add_f32 v98, 1.0, v98\n"
        "v_mfma_f32_32x32x16_bf16 v[48:63], a[180:183], v[72:75], v[48:63]\n"
        "v_add_f32 v99, 1.0, v99\n"
        "v_rcp_f32 v98, v98\n"
        "v_rcp_f32 v99, v99\n"
        "s_waitcnt lgkmcnt(2)\n"
        "v_mfma_f32_32x32x16_bf16 v[32:47], a[184:187], v[76:79], v[32:47]\n"
        "ds_read_b128 v[72:75], v121 offset:26624\n"
        "v_mul_f32 v22, v22, v98\n"
        "v_mul_f32 v23, v23, v99\n"
        "v_cvt_pk_bf16_f32 v91, v22, v23\n"
        "v_mfma_f32_32x32x16_bf16 v[48:63], a[188:191], v[76:79], v[48:63]\n"
        "s_sub_u32 s52, s48, 1\n"
        "buffer_store_dwordx4 v[88:91], v125, s[40:43], s50 offen offset:1024 sc1\n"
        "s_waitcnt lgkmcnt(2)\n"
        "v_mfma_f32_32x32x16_bf16 v[32:47], a[192:195], v[64:67], v[32:47]\n"
        "ds_read_b128 v[76:79], v121 offset:27648\n"
        "v_mul_f32 v96, 0xbfb8aa3b, v24\n"
        "v_mul_f32 v97, 0xbfb8aa3b, v25\n"
        "v_exp_f32 v96, v96\n"
        "v_mfma_f32_32x32x16_bf16 v[48:63], a[196:199], v[64:67], v[48:63]\n"
        "s_max_i32 s52, s52, 0\n"
        "v_exp_f32 v97, v97\n"
        "v_add_f32 v96, 1.0, v96\n"
        "v_add_f32 v97, 1.0, v97\n"
        "s_waitcnt lgkmcnt(2)\n"
        "v_mfma_f32_32x32x16_bf16 v[32:47], a[200:203], v[68:71], v[32:47]\n"
        "ds_read_b128 v[64:67], v121 offset:28672\n"
        "v_rcp_f32 v96, v96\n"
        "v_rcp_f32 v97, v97\n"
        "v_mul_f32 v24, v24, v96\n"
        "v_mfma_f32_32x32x16_bf16 v[48:63], a[204:207], v[68:71], v[48:63]\n"
        "v_mul_f32 v25, v25, v97\n"
        "v_cvt_pk_bf16_f32 v92, v24, v25\n"
        "v_mul_f32 v98, 0xbfb8aa3b, v26\n"
        "v_mul_f32 v99, 0xbfb8aa3b, v27\n"
        "s_waitcnt lgkmcnt(2)\n"
        "v_mfma_f32_32x32x16_bf16 v[32:47], a[208:211], v[72:75], v[32:47]\n"
        "ds_read_b128 v[68:71], v121 offset:29696\n"
        "v_exp_f32 v98, v98\n"
        "v_exp_f32 v99, v99\n"
        "v_add_f32 v98, 1.0, v98\n"
        "v_mfma_f32_32x32x16_bf16 v[48:63], a[212:215], v[72:75], v[48:63]\n"
        "v_add_f32 v99, 1.0, v99\n"
        "v_rcp_f32 v98, v98\n"
        "v_rcp_f32 v99, v99\n"
        "s_waitcnt lgkmcnt(2)\n"
        "v_mfma_f32_32x32x16_bf16 v[32:47], a[216:219], v[76:79], v[32:47]\n"
        "ds_read_b128 v[72:75], v121 offset:30720\n"
        "s_add_u32 m0, s55, 8192\n"
        "s_add_u32 s53, s49, 8192\n"
        "buffer_load_dwordx4 v120, s[40:43], s53 offen offset:0 sc1 lds\n"
        "v_mfma_f32_32x32x16_bf16 v[48:63], a[220:223], v[76:79], v[48:63]\n"
        "v_mul_f32 v26, v26, v98\n"
        "v_mul_f32 v27, v27, v99\n"
        "v_cvt_pk_bf16_f32 v93, v26, v27\n"
        "v_mul_f32 v96, 0xbfb8aa3b, v28\n"
        "v_mul_f32 v97, 0xbfb8aa3b, v29\n"
        "s_waitcnt lgkmcnt(2)\n"
        "v_mfma_f32_32x32x16_bf16 v[32:47], a[224:227], v[64:67], v[32:47]\n"
        "ds_read_b128 v[76:79], v121 offset:31744\n"
        "v_exp_f32 v96, v96\n"
        "v_exp_f32 v97, v97\n"
        "v_add_f32 v96, 1.0, v96\n"
        "v_mfma_f32_32x32x16_bf16 v[48:63], a[228:231], v[64:67], v[48:63]\n"
        "s_add_u32 s49, s49, 36864\n"
        "v_add_f32 v97, 1.0, v97\n"
        "v_rcp_f32 v96, v96\n"
        "v_rcp_f32 v97, v97\n"
        "s_waitcnt lgkmcnt(2)\n"
        "v_mfma_f32_32x32x16_bf16 v[32:47], a[232:235], v[68:71], v[32:47]\n"
        "ds_read_b128 v[64:67], v121 offset:32768\n"
        "v_mul_f32 v28, v28, v96\n"
        "v_mul_f32 v29, v29, v97\n"
        "v_cvt_pk_bf16_f32 v94, v28, v29\n"
        "v_mul_f32 v98, 0xbfb8aa3b, v30\n"
        "v_mul_f32 v99, 0xbfb8aa3b, v31\n"
        "v_mfma_f32_32x32x16_bf16 v[48:63], a[236:239], v[68:71], v[48:63]\n"
        "s_add_u32 s55, s55, 36864\n"
        "v_exp_f32 v98, v98\n"
        "v_exp_f32 v99, v99\n"
        "v_add_f32 v98, 1.0, v98\n"
        "s_waitcnt lgkmcnt(2)\n"
        "v_mfma_f32_32x32x16_bf16 v[32:47], a[240:243], v[72:75], v[32:47]\n"
        "ds_read_b128 v[68:71], v121 offset:33792\n"
        "v_add_f32 v99, 1.0, v99\n"
        "v_rcp_f32 v98, v98\n"
        "v_rcp_f32 v99, v99\n"
        "v_mfma_f32_32x32x16_bf16 v[48:63], a[244:247], v[72:75], v[48:63]\n"
        "s_cmp_ge_u32 s55, 110592\n"
        "v_mul_f32 v30, v30, v98\n"
        "v_mul_f32 v31, v31, v99\n"
        "v_cvt_pk_bf16_f32 v95, v30, v31\n"
        "s_waitcnt lgkmcnt(2)\n"
        "v_mfma_f32_32x32x16_bf16 v[32:47], a[248:251], v[76:79], v[32:47]\n"
        "ds_read_b128 v[72:75], v121 offset:34816\n"
        "buffer_store_dwordx4 v[92:95], v125, s[40:43], s50 offen offset:1536 sc1\n"
        "v_mfma_f32_32x32x16_bf16 v[48:63], a[252:255], v[76:79], v[48:63]\n"
        "s_cselect_b32 s53, 110592, 0\n"
        "s_waitcnt lgkmcnt(2)\n"
        "v_mfma_f32_32x32x16_bf16 v[32:47], v[224:227], v[64:67], v[32:47]\n"
        "ds_read_b128 v[76:79], v121 offset:35840\n"
        "v_mfma_f32_32x32x16_bf16 v[48:63], v[228:231], v[64:67], v[48:63]\n"
        "s_sub_u32 s55, s55, s53\n"
        "s_waitcnt lgkmcnt(2)\n"
        "v_mfma_f32_32x32x16_bf16 v[32:47], v[232:235], v[68:71], v[32:47]\n"
        "v_mov_b32 v122, s52\n"
        "v_add_u32 v123, v124, v123\n"
        "v_add_u32 v121, s54, v120\n"
        "s_mov_b32 s50, s51\n"
        "s_add_u32 s51, s51, %4\n"
        "s_add_u32 s48, s48, 1\n"
        "s_cmp_ge_u32 s48, 3\n"
        "v_mfma_f32_32x32x16_bf16 v[48:63], v[236:239], v[68:71], v[48:63]\n"
        "s_mov_b64 exec, 3\n"
        "s_waitcnt vmcnt(13) lgkmcnt(0)\n"
        "buffer_store_dword v122, v123, s[40:43], 0 offen sc1\n"
        "s_mov_b64 exec, -1\n"
        "s_barrier\n"
        "v_mfma_f32_32x32x16_bf16 v[32:47], v[240:243], v[72:75], v[32:47]\n"
        "ds_read_b128 v[64:67], v121 offset:0\n"
        "s_add_u32 m0, s55, 0\n"
        "s_add_u32 s53, s49, 0\n"
        "buffer_load_dwordx4 v120, s[40:43], s53 offen offset:0 sc1 lds\n"
        "v_mfma_f32_32x32x16_bf16 v[48:63], v[244:247], v[72:75], v[48:63]\n"
        "ds_read_b128 v[68:71], v121 offset:1024\n"
        "v_mfma_f32_32x32x16_bf16 v[32:47], v[248:251], v[76:79], v[32:47]\n"
        "ds_read_b128 v[72:75], v121 offset:2048\n"
        "buffer_load_dwordx4 v120, s[40:43], s53 offen offset:1024 sc1 lds\n"
        "v_mfma_f32_32x32x16_bf16 v[48:63], v[252:255], v[76:79], v[48:63]\n"
        "s_cmp_eq_u32 s48, 65\n"
        "s_cmp_eq_u32 s48, 12\n"
        "s_cbranch_scc0 12b\n"
        "s_lshl4_add_u32 s52, %9, 0x426800\n"
        "s_mov_b32 s64, 0x10000\n"
        "30:\n"
        "buffer_load_dword v122, off, s[40:43], s52 sc1\n"
        "s_waitcnt vmcnt(0)\n"
        "v_cmp_eq_u32 vcc, 0, v122\n"
        "s_cbranch_vccz 12b\n"
        "s_sub_u32 s64, s64, 1\n"
        "s_cbranch_scc0 30b\n"
        "s_branch 12b\n"
        "20:\n"
        "v_mov_b32 v122, 64\n"
        "v_add_u32 v123, v124, v123\n"
        "s_waitcnt vmcnt(0) lgkmcnt(0)\n"
        "s_mov_b64 exec, 3\n"
        "buffer_store_dword v122, v123, s[40:43], 0 offen sc1\n"
        "s_mov_b64 exec, -1\n"
        :: "s"(ws), "s"(W), "s"(ldw), "s"(out), "s"(stride), "s"(flag), "s"(w), "s"(kind), "s"(flag1), "s"(blk)
        : "v0", "v1", "v2", "v3", "v4", "v5", "v6", "v7", "v8", "v9", "v10", "v11", "v12", "v13", "v14", "v15", "v16", "v17", "v18", "v19", "v20", "v21", "v22", "v23", "v24", "v25", "v26", "v27", "v28", "v29", "v30", "v31", "v32", "v33", "v34", "v35", "v36", "v37", "v38", "v39", "v40", "v41", "v42", "v43", "v44", "v45", "v46", "v47", "v48", "v49", "v50", "v51", "v52", "v53", "v54", "v55", "v56", "v57", "v58", "v59", "v60", "v61", "v62", "v63", "v64", "v65", "v66", "v67", "v68", "v69", "v70", "v71", "v72", "v73", "v74", "v75", "v76", "v77", "v78", "v79", "v80", "v81", "v82", "v83", "v84", "v85", "v86", "v87", "v88", "v89", "v90", "v91", "v92", "v93", "v94", "v95", "v96", "v97", "v98", "v99", "v100", "v101", "v102", "v103", "v104", "v105", "v106", "v107", "v108", "v109", "v110", "v111", "v112", "v113", "v114", "v115", "v116", "v117", "v118", "v119", "v120", "v121", "v122", "v123", "v124", "v125", "v126", "v127", "v128", "v129", "v130", "v131", "v132", "v133", "v134", "v135", "v136", "v137", "v138", "v139", "v140", "v141", "v142", "v143", "v144", "v145", "v146", "v147", "v148", "v149", "v150", "v151", "v152", "v153", "v154", "v155", "v156", "v157", "v158", "v159", "v160", "v161", "v162", "v163", "v164", "v165", "v166", "v167", "v168", "v169", "v170", "v171", "v172", "v173", "v174", "v175", "v176", "v177", "v178", "v179", "v180", "v181", "v182", "v183", "v184", "v185", "v186", "v187", "v188", "v189", "v190", "v191", "v192", "v193", "v194", "v195", "v196", "v197", "v198", "v199", "v200", "v201", "v202", "v203", "v204", "v205", "v206", "v207", "v208", "v209", "v210", "v211", "v212", "v213", "v214", "v215", "v216", "v217", "v218", "v219", "v220", "v221", "v222", "v223", "v224", "v225", "v226", "v227", "v228", "v229", "v230", "v231", "v232", "v233", "v234", "v235", "v236", "v237", "v238", "v239", "v240", "v241", "v242", "v243", "v244", "v245", "v246", "v247", "v248", "v249", "v250", "v251", "v252", "v253", "v254", "v255", "a0", "a1", "a2", "a3", "a4", "a5", "a6", "a7", "a8", "a9", "a10", "a11", "a12", "a13", "a14", "a15", "a16", "a17", "a18", "a19", "a20", "a21", "a22", "a23", "a24", "a25", "a26", "a27", "a28", "a29", "a30", "a31", "a32", "a33", "a34", "a35", "a36", "a37", "a38", "a39", "a40", "a41", "a42", "a43", "a44", "a45", "a46", "a47", "a48", "a49", "a50", "a51", "a52", "a53", "a54", "a55", "a56", "a57", "a58", "a59", "a60", "a61", "a62", "a63", "a64", "a65", "a66", "a67", "a68", "a69", "a70", "a71", "a72", "a73", "a74", "a75", "a76", "a77", "a78", "a79", "a80", "a81", "a82", "a83", "a84", "a85", "a86", "a87", "a88", "a89", "a90", "a91", "a92", "a93", "a94", "a95", "a96", "a97", "a98", "a99", "a100", "a101", "a102", "a103", "a104", "a105", "a106", "a107", "a108", "a109", "a110", "a111", "a112", "a113", "a114", "a115", "a116", "a117", "a118", "a119", "a120", "a121", "a122", "a123", "a124", "a125", "a126", "a127", "a128", "a129", "a130", "a131", "a132", "a133", "a134", "a135", "a136", "a137", "a138", "a139", "a140", "a141", "a142", "a143", "a144", "a145", "a146", "a147", "a148", "a149", "a150", "a151", "a152", "a153", "a154", "a155", "a156", "a157", "a158", "a159", "a160", "a161", "a162", "a163", "a164", "a165", "a166", "a167", "a168", "a169", "a170", "a171", "a172", "a173", "a174", "a175", "a176", "a177", "a178", "a179", "a180", "a181", "a182", "a183", "a184", "a185", "a186", "a187", "a188", "a189", "a190", "a191", "a192", "a193", "a194", "a195", "a196", "a197", "a198", "a199", "a200", "a201", "a202", "a203", "a204", "a205", "a206", "a207", "a208", "a209", "a210", "a211", "a212", "a213", "a214", "a215", "a216", "a217", "a218", "a219", "a220", "a221", "a222", "a223", "a224", "a225", "a226", "a227", "a228", "a229", "a230", "a231", "a232", "a233", "a234", "a235", "a236", "a237", "a238", "a239", "a240", "a241", "a242", "a243", "a244", "a245", "a246", "a247", "a248", "a249", "a250", "a251", "a252", "a253", "a254", "a255", "s40", "s41", "s42", "s43", "s44", "s45", "s46", "s47", "s48", "s49", "s50", "s51", "s52", "s53", "s54", "s55", "s56", "s57", "s58", "s59", "s60", "s61", "s62", "s63", "s64", "vcc", "scc", "memory");
}

static_assert(FL_KM == 4198400 && FL_KV == 4194304 && FL_OF == 4210688 && OFF_KF == 8388608 && OFF_KM == 96468992 && OFF_OF == 58720256 && OFF_VF == 25165824 && OFF_VM == 100663296 && OFF_XF == 5242880 && SPIN_MAX == 65536 && WS_SPAN == 109051904, "constants of the assembly block");
__device__ __forceinline__ void attend_weights(const u16* W, int w, int lane) {
    const rsrc_t rw = __builtin_amdgcn_make_buffer_rsrc((void*)W, 0, 4718592, 0x00020000);
    const int off = (lane >> 4) * 8192 + (((lane >> 4) * 64 ^ lane * 16) & 0xf0);
    for (int r = 0; r < 36; ++r) asm volatile("s_mov_b32 m0, %0\n\ts_nop 0\n\tbuffer_load_dwordx4 %1, %2, %3 offen lds\n\ts_sleep 2" :: "s"(r * 4096 + w * 1024), "v"(off), "s"(rw), "s"(r * 131072 + w * 32768) : "memory");
}
__device__ __forceinline__ void attend(unsigned char* ws, int hd, int w, unsigned par, unsigned from) {
    asm volatile(
        "s_mov_b64 s[40:41], %0\n"
        "s_mov_b32 s42, 0x6800000\n"
        "s_mov_b32 s43, 0x20000\n"
        "v_mbcnt_lo_u32_b32 v0, -1, 0\n"
        "v_mbcnt_hi_u32_b32 v0, -1, v0\n"
        "v_lshlrev_b32 v232, 4, v0\n"
        "s_mov_b32 s48, %3\n"
        "s_mul_i32 s49, %3, 36864\n"
        "s_add_u32 s49, s49, 0x500000\n"
        "s_lshl_b32 s52, %3, 5\n"
        "s_add_u32 s52, s52, %2\n"
        "s_lshl_b32 s50, s52, 13\n"
        "s_lshl_b32 s51, s52, 11\n"
        "s_lshr_b32 s52, %2, 3\n"
        "s_lshl3_add_u32 s52, %3, s52\n"
        "s_lshl_b32 s57, s52, 7\n"
        "s_and_b32 s52, %2, 7\n"
        "s_lshl2_add_u32 s57, s52, s57\n"
        "s_add_u32 s57, s57, 0x404000\n"
        "s_lshl_b32 s59, %2, 7\n"
        "s_add_u32 s59, s59, 0x400000\n"
        "s_lshr_b32 s52, %2, 1\n"
        "s_lshl_b32 s52, s52, 8\n"
        "s_and_b32 s53, %2, 1\n"
        "s_lshl_b32 s53, s53, 6\n"
        "s_add_u32 s56, s52, s53\n"
        "s_and_b32 s52, %1, 1\n"
        "s_lshl_b32 s52, s52, 13\n"
        "s_add_u32 s56, s56, s52\n"
        "s_lshl_b32 s52, %3, 15\n"
        "s_add_u32 s56, s56, s52\n"
        "s_add_u32 s56, s56, 53376\n"
        "s_mov_b32 s54, 0\n"
        "s_mov_b32 s55, 0x10000\n"
        "s_mov_b32 s58, 0x3e0293ee\n"
        "v_mov_b32 v248, 0xd028965f\n"
        "v_mov_b32 v254, 0\n"
        "v_lshrrev_b32 v1, 5, v0\n"
        "v_bfe_u32 v2, v0, 2, 2\n"
        "v_lshl_add_u32 v3, v1, 3, v2\n"
        "v_and_b32 v4, 16, v0\n"
        "v_lshlrev_b32 v4, 1, v4\n"
        "v_lshl_add_u32 v3, v3, 8, v4\n"
        "v_and_b32 v4, 3, v0\n"
        "v_lshl_add_u32 v3, v4, 3, v3\n"
        "v_lshlrev_b32 v247, 2, v4\n"
        "v_and_b32 v5, 31, v0\n"
        "v_lshlrev_b32 v246, 4, v5\n"
        "v_lshl_add_u32 v246, v1, 3, v246\n"
        "v_and_b32 v6, 7, v0\n"
        "v_lshlrev_b32 v6, 5, v6\n"
        "v_lshl_add_u32 v245, v1, 4, v6\n"
        "v_and_b32 v6, 0xf0, v232\n"
        "v_lshl_add_u32 v6, v1, 8, v6\n"
        "v_and_b32 v255, 16, v0\n"
        "v_lshl_add_u32 v255, v255, 13, v6\n"
        "v_lshlrev_b32 v6, 2, v1\n"
        "v_sub_u32 v5, v5, v6\n"
        "v_cmp_le_i32 s[60:61], 0, v5\n"
        "v_cmp_le_i32 s[62:63], 1, v5\n"
        "v_cmp_le_i32 s[64:65], 2, v5\n"
        "v_cmp_le_i32 s[66:67], 3, v5\n"
        "v_cmp_le_i32 s[68:69], 8, v5\n"
        "v_cmp_le_i32 s[70:71], 9, v5\n"
        "v_cmp_le_i32 s[72:73], 10, v5\n"
        "v_cmp_le_i32 s[74:75], 11, v5\n"
        "v_cmp_le_i32 s[76:77], 16, v5\n"
        "v_cmp_le_i32 s[78:79], 17, v5\n"
        "v_cmp_le_i32 s[80:81], 18, v5\n"
        "v_cmp_le_i32 s[82:83], 19, v5\n"
        "v_cmp_le_i32 s[84:85], 24, v5\n"
        "v_cmp_le_i32 s[86:87], 25, v5\n"
        "v_cmp_le_i32 s[88:89], 26, v5\n"
        "v_cmp_le_i32 s[90:91], 27, v5\n"
        "v_xor_b32 v4, 0, v2\n"
        "v_lshl_add_u32 v233, v4, 6, v3\n"
        "v_add_u32 v234, 65536, v233\n"
        "v_add_u32 v235, 131072, v233\n"
        "v_xor_b32 v4, 1, v2\n"
        "v_lshl_add_u32 v236, v4, 6, v3\n"
        "v_add_u32 v237, 65536, v236\n"
        "v_add_u32 v238, 131072, v236\n"
        "v_xor_b32 v4, 2, v2\n"
        "v_lshl_add_u32 v239, v4, 6, v3\n"
        "v_add_u32 v240, 65536, v239\n"
        "v_add_u32 v241, 131072, v239\n"
        "v_xor_b32 v4, 3, v2\n"
        "v_lshl_add_u32 v242, v4, 6, v3\n"
        "v_add_u32 v243, 65536, v242\n"
        "v_add_u32 v244, 131072, v242\n"
        "s_waitcnt vmcnt(0)\n"
        "s_barrier\n"
        "buffer_load_dwordx4 a[0:3], v232, s[40:43], s49 offen offset:0 sc1\n"
        "buffer_load_dwordx4 a[4:7], v232, s[40:43], s49 offen offset:1024 sc1\n"
        "buffer_load_dwordx4 a[8:11], v232, s[40:43], s49 offen offset:2048 sc1\n"
        "buffer_load_dwordx4 a[12:15], v232, s[40:43], s49 offen offset:3072 sc1\n"
        "s_add_u32 s49, s49, 4096\n"
        "buffer_load_dwordx4 a[16:19], v232, s[40:43], s49 offen offset:0 sc1\n"
        "buffer_load_dwordx4 a[20:23], v232, s[40:43], s49 offen offset:1024 sc1\n"
        "buffer_load_dwordx4 a[24:27], v232, s[40:43], s49 offen offset:2048 sc1\n"
        "buffer_load_dwordx4 a[28:31], v232, s[40:43], s49 offen offset:3072 sc1\n"
        "s_add_u32 s49, s49, 4096\n"
        "buffer_load_dwordx4 a[32:35], v232, s[40:43], s49 offen offset:0 sc1\n"
        "buffer_load_dwordx4 a[36:39], v232, s[40:43], s49 offen offset:1024 sc1\n"
        "buffer_load_dwordx4 a[40:43], v232, s[40:43], s49 offen offset:2048 sc1\n"
        "buffer_load_dwordx4 a[44:47], v232, s[40:43], s49 offen offset:3072 sc1\n"
        "s_add_u32 s49, s49, 4096\n"
        "buffer_load_dwordx4 a[48:51], v232, s[40:43], s49 offen offset:0 sc1\n"
        "buffer_load_dwordx4 a[52:55], v232, s[40:43], s49 offen offset:1024 sc1\n"
        "buffer_load_dwordx4 a[56:59], v232, s[40:43], s49 offen offset:2048 sc1\n"
        "buffer_load_dwordx4 a[60:63], v232, s[40:43], s49 offen offset:3072 sc1\n"
        "s_add_u32 s49, s49, 4096\n"
        "buffer_load_dwordx4 a[64:67], v232, s[40:43], s49 offen offset:0 sc1\n"
        "buffer_load_dwordx4 a[68:71], v232, s[40:43], s49 offen offset:1024 sc1\n"
        "buffer_load_dwordx4 a[72:75], v232, s[40:43], s49 offen offset:2048 sc1\n"
        "buffer_load_dwordx4 a[76:79], v232, s[40:43], s49 offen offset:3072 sc1\n"
        "s_add_u32 s49, s49, 4096\n"
        "buffer_load_dwordx4 a[80:83], v232, s[40:43], s49 offen offset:0 sc1\n"
        "buffer_load_dwordx4 a[84:87], v232, s[40:43], s49 offen offset:1024 sc1\n"
        "buffer_load_dwordx4 a[88:91], v232, s[40:43], s49 offen offset:2048 sc1\n"
        "buffer_load_dwordx4 a[92:95], v232, s[40:43], s49 offen offset:3072 sc1\n"
        "s_add_u32 s49, s49, 4096\n"
        "buffer_load_dwordx4 a[96:99], v232, s[40:43], s49 offen offset:0 sc1\n"
        "buffer_load_dwordx4 a[100:103], v232, s[40:43], s49 offen offset:1024 sc1\n"
        "buffer_load_dwordx4 a[104:107], v232, s[40:43], s49 offen offset:2048 sc1\n"
        "buffer_load_dwordx4 a[108:111], v232, s[40:43], s49 offen offset:3072 sc1\n"
        "s_add_u32 s49, s49, 4096\n"
        "buffer_load_dwordx4 a[112:115], v232, s[40:43], s49 offen offset:0 sc1\n"
        "buffer_load_dwordx4 a[116:119], v232, s[40:43], s49 offen offset:1024 sc1\n"
        "buffer_load_dwordx4 a[120:123], v232, s[40:43], s49 offen offset:2048 sc1\n"
        "buffer_load_dwordx4 a[124:127], v232, s[40:43], s49 offen offset:3072 sc1\n"
        "s_add_u32 s49, s49, 4096\n"
        "buffer_load_dwordx4 a[128:131], v232, s[40:43], s49 offen offset:0 sc1\n"
        "buffer_load_dwordx4 a[132:135], v232, s[40:43], s49 offen offset:1024 sc1\n"
        "buffer_load_dwordx4 a[136:139], v232, s[40:43], s49 offen offset:2048 sc1\n"
        "buffer_load_dwordx4 a[140:143], v232, s[40:43], s49 offen offset:3072 sc1\n"
        "s_add_u32 s49, s49, 114688\n"
        "s_mov_b32 s52, 256\n"
        "6:\n"
        "s_memrealtime vcc\n"
        "s_waitcnt lgkmcnt(0)\n"
        "s_sub_u32 vcc_lo, vcc_lo, %4\n"
        "s_cmp_ge_i32 vcc_lo, 0\n"
        "s_cbranch_scc1 1f\n"
        "s_sleep 1\n"
        "s_sub_u32 s52, s52, 1\n"
        "s_cbranch_scc0 6b\n"
        "1:\n"
        "v_cmp_ge_u32 vcc, s48, v254\n"
        "s_cbranch_vccz 3f\n"
        "2:\n"
        "buffer_load_dword v254, v247, s[40:43], s59 offen sc1\n"
        "s_waitcnt vmcnt(0)\n"
        "v_cmp_ge_u32 vcc, s48, v254\n"
        "s_cbranch_vccz 3f\n"
        "s_cmp_eq_u32 s55, 0\n"
        "s_cbranch_scc1 3f\n"
        "s_sub_u32 s55, s55, 1\n"
        "s_sleep 8\n"
        "s_branch 2b\n"
        "3:\n"
        "s_waitcnt vmcnt(1)\n"
        "ds_read_b64_tr_b16 v[0:1], v233 offset:0\n"
        "ds_read_b64_tr_b16 v[2:3], v233 offset:1024\n"
        "ds_read_b64_tr_b16 v[4:5], v236 offset:0\n"
        "ds_read_b64_tr_b16 v[6:7], v236 offset:1024\n"
        "ds_read_b64_tr_b16 v[8:9], v239 offset:0\n"
        "ds_read_b64_tr_b16 v[10:11], v239 offset:1024\n"
        "ds_read_b64_tr_b16 v[12:13], v242 offset:0\n"
        "ds_read_b64_tr_b16 v[14:15], v242 offset:1024\n"
        "ds_read_b64_tr_b16 v[16:17], v233 offset:4096\n"
        "ds_read_b64_tr_b16 v[18:19], v233 offset:5120\n"
        "ds_read_b64_tr_b16 v[20:21], v236 offset:4096\n"
        "ds_read_b64_tr_b16 v[22:23], v236 offset:5120\n"
        "s_add_u32 s52, s50, 0x800000+0\n"
        "buffer_load_dwordx4 a[144:147], v232, s[40:43], s52 offen offset:0 sc1\n"
        "buffer_load_dwordx4 a[148:151], v232, s[40:43], s52 offen offset:1024 sc1\n"
        "buffer_load_dwordx4 a[152:155], v232, s[40:43], s52 offen offset:2048 sc1\n"
        "buffer_load_dwordx4 a[156:159], v232, s[40:43], s52 offen offset:3072 sc1\n"
        "s_add_u32 s52, s50, 0x800000+4096\n"
        "buffer_load_dwordx4 a[160:163], v232, s[40:43], s52 offen offset:0 sc1\n"
        "buffer_load_dwordx4 a[164:167], v232, s[40:43], s52 offen offset:1024 sc1\n"
        "buffer_load_dwordx4 a[168:171], v232, s[40:43], s52 offen offset:2048 sc1\n"
        "buffer_load_dwordx4 a[172:175], v232, s[40:43], s52 offen offset:3072 sc1\n"
        "s_add_u32 s52, s50, 0x1800000+0\n"
        "buffer_load_dwordx4 a[176:179], v232, s[40:43], s52 offen offset:0 sc1\n"
        "buffer_load_dwordx4 a[180:183], v232, s[40:43], s52 offen offset:1024 sc1\n"
        "buffer_load_dwordx4 a[184:187], v232, s[40:43], s52 offen offset:2048 sc1\n"
        "buffer_load_dwordx4 a[188:191], v232, s[40:43], s52 offen offset:3072 sc1\n"
        "s_add_u32 s52, s50, 0x1800000+4096\n"
        "buffer_load_dwordx4 a[192:195], v232, s[40:43], s52 offen offset:0 sc1\n"
        "buffer_load_dwordx4 a[196:199], v232, s[40:43], s52 offen offset:1024 sc1\n"
        "buffer_load_dwordx4 a[200:203], v232, s[40:43], s52 offen offset:2048 sc1\n"
        "buffer_load_dwordx4 a[204:207], v232, s[40:43], s52 offen offset:3072 sc1\n"
        "s_waitcnt lgkmcnt(10)\n"
        "v_mfma_f32_32x32x16_bf16 v[32:47], v[0:3], a[0:3], 0\n"
        "ds_read_b64_tr_b16 v[24:25], v239 offset:4096\n"
        "ds_read_b64_tr_b16 v[26:27], v239 offset:5120\n"
        "s_waitcnt lgkmcnt(10)\n"
        "v_mfma_f32_32x32x16_bf16 v[48:63], v[4:7], a[0:3], 0\n"
        "ds_read_b64_tr_b16 v[28:29], v242 offset:4096\n"
        "ds_read_b64_tr_b16 v[30:31], v242 offset:5120\n"
        "s_waitcnt lgkmcnt(10)\n"
        "v_mfma_f32_32x32x16_bf16 v[64:79], v[8:11], a[0:3], 0\n"
        "ds_read_b64_tr_b16 v[0:1], v233 offset:8192\n"
        "ds_read_b64_tr_b16 v[2:3], v233 offset:9216\n"
        "s_waitcnt lgkmcnt(10)\n"
        "v_mfma_f32_32x32x16_bf16 v[80:95], v[12:15], a[0:3], 0\n"
        "ds_read_b64_tr_b16 v[4:5], v236 offset:8192\n"
        "ds_read_b64_tr_b16 v[6:7], v236 offset:9216\n"
        "s_waitcnt lgkmcnt(10)\n"
        "v_mfma_f32_32x32x16_bf16 v[32:47], v[16:19], a[4:7], v[32:47]\n"
        "ds_read_b64_tr_b16 v[8:9], v239 offset:8192\n"
        "ds_read_b64_tr_b16 v[10:11], v239 offset:9216\n"
        "buffer_load_dwordx4 a[0:3], v232, s[40:43], s49 offen offset:0 sc1\n"
        "s_waitcnt lgkmcnt(10)\n"
        "v_mfma_f32_32x32x16_bf16 v[48:63], v[20:23], a[4:7], v[48:63]\n"
        "ds_read_b64_tr_b16 v[12:13], v242 offset:8192\n"
        "ds_read_b64_tr_b16 v[14:15], v242 offset:9216\n"
        "s_waitcnt lgkmcnt(10)\n"
        "v_mfma_f32_32x32x16_bf16 v[64:79], v[24:27], a[4:7], v[64:79]\n"
        "ds_read_b64_tr_b16 v[16:17], v233 offset:12288\n"
        "ds_read_b64_tr_b16 v[18:19], v233 offset:13312\n"
        "s_waitcnt lgkmcnt(10)\n"
        "v_mfma_f32_32x32x16_bf16 v[80:95], v[28:31], a[4:7], v[80:95]\n"
        "ds_read_b64_tr_b16 v[20:21], v236 offset:12288\n"
        "ds_read_b64_tr_b16 v[22:23], v236 offset:13312\n"
        "s_waitcnt lgkmcnt(10)\n"
        "v_mfma_f32_32x32x16_bf16 v[32:47], v[0:3], a[8:11], v[32:47]\n"
        "ds_read_b64_tr_b16 v[24:25], v239 offset:12288\n"
        "ds_read_b64_tr_b16 v[26:27], v239 offset:13312\n"
        "buffer_load_dwordx4 a[4:7], v232, s[40:43], s49 offen offset:1024 sc1\n"
        "s_waitcnt lgkmcnt(10)\n"
        "v_mfma_f32_32x32x16_bf16 v[48:63], v[4:7], a[8:11], v[48:63]\n"
        "ds_read_b64_tr_b16 v[28:29], v242 offset:12288\n"
        "ds_read_b64_tr_b16 v[30:31], v242 offset:13312\n"
        "s_waitcnt lgkmcnt(10)\n"
        "v_mfma_f32_32x32x16_bf16 v[64:79], v[8:11], a[8:11], v[64:79]\n"
        "ds_read_b64_tr_b16 v[0:1], v233 offset:16384\n"
        "ds_read_b64_tr_b16 v[2:3], v233 offset:17408\n"
        "s_waitcnt lgkmcnt(10)\n"
        "v_mfma_f32_32x32x16_bf16 v[80:95], v[12:15], a[8:11], v[80:95]\n"
        "ds_read_b64_tr_b16 v[4:5], v236 offset:16384\n"
        "ds_read_b64_tr_b16 v[6:7], v236 offset:17408\n"
        "s_waitcnt lgkmcnt(10)\n"
        "v_mfma_f32_32x32x16_bf16 v[32:47], v[16:19], a[12:15], v[32:47]\n"
        "ds_read_b64_tr_b16 v[8:9], v239 offset:16384\n"
        "ds_read_b64_tr_b16 v[10:11], v239 offset:17408\n"
        "buffer_load_dwordx4 a[8:11], v232, s[40:43], s49 offen offset:2048 sc1\n"
        "s_waitcnt lgkmcnt(10)\n"
        "v_mfma_f32_32x32x16_bf16 v[48:63], v[20:23], a[12:15], v[48:63]\n"
        "ds_read_b64_tr_b16 v[12:13], v242 offset:16384\n"
        "ds_read_b64_tr_b16 v[14:15], v242 offset:17408\n"
        "s_waitcnt lgkmcnt(10)\n"
        "v_mfma_f32_32x32x16_bf16 v[64:79], v[24:27], a[12:15], v[64:79]\n"
        "ds_read_b64_tr_b16 v[16:17], v233 offset:20480\n"
        "ds_read_b64_tr_b16 v[18:19], v233 offset:21504\n"
        "s_waitcnt lgkmcnt(10)\n"
        "v_mfma_f32_32x32x16_bf16 v[80:95], v[28:31], a[12:15], v[80:95]\n"
        "ds_read_b64_tr_b16 v[20:21], v236 offset:20480\n"
        "ds_read_b64_tr_b16 v[22:23], v236 offset:21504\n"
        "s_waitcnt lgkmcnt(10)\n"
        "v_mfma_f32_32x32x16_bf16 v[32:47], v[0:3], a[16:19], v[32:47]\n"
        "ds_read_b64_tr_b16 v[24:25], v239 offset:20480\n"
        "ds_read_b64_tr_b16 v[26:27], v239 offset:21504\n"
        "buffer_load_dwordx4 a[12:15], v232, s[40:43], s49 offen offset:3072 sc1\n"
        "s_waitcnt lgkmcnt(10)\n"
        "v_mfma_f32_32x32x16_bf16 v[48:63], v[4:7], a[16:19], v[48:63]\n"
        "ds_read_b64_tr_b16 v[28:29], v242 offset:20480\n"
        "ds_read_b64_tr_b16 v[30:31], v242 offset:21504\n"
        "s_waitcnt lgkmcnt(10)\n"
        "v_mfma_f32_32x32x16_bf16 v[64:79], v[8:11], a[16:19], v[64:79]\n"
        "ds_read_b64_tr_b16 v[0:1], v233 offset:24576\n"
        "ds_read_b64_tr_b16 v[2:3], v233 offset:25600\n"
        "s_waitcnt lgkmcnt(10)\n"
        "v_mfma_f32_32x32x16_bf16 v[80:95], v[12:15], a[16:19], v[80:95]\n"
        "ds_read_b64_tr_b16 v[4:5], v236 offset:24576\n"
        "ds_read_b64_tr_b16 v[6:7], v236 offset:25600\n"
        "s_waitcnt lgkmcnt(10)\n"
        "v_mfma_f32_32x32x16_bf16 v[32:47], v[16:19], a[20:23], v[32:47]\n"
        "ds_read_b64_tr_b16 v[8:9], v239 offset:24576\n"
        "ds_read_b64_tr_b16 v[10:11], v239 offset:25600\n"
        "s_add_u32 s49, s49, 4096\n"
        "buffer_load_dwordx4 a[16:19], v232, s[40:43], s49 offen offset:0 sc1\n"
        "s_waitcnt lgkmcnt(10)\n"
        "v_mfma_f32_32x32x16_bf16 v[48:63], v[20:23], a[20:23], v[48:63]\n"
        "ds_read_b64_tr_b16 v[12:13], v242 offset:24576\n"
        "ds_read_b64_tr_b16 v[14:15], v242 offset:25600\n"
        "s_waitcnt lgkmcnt(10)\n"
        "v_mfma_f32_32x32x16_bf16 v[64:79], v[24:27], a[20:23], v[64:79]\n"
        "ds_read_b64_tr_b16 v[16:17], v233 offset:28672\n"
        "ds_read_b64_tr_b16 v[18:19], v233 offset:29696\n"
        "s_waitcnt lgkmcnt(10)\n"
        "v_mfma_f32_32x32x16_bf16 v[80:95], v[28:31], a[20:23], v[80:95]\n"
        "ds_read_b64_tr_b16 v[20:21], v236 offset:28672\n"
        "ds_read_b64_tr_b16 v[22:23], v236 offset:29696\n"
        "s_waitcnt lgkmcnt(10)\n"
        "v_mfma_f32_32x32x16_bf16 v[32:47], v[0:3], a[24:27], v[32:47]\n"
        "ds_read_b64_tr_b16 v[24:25], v239 offset:28672\n"
        "ds_read_b64_tr_b16 v[26:27], v239 offset:29696\n"
        "buffer_load_dwordx4 a[20:23], v232, s[40:43], s49 offen offset:1024 sc1\n"
        "s_waitcnt lgkmcnt(10)\n"
        "v_mfma_f32_32x32x16_bf16 v[48:63], v[4:7], a[24:27], v[48:63]\n"
        "ds_read_b64_tr_b16 v[28:29], v242 offset:28672\n"
        "ds_read_b64_tr_b16 v[30:31], v242 offset:29696\n"
        "s_waitcnt lgkmcnt(10)\n"
        "v_mfma_f32_32x32x16_bf16 v[64:79], v[8:11], a[24:27], v[64:79]\n"
        "ds_read_b64_tr_b16 v[0:1], v233 offset:32768\n"
        "ds_read_b64_tr_b16 v[2:3], v233 offset:33792\n"
        "s_waitcnt lgkmcnt(10)\n"
        "v_mfma_f32_32x32x16_bf16 v[80:95], v[12:15], a[24:27], v[80:95]\n"
        "ds_read_b64_tr_b16 v[4:5], v236 offset:32768\n"
        "ds_read_b64_tr_b16 v[6:7], v236 offset:33792\n"
        "s_waitcnt lgkmcnt(10)\n"
        "v_mfma_f32_32x32x16_bf16 v[32:47], v[16:19], a[28:31], v[32:47]\n"
        "ds_read_b64_tr_b16 v[8:9], v239 offset:32768\n"
        "ds_read_b64_tr_b16 v[10:11], v239 offset:33792\n"
        "buffer_load_dwordx4 a[24:27], v232, s[40:43], s49 offen offset:2048 sc1\n"
        "s_waitcnt lgkmcnt(10)\n"
        "v_mfma_f32_32x32x16_bf16 v[48:63], v[20:23], a[28:31], v[48:63]\n"
        "ds_read_b64_tr_b16 v[12:13], v242 offset:32768\n"
        "ds_read_b64_tr_b16 v[14:15], v242 offset:33792\n"
        "s_waitcnt lgkmcnt(10)\n"
        "v_mfma_f32_32x32x16_bf16 v[64:79], v[24:27], a[28:31], v[64:79]\n"
        "ds_read_b64_tr_b16 v[16:17], v233 offset:36864\n"
        "ds_read_b64_tr_b16 v[18:19], v233 offset:37888\n"
        "s_waitcnt lgkmcnt(10)\n"
        "v_mfma_f32_32x32x16_bf16 v[80:95], v[28:31], a[28:31], v[80:95]\n"
        "ds_read_b64_tr_b16 v[20:21], v236 offset:36864\n"
        "ds_read_b64_tr_b16 v[22:23], v236 offset:37888\n"
        "s_waitcnt lgkmcnt(10)\n"
        "v_mfma_f32_32x32x16_bf16 v[32:47], v[0:3], a[32:35], v[32:47]\n"
        "ds_read_b64_tr_b16 v[24:25], v239 offset:36864\n"
        "ds_read_b64_tr_b16 v[26:27], v239 offset:37888\n"
        "buffer_load_dwordx4 a[28:31], v232, s[40:43], s49 offen offset:3072 sc1\n"
        "s_waitcnt lgkmcnt(10)\n"
        "v_mfma_f32_32x32x16_bf16 v[48:63], v[4:7], a[32:35], v[48:63]\n"
        "ds_read_b64_tr_b16 v[28:29], v242 offset:36864\n"
        "ds_read_b64_tr_b16 v[30:31], v242 offset:37888\n"
        "s_waitcnt lgkmcnt(10)\n"
        "v_mfma_f32_32x32x16_bf16 v[64:79], v[8:11], a[32:35], v[64:79]\n"
        "ds_read_b64_tr_b16 v[0:1], v233 offset:40960\n"
        "ds_read_b64_tr_b16 v[2:3], v233 offset:41984\n"
        "s_waitcnt lgkmcnt(10)\n"
        "v_mfma_f32_32x32x16_bf16 v[80:95], v[12:15], a[32:35], v[80:95]\n"
        "ds_read_b64_tr_b16 v[4:5], v236 offset:40960\n"
        "ds_read_b64_tr_b16 v[6:7], v236 offset:41984\n"
        "s_waitcnt lgkmcnt(10)\n"
        "v_mfma_f32_32x32x16_bf16 v[32:47], v[16:19], a[36:39], v[32:47]\n"
        "ds_read_b64_tr_b16 v[8:9], v239 offset:40960\n"
        "ds_read_b64_tr_b16 v[10:11], v239 offset:41984\n"
        "s_add_u32 s49, s49, 4096\n"
        "buffer_load_dwordx4 a[32:35], v232, s[40:43], s49 offen offset:0 sc1\n"
        "s_waitcnt lgkmcnt(10)\n"
        "v_mfma_f32_32x32x16_bf16 v[48:63], v[20:23], a[36:39], v[48:63]\n"
        "ds_read_b64_tr_b16 v[12:13], v242 offset:40960\n"
        "ds_read_b64_tr_b16 v[14:15], v242 offset:41984\n"
        "s_waitcnt lgkmcnt(10)\n"
        "v_mfma_f32_32x32x16_bf16 v[64:79], v[24:27], a[36:39], v[64:79]\n"
        "ds_read_b64_tr_b16 v[16:17], v233 offset:45056\n"
        "ds_read_b64_tr_b16 v[18:19], v233 offset:46080\n"
        "s_waitcnt lgkmcnt(10)\n"
        "v_mfma_f32_32x32x16_bf16 v[80:95], v[28:31], a[36:39], v[80:95]\n"
        "ds_read_b64_tr_b16 v[20:21], v236 offset:45056\n"
        "ds_read_b64_tr_b16 v[22:23], v236 offset:46080\n"
        "s_waitcnt lgkmcnt(10)\n"
        "v_mfma_f32_32x32x16_bf16 v[32:47], v[0:3], a[40:43], v[32:47]\n"
        "ds_read_b64_tr_b16 v[24:25], v239 offset:45056\n"
        "ds_read_b64_tr_b16 v[26:27], v239 offset:46080\n"
        "buffer_load_dwordx4 a[36:39], v232, s[40:43], s49 offen offset:1024 sc1\n"
        "s_waitcnt lgkmcnt(10)\n"
        "v_mfma_f32_32x32x16_bf16 v[48:63], v[4:7], a[40:43], v[48:63]\n"
        "ds_read_b64_tr_b16 v[28:29], v242 offset:45056\n"
        "ds_read_b64_tr_b16 v[30:31], v242 offset:46080\n"
        "s_waitcnt lgkmcnt(10)\n"
        "v_mfma_f32_32x32x16_bf16 v[64:79], v[8:11], a[40:43], v[64:79]\n"
        "ds_read_b64_tr_b16 v[0:1], v233 offset:49152\n"
        "ds_read_b64_tr_b16 v[2:3], v233 offset:50176\n"
        "s_waitcnt lgkmcnt(10)\n"
        "v_mfma_f32_32x32x16_bf16 v[80:95], v[12:15], a[40:43], v[80:95]\n"
        "ds_read_b64_tr_b16 v[4:5], v236 offset:49152\n"
        "ds_read_b64_tr_b16 v[6:7], v236 offset:50176\n"
        "s_waitcnt lgkmcnt(10)\n"
        "v_mfma_f32_32x32x16_bf16 v[32:47], v[16:19], a[44:47], v[32:47]\n"
        "ds_read_b64_tr_b16 v[8:9], v239 offset:49152\n"
        "ds_read_b64_tr_b16 v[10:11], v239 offset:50176\n"
        "buffer_load_dwordx4 a[40:43], v232, s[40:43], s49 offen offset:2048 sc1\n"
        "s_waitcnt lgkmcnt(10)\n"
        "v_mfma_f32_32x32x16_bf16 v[48:63], v[20:23], a[44:47], v[48:63]\n"
        "ds_read_b64_tr_b16 v[12:13], v242 offset:49152\n"
        "ds_read_b64_tr_b16 v[14:15], v242 offset:50176\n"
        "s_waitcnt lgkmcnt(10)\n"
        "v_mfma_f32_32x32x16_bf16 v[64:79], v[24:27], a[44:47], v[64:79]\n"
        "ds_read_b64_tr_b16 v[16:17], v233 offset:53248\n"
        "ds_read_b64_tr_b16 v[18:19], v233 offset:54272\n"
        "s_waitcnt lgkmcnt(10)\n"
        "v_mfma_f32_32x32x16_bf16 v[80:95], v[28:31], a[44:47], v[80:95]\n"
        "ds_read_b64_tr_b16 v[20:21], v236 offset:53248\n"
        "ds_read_b64_tr_b16 v[22:23], v236 offset:54272\n"
        "s_waitcnt lgkmcnt(10)\n"
        "v_mfma_f32_32x32x16_bf16 v[32:47], v[0:3], a[48:51], v[32:47]\n"
        "ds_read_b64_tr_b16 v[24:25], v239 offset:53248\n"
        "ds_read_b64_tr_b16 v[26:27], v239 offset:54272\n"
        "buffer_load_dwordx4 a[44:47], v232, s[40:43], s49 offen offset:3072 sc1\n"
        "s_waitcnt lgkmcnt(10)\n"
        "v_mfma_f32_32x32x16_bf16 v[48:63], v[4:7], a[48:51], v[48:63]\n"
        "ds_read_b64_tr_b16 v[28:29], v242 offset:53248\n"
        "ds_read_b64_tr_b16 v[30:31], v242 offset:54272\n"
        "s_waitcnt lgkmcnt(10)\n"
        "v_mfma_f32_32x32x16_bf16 v[64:79], v[8:11], a[48:51], v[64:79]\n"
        "ds_read_b64_tr_b16 v[0:1], v233 offset:57344\n"
        "ds_read_b64_tr_b16 v[2:3], v233 offset:58368\n"
        "s_waitcnt lgkmcnt(10)\n"
        "v_mfma_f32_32x32x16_bf16 v[80:95], v[12:15], a[48:51], v[80:95]\n"
        "ds_read_b64_tr_b16 v[4:5], v236 offset:57344\n"
        "ds_read_b64_tr_b16 v[6:7], v236 offset:58368\n"
        "s_waitcnt lgkmcnt(10)\n"
        "v_mfma_f32_32x32x16_bf16 v[32:47], v[16:19], a[52:55], v[32:47]\n"
        "ds_read_b64_tr_b16 v[8:9], v239 offset:57344\n"
        "ds_read_b64_tr_b16 v[10:11], v239 offset:58368\n"
        "s_add_u32 s49, s49, 4096\n"
        "buffer_load_dwordx4 a[48:51], v232, s[40:43], s49 offen offset:0 sc1\n"
        "s_waitcnt lgkmcnt(10)\n"
        "v_mfma_f32_32x32x16_bf16 v[48:63], v[20:23], a[52:55], v[48:63]\n"
        "ds_read_b64_tr_b16 v[12:13], v242 offset:57344\n"
        "ds_read_b64_tr_b16 v[14:15], v242 offset:58368\n"
        "s_waitcnt lgkmcnt(10)\n"
        "v_mfma_f32_32x32x16_bf16 v[64:79], v[24:27], a[52:55], v[64:79]\n"
        "ds_read_b64_tr_b16 v[16:17], v233 offset:61440\n"
        "ds_read_b64_tr_b16 v[18:19], v233 offset:62464\n"
        "s_waitcnt lgkmcnt(10)\n"
        "v_mfma_f32_32x32x16_bf16 v[80:95], v[28:31], a[52:55], v[80:95]\n"
        "ds_read_b64_tr_b16 v[20:21], v236 offset:61440\n"
        "ds_read_b64_tr_b16 v[22:23], v236 offset:62464\n"
        "s_waitcnt lgkmcnt(10)\n"
        "v_mfma_f32_32x32x16_bf16 v[32:47], v[0:3], a[56:59], v[32:47]\n"
        "ds_read_b64_tr_b16 v[24:25], v239 offset:61440\n"
        "ds_read_b64_tr_b16 v[26:27], v239 offset:62464\n"
        "buffer_load_dwordx4 a[52:55], v232, s[40:43], s49 offen offset:1024 sc1\n"
        "s_waitcnt lgkmcnt(10)\n"
        "v_mfma_f32_32x32x16_bf16 v[48:63], v[4:7], a[56:59], v[48:63]\n"
        "ds_read_b64_tr_b16 v[28:29], v242 offset:61440\n"
        "ds_read_b64_tr_b16 v[30:31], v242 offset:62464\n"
        "s_waitcnt lgkmcnt(10)\n"
        "v_mfma_f32_32x32x16_bf16 v[64:79], v[8:11], a[56:59], v[64:79]\n"
        "ds_read_b64_tr_b16 v[0:1], v234 offset:0\n"
        "ds_read_b64_tr_b16 v[2:3], v234 offset:1024\n"
        "s_waitcnt lgkmcnt(10)\n"
        "v_mfma_f32_32x32x16_bf16 v[80:95], v[12:15], a[56:59], v[80:95]\n"
        "ds_read_b64_tr_b16 v[4:5], v237 offset:0\n"
        "ds_read_b64_tr_b16 v[6:7], v237 offset:1024\n"
        "s_waitcnt lgkmcnt(10)\n"
        "v_mfma_f32_32x32x16_bf16 v[32:47], v[16:19], a[60:63], v[32:47]\n"
        "ds_read_b64_tr_b16 v[8:9], v240 offset:0\n"
        "ds_read_b64_tr_b16 v[10:11], v240 offset:1024\n"
        "buffer_load_dwordx4 a[56:59], v232, s[40:43], s49 offen offset:2048 sc1\n"
        "s_waitcnt lgkmcnt(10)\n"
        "v_mfma_f32_32x32x16_bf16 v[48:63], v[20:23], a[60:63], v[48:63]\n"
        "ds_read_b64_tr_b16 v[12:13], v243 offset:0\n"
        "ds_read_b64_tr_b16 v[14:15], v243 offset:1024\n"
        "s_waitcnt lgkmcnt(10)\n"
        "v_mfma_f32_32x32x16_bf16 v[64:79], v[24:27], a[60:63], v[64:79]\n"
        "ds_read_b64_tr_b16 v[16:17], v234 offset:4096\n"
        "ds_read_b64_tr_b16 v[18:19], v234 offset:5120\n"
        "s_waitcnt lgkmcnt(10)\n"
        "v_mfma_f32_32x32x16_bf16 v[80:95], v[28:31], a[60:63], v[80:95]\n"
        "ds_read_b64_tr_b16 v[20:21], v237 offset:4096\n"
        "ds_read_b64_tr_b16 v[22:23], v237 offset:5120\n"
        "s_waitcnt lgkmcnt(10)\n"
        "v_mfma_f32_32x32x16_bf16 v[32:47], v[0:3], a[64:67], v[32:47]\n"
        "ds_read_b64_tr_b16 v[24:25], v240 offset:4096\n"
        "ds_read_b64_tr_b16 v[26:27], v240 offset:5120\n"
        "buffer_load_dwordx4 a[60:63], v232, s[40:43], s49 offen offset:3072 sc1\n"
        "s_waitcnt lgkmcnt(10)\n"
        "v_mfma_f32_32x32x16_bf16 v[48:63], v[4:7], a[64:67], v[48:63]\n"
        "ds_read_b64_tr_b16 v[28:29], v243 offset:4096\n"
        "ds_read_b64_tr_b16 v[30:31], v243 offset:5120\n"
        "s_waitcnt lgkmcnt(10)\n"
        "v_mfma_f32_32x32x16_bf16 v[64:79], v[8:11], a[64:67], v[64:79]\n"
        "ds_read_b64_tr_b16 v[0:1], v234 offset:8192\n"
        "ds_read_b64_tr_b16 v[2:3], v234 offset:9216\n"
        "s_waitcnt lgkmcnt(10)\n"
        "v_mfma_f32_32x32x16_bf16 v[80:95], v[12:15], a[64:67], v[80:95]\n"
        "ds_read_b64_tr_b16 v[4:5], v237 offset:8192\n"
        "ds_read_b64_tr_b16 v[6:7], v237 offset:9216\n"
        "s_waitcnt lgkmcnt(10)\n"
        "v_mfma_f32_32x32x16_bf16 v[32:47], v[16:19], a[68:71], v[32:47]\n"
        "ds_read_b64_tr_b16 v[8:9], v240 offset:8192\n"
        "ds_read_b64_tr_b16 v[10:11], v240 offset:9216\n"
        "s_add_u32 s49, s49, 4096\n"
        "buffer_load_dwordx4 a[64:67], v232, s[40:43], s49 offen offset:0 sc1\n"
        "s_waitcnt lgkmcnt(10)\n"
        "v_mfma_f32_32x32x16_bf16 v[48:63], v[20:23], a[68:71], v[48:63]\n"
        "ds_read_b64_tr_b16 v[12:13], v243 offset:8192\n"
        "ds_read_b64_tr_b16 v[14:15], v243 offset:9216\n"
        "s_waitcnt lgkmcnt(10)\n"
        "v_mfma_f32_32x32x16_bf16 v[64:79], v[24:27], a[68:71], v[64:79]\n"
        "ds_read_b64_tr_b16 v[16:17], v234 offset:12288\n"
        "ds_read_b64_tr_b16 v[18:19], v234 offset:13312\n"
        "s_waitcnt lgkmcnt(10)\n"
        "v_mfma_f32_32x32x16_bf16 v[80:95], v[28:31], a[68:71], v[80:95]\n"
        "ds_read_b64_tr_b16 v[20:21], v237 offset:12288\n"
        "ds_read_b64_tr_b16 v[22:23], v237 offset:13312\n"
        "s_waitcnt lgkmcnt(10)\n"
        "v_mfma_f32_32x32x16_bf16 v[32:47], v[0:3], a[72:75], v[32:47]\n"
        "ds_read_b64_tr_b16 v[24:25], v240 offset:12288\n"
        "ds_read_b64_tr_b16 v[26:27], v240 offset:13312\n"
        "buffer_load_dwordx4 a[68:71], v232, s[40:43], s49 offen offset:1024 sc1\n"
        "s_waitcnt lgkmcnt(10)\n"
        "v_mfma_f32_32x32x16_bf16 v[48:63], v[4:7], a[72:75], v[48:63]\n"
        "ds_read_b64_tr_b16 v[28:29], v243 offset:12288\n"
        "ds_read_b64_tr_b16 v[30:31], v243 offset:13312\n"
        "s_waitcnt lgkmcnt(10)\n"
        "v_mfma_f32_32x32x16_bf16 v[64:79], v[8:11], a[72:75], v[64:79]\n"
        "ds_read_b64_tr_b16 v[0:1], v234 offset:16384\n"
        "ds_read_b64_tr_b16 v[2:3], v234 offset:17408\n"
        "s_waitcnt lgkmcnt(10)\n"
        "v_mfma_f32_32x32x16_bf16 v[80:95], v[12:15], a[72:75], v[80:95]\n"
        "ds_read_b64_tr_b16 v[4:5], v237 offset:16384\n"
        "ds_read_b64_tr_b16 v[6:7], v237 offset:17408\n"
        "s_waitcnt lgkmcnt(10)\n"
        "v_mfma_f32_32x32x16_bf16 v[32:47], v[16:19], a[76:79], v[32:47]\n"
        "ds_read_b64_tr_b16 v[8:9], v240 offset:16384\n"
        "ds_read_b64_tr_b16 v[10:11], v240 offset:17408\n"
        "buffer_load_dwordx4 a[72:75], v232, s[40:43], s49 offen offset:2048 sc1\n"
        "s_waitcnt lgkmcnt(10)\n"
        "v_mfma_f32_32x32x16_bf16 v[48:63], v[20:23], a[76:79], v[48:63]\n"
        "ds_read_b64_tr_b16 v[12:13], v243 offset:16384\n"
        "ds_read_b64_tr_b16 v[14:15], v243 offset:17408\n"
        "s_waitcnt lgkmcnt(10)\n"
        "v_mfma_f32_32x32x16_bf16 v[64:79], v[24:27], a[76:79], v[64:79]\n"
        "ds_read_b64_tr_b16 v[16:17], v234 offset:20480\n"
        "ds_read_b64_tr_b16 v[18:19], v234 offset:21504\n"
        "s_waitcnt lgkmcnt(10)\n"
        "v_mfma_f32_32x32x16_bf16 v[80:95], v[28:31], a[76:79], v[80:95]\n"
        "ds_read_b64_tr_b16 v[20:21], v237 offset:20480\n"
        "ds_read_b64_tr_b16 v[22:23], v237 offset:21504\n"
        "s_waitcnt lgkmcnt(10)\n"
        "v_mfma_f32_32x32x16_bf16 v[32:47], v[0:3], a[80:83], v[32:47]\n"
        "ds_read_b64_tr_b16 v[24:25], v240 offset:20480\n"
        "ds_read_b64_tr_b16 v[26:27], v240 offset:21504\n"
        "buffer_load_dwordx4 a[76:79], v232, s[40:43], s49 offen offset:3072 sc1\n"
        "s_waitcnt lgkmcnt(10)\n"
        "v_mfma_f32_32x32x16_bf16 v[48:63], v[4:7], a[80:83], v[48:63]\n"
        "ds_read_b64_tr_b16 v[28:29], v243 offset:20480\n"
        "ds_read_b64_tr_b16 v[30:31], v243 offset:21504\n"
        "s_waitcnt lgkmcnt(10)\n"
        "v_mfma_f32_32x32x16_bf16 v[64:79], v[8:11], a[80:83], v[64:79]\n"
        "ds_read_b64_tr_b16 v[0:1], v234 offset:24576\n"
        "ds_read_b64_tr_b16 v[2:3], v234 offset:25600\n"
        "s_waitcnt lgkmcnt(10)\n"
        "v_mfma_f32_32x32x16_bf16 v[80:95], v[12:15], a[80:83], v[80:95]\n"
        "ds_read_b64_tr_b16 v[4:5], v237 offset:24576\n"
        "ds_read_b64_tr_b16 v[6:7], v237 offset:25600\n"
        "s_waitcnt lgkmcnt(10)\n"
        "v_mfma_f32_32x32x16_bf16 v[32:47], v[16:19], a[84:87], v[32:47]\n"
        "ds_read_b64_tr_b16 v[8:9], v240 offset:24576\n"
        "ds_read_b64_tr_b16 v[10:11], v240 offset:25600\n"
        "s_add_u32 s49, s49, 4096\n"
        "buffer_load_dwordx4 a[80:83], v232, s[40:43], s49 offen offset:0 sc1\n"
        "s_waitcnt lgkmcnt(10)\n"
        "v_mfma_f32_32x32x16_bf16 v[48:63], v[20:23], a[84:87], v[48:63]\n"
        "ds_read_b64_tr_b16 v[12:13], v243 offset:24576\n"
        "ds_read_b64_tr_b16 v[14:15], v243 offset:25600\n"
        "s_waitcnt lgkmcnt(10)\n"
        "v_mfma_f32_32x32x16_bf16 v[64:79], v[24:27], a[84:87], v[64:79]\n"
        "ds_read_b64_tr_b16 v[16:17], v234 offset:28672\n"
        "ds_read_b64_tr_b16 v[18:19], v234 offset:29696\n"
        "s_waitcnt lgkmcnt(10)\n"
        "v_mfma_f32_32x32x16_bf16 v[80:95], v[28:31], a[84:87], v[80:95]\n"
        "ds_read_b64_tr_b16 v[20:21], v237 offset:28672\n"
        "ds_read_b64_tr_b16 v[22:23], v237 offset:29696\n"
        "s_waitcnt lgkmcnt(10)\n"
        "v_mfma_f32_32x32x16_bf16 v[32:47], v[0:3], a[88:91], v[32:47]\n"
        "ds_read_b64_tr_b16 v[24:25], v240 offset:28672\n"
        "ds_read_b64_tr_b16 v[26:27], v240 offset:29696\n"
        "buffer_load_dwordx4 a[84:87], v232, s[40:43], s49 offen offset:1024 sc1\n"
        "s_waitcnt lgkmcnt(10)\n"
        "v_mfma_f32_32x32x16_bf16 v[48:63], v[4:7], a[88:91], v[48:63]\n"
        "ds_read_b64_tr_b16 v[28:29], v243 offset:28672\n"
        "ds_read_b64_tr_b16 v[30:31], v243 offset:29696\n"
        "s_waitcnt lgkmcnt(10)\n"
        "v_mfma_f32_32x32x16_bf16 v[64:79], v[8:11], a[88:91], v[64:79]\n"
        "ds_read_b64_tr_b16 v[0:1], v234 offset:32768\n"
        "ds_read_b64_tr_b16 v[2:3], v234 offset:33792\n"
        "s_waitcnt lgkmcnt(10)\n"
        "v_mfma_f32_32x32x16_bf16 v[80:95], v[12:15], a[88:91], v[80:95]\n"
        "ds_read_b64_tr_b16 v[4:5], v237 offset:32768\n"
        "ds_read_b64_tr_b16 v[6:7], v237 offset:33792\n"
        "s_waitcnt lgkmcnt(10)\n"
        "v_mfma_f32_32x32x16_bf16 v[32:47], v[16:19], a[92:95], v[32:47]\n"
        "ds_read_b64_tr_b16 v[8:9], v240 offset:32768\n"
        "ds_read_b64_tr_b16 v[10:11], v240 offset:33792\n"
        "buffer_load_dwordx4 a[88:91], v232, s[40:43], s49 offen offset:2048 sc1\n"
        "s_waitcnt lgkmcnt(10)\n"
        "v_mfma_f32_32x32x16_bf16 v[48:63], v[20:23], a[92:95], v[48:63]\n"
        "ds_read_b64_tr_b16 v[12:13], v243 offset:32768\n"
        "ds_read_b64_tr_b16 v[14:15], v243 offset:33792\n"
        "s_waitcnt lgkmcnt(10)\n"
        "v_mfma_f32_32x32x16_bf16 v[64:79], v[24:27], a[92:95], v[64:79]\n"
        "ds_read_b64_tr_b16 v[16:17], v234 offset:36864\n"
        "ds_read_b64_tr_b16 v[18:19], v234 offset:37888\n"
        "s_waitcnt lgkmcnt(10)\n"
        "v_mfma_f32_32x32x16_bf16 v[80:95], v[28:31], a[92:95], v[80:95]\n"
        "ds_read_b64_tr_b16 v[20:21], v237 offset:36864\n"
        "ds_read_b64_tr_b16 v[22:23], v237 offset:37888\n"
        "s_waitcnt lgkmcnt(10)\n"
        "v_mfma_f32_32x32x16_bf16 v[32:47], v[0:3], a[96:99], v[32:47]\n"
        "ds_read_b64_tr_b16 v[24:25], v240 offset:36864\n"
        "ds_read_b64_tr_b16 v[26:27], v240 offset:37888\n"
        "buffer_load_dwordx4 a[92:95], v232, s[40:43], s49 offen offset:3072 sc1\n"
        "s_waitcnt lgkmcnt(10)\n"
        "v_mfma_f32_32x32x16_bf16 v[48:63], v[4:7], a[96:99], v[48:63]\n"
        "ds_read_b64_tr_b16 v[28:29], v243 offset:36864\n"
        "ds_read_b64_tr_b16 v[30:31], v243 offset:37888\n"
        "s_waitcnt lgkmcnt(10)\n"
        "v_mfma_f32_32x32x16_bf16 v[64:79], v[8:11], a[96:99], v[64:79]\n"
        "ds_read_b64_tr_b16 v[0:1], v234 offset:40960\n"
        "ds_read_b64_tr_b16 v[2:3], v234 offset:41984\n"
        "s_waitcnt lgkmcnt(10)\n"
        "v_mfma_f32_32x32x16_bf16 v[80:95], v[12:15], a[96:99], v[80:95]\n"
        "ds_read_b64_tr_b16 v[4:5], v237 offset:40960\n"
        "ds_read_b64_tr_b16 v[6:7], v237 offset:41984\n"
        "s_waitcnt lgkmcnt(10)\n"
        "v_mfma_f32_32x32x16_bf16 v[32:47], v[16:19], a[100:103], v[32:47]\n"
        "ds_read_b64_tr_b16 v[8:9], v240 offset:40960\n"
        "ds_read_b64_tr_b16 v[10:11], v240 offset:41984\n"
        "s_add_u32 s49, s49, 4096\n"
        "buffer_load_dwordx4 a[96:99], v232, s[40:43], s49 offen offset:0 sc1\n"
        "s_waitcnt lgkmcnt(10)\n"
        "v_mfma_f32_32x32x16_bf16 v[48:63], v[20:23], a[100:103], v[48:63]\n"
        "ds_read_b64_tr_b16 v[12:13], v243 offset:40960\n"
        "ds_read_b64_tr_b16 v[14:15], v243 offset:41984\n"
        "s_waitcnt lgkmcnt(10)\n"
        "v_mfma_f32_32x32x16_bf16 v[64:79], v[24:27], a[100:103], v[64:79]\n"
        "ds_read_b64_tr_b16 v[16:17], v234 offset:45056\n"
        "ds_read_b64_tr_b16 v[18:19], v234 offset:46080\n"
        "s_waitcnt lgkmcnt(10)\n"
        "v_mfma_f32_32x32x16_bf16 v[80:95], v[28:31], a[100:103], v[80:95]\n"
        "ds_read_b64_tr_b16 v[20:21], v237 offset:45056\n"
        "ds_read_b64_tr_b16 v[22:23], v237 offset:46080\n"
        "s_waitcnt lgkmcnt(10)\n"
        "v_mfma_f32_32x32x16_bf16 v[32:47], v[0:3], a[104:107], v[32:47]\n"
        "ds_read_b64_tr_b16 v[24:25], v240 offset:45056\n"
        "ds_read_b64_tr_b16 v[26:27], v240 offset:46080\n"
        "buffer_load_dwordx4 a[100:103], v232, s[40:43], s49 offen offset:1024 sc1\n"
        "s_waitcnt lgkmcnt(10)\n"
        "v_mfma_f32_32x32x16_bf16 v[48:63], v[4:7], a[104:107], v[48:63]\n"
        "ds_read_b64_tr_b16 v[28:29], v243 offset:45056\n"
        "ds_read_b64_tr_b16 v[30:31], v243 offset:46080\n"
        "s_waitcnt lgkmcnt(10)\n"
        "v_mfma_f32_32x32x16_bf16 v[64:79], v[8:11], a[104:107], v[64:79]\n"
        "ds_read_b64_tr_b16 v[0:1], v234 offset:49152\n"
        "ds_read_b64_tr_b16 v[2:3], v234 offset:50176\n"
        "s_waitcnt lgkmcnt(10)\n"
        "v_mfma_f32_32x32x16_bf16 v[80:95], v[12:15], a[104:107], v[80:95]\n"
        "ds_read_b64_tr_b16 v[4:5], v237 offset:49152\n"
        "ds_read_b64_tr_b16 v[6:7], v237 offset:50176\n"
        "s_waitcnt lgkmcnt(10)\n"
        "v_mfma_f32_32x32x16_bf16 v[32:47], v[16:19], a[108:111], v[32:47]\n"
        "ds_read_b64_tr_b16 v[8:9], v240 offset:49152\n"
        "ds_read_b64_tr_b16 v[10:11], v240 offset:50176\n"
        "buffer_load_dwordx4 a[104:107], v232, s[40:43], s49 offen offset:2048 sc1\n"
        "s_waitcnt lgkmcnt(10)\n"
        "v_mfma_f32_32x32x16_bf16 v[48:63], v[20:23], a[108:111], v[48:63]\n"
        "ds_read_b64_tr_b16 v[12:13], v243 offset:49152\n"
        "ds_read_b64_tr_b16 v[14:15], v243 offset:50176\n"
        "s_waitcnt lgkmcnt(10)\n"
        "v_mfma_f32_32x32x16_bf16 v[64:79], v[24:27], a[108:111], v[64:79]\n"
        "ds_read_b64_tr_b16 v[16:17], v234 offset:53248\n"
        "ds_read_b64_tr_b16 v[18:19], v234 offset:54272\n"
        "s_waitcnt lgkmcnt(10)\n"
        "v_mfma_f32_32x32x16_bf16 v[80:95], v[28:31], a[108:111], v[80:95]\n"
        "ds_read_b64_tr_b16 v[20:21], v237 offset:53248\n"
        "ds_read_b64_tr_b16 v[22:23], v237 offset:54272\n"
        "s_waitcnt lgkmcnt(10)\n"
        "v_mfma_f32_32x32x16_bf16 v[32:47], v[0:3], a[112:115], v[32:47]\n"
        "ds_read_b64_tr_b16 v[24:25], v240 offset:53248\n"
        "ds_read_b64_tr_b16 v[26:27], v240 offset:54272\n"
        "buffer_load_dwordx4 a[108:111], v232, s[40:43], s49 offen offset:3072 sc1\n"
        "s_waitcnt lgkmcnt(10)\n"
        "v_mfma_f32_32x32x16_bf16 v[48:63], v[4:7], a[112:115], v[48:63]\n"
        "ds_read_b64_tr_b16 v[28:29], v243 offset:53248\n"
        "ds_read_b64_tr_b16 v[30:31], v243 offset:54272\n"
        "s_waitcnt lgkmcnt(10)\n"
        "v_mfma_f32_32x32x16_bf16 v[64:79], v[8:11], a[112:115], v[64:79]\n"
        "ds_read_b64_tr_b16 v[0:1], v234 offset:57344\n"
        "ds_read_b64_tr_b16 v[2:3], v234 offset:58368\n"
        "s_waitcnt lgkmcnt(10)\n"
        "v_mfma_f32_32x32x16_bf16 v[80:95], v[12:15], a[112:115], v[80:95]\n"
        "ds_read_b64_tr_b16 v[4:5], v237 offset:57344\n"
        "ds_read_b64_tr_b16 v[6:7], v237 offset:58368\n"
        "s_waitcnt lgkmcnt(10)\n"
        "v_mfma_f32_32x32x16_bf16 v[32:47], v[16:19], a[116:119], v[32:47]\n"
        "ds_read_b64_tr_b16 v[8:9], v240 offset:57344\n"
        "ds_read_b64_tr_b16 v[10:11], v240 offset:58368\n"
        "s_add_u32 s49, s49, 4096\n"
        "buffer_load_dwordx4 a[112:115], v232, s[40:43], s49 offen offset:0 sc1\n"
        "s_waitcnt lgkmcnt(10)\n"
        "v_mfma_f32_32x32x16_bf16 v[48:63], v[20:23], a[116:119], v[48:63]\n"
        "ds_read_b64_tr_b16 v[12:13], v243 offset:57344\n"
        "ds_read_b64_tr_b16 v[14:15], v243 offset:58368\n"
        "s_waitcnt lgkmcnt(10)\n"
        "v_mfma_f32_32x32x16_bf16 v[64:79], v[24:27], a[116:119], v[64:79]\n"
        "ds_read_b64_tr_b16 v[16:17], v234 offset:61440\n"
        "ds_read_b64_tr_b16 v[18:19], v234 offset:62464\n"
        "s_waitcnt lgkmcnt(10)\n"
        "v_mfma_f32_32x32x16_bf16 v[80:95], v[28:31], a[116:119], v[80:95]\n"
        "ds_read_b64_tr_b16 v[20:21], v237 offset:61440\n"
        "ds_read_b64_tr_b16 v[22:23], v237 offset:62464\n"
        "s_waitcnt lgkmcnt(10)\n"
        "v_mfma_f32_32x32x16_bf16 v[32:47], v[0:3], a[120:123], v[32:47]\n"
        "ds_read_b64_tr_b16 v[24:25], v240 offset:61440\n"
        "ds_read_b64_tr_b16 v[26:27], v240 offset:62464\n"
        "buffer_load_dwordx4 a[116:119], v232, s[40:43], s49 offen offset:1024 sc1\n"
        "s_waitcnt lgkmcnt(10)\n"
        "v_mfma_f32_32x32x16_bf16 v[48:63], v[4:7], a[120:123], v[48:63]\n"
        "ds_read_b64_tr_b16 v[28:29], v243 offset:61440\n"
        "ds_read_b64_tr_b16 v[30:31], v243 offset:62464\n"
        "s_waitcnt lgkmcnt(10)\n"
        "v_mfma_f32_32x32x16_bf16 v[64:79], v[8:11], a[120:123], v[64:79]\n"
        "ds_read_b64_tr_b16 v[0:1], v235 offset:0\n"
        "ds_read_b64_tr_b16 v[2:3], v235 offset:1024\n"
        "s_waitcnt lgkmcnt(10)\n"
        "v_mfma_f32_32x32x16_bf16 v[80:95], v[12:15], a[120:123], v[80:95]\n"
        "ds_read_b64_tr_b16 v[4:5], v238 offset:0\n"
        "ds_read_b64_tr_b16 v[6:7], v238 offset:1024\n"
        "s_waitcnt lgkmcnt(10)\n"
        "v_mfma_f32_32x32x16_bf16 v[32:47], v[16:19], a[124:127], v[32:47]\n"
        "ds_read_b64_tr_b16 v[8:9], v241 offset:0\n"
        "ds_read_b64_tr_b16 v[10:11], v241 offset:1024\n"
        "buffer_load_dwordx4 a[120:123], v232, s[40:43], s49 offen offset:2048 sc1\n"
        "s_waitcnt lgkmcnt(10)\n"
        "v_mfma_f32_32x32x16_bf16 v[48:63], v[20:23], a[124:127], v[48:63]\n"
        "ds_read_b64_tr_b16 v[12:13], v244 offset:0\n"
        "ds_read_b64_tr_b16 v[14:15], v244 offset:1024\n"
        "s_waitcnt lgkmcnt(10)\n"
        "v_mfma_f32_32x32x16_bf16 v[64:79], v[24:27], a[124:127], v[64:79]\n"
        "ds_read_b64_tr_b16 v[16:17], v235 offset:4096\n"
        "ds_read_b64_tr_b16 v[18:19], v235 offset:5120\n"
        "s_waitcnt lgkmcnt(10)\n"
        "v_mfma_f32_32x32x16_bf16 v[80:95], v[28:31], a[124:127], v[80:95]\n"
        "ds_read_b64_tr_b16 v[20:21], v238 offset:4096\n"
        "ds_read_b64_tr_b16 v[22:23], v238 offset:5120\n"
        "s_waitcnt lgkmcnt(10)\n"
        "v_mfma_f32_32x32x16_bf16 v[32:47], v[0:3], a[128:131], v[32:47]\n"
        "ds_read_b64_tr_b16 v[24:25], v241 offset:4096\n"
        "ds_read_b64_tr_b16 v[26:27], v241 offset:5120\n"
        "buffer_load_dwordx4 a[124:127], v232, s[40:43], s49 offen offset:3072 sc1\n"
        "s_waitcnt lgkmcnt(10)\n"
        "v_mfma_f32_32x32x16_bf16 v[48:63], v[4:7], a[128:131], v[48:63]\n"
        "ds_read_b64_tr_b16 v[28:29], v244 offset:4096\n"
        "ds_read_b64_tr_b16 v[30:31], v244 offset:5120\n"
        "s_waitcnt lgkmcnt(10)\n"
        "v_mfma_f32_32x32x16_bf16 v[64:79], v[8:11], a[128:131], v[64:79]\n"
        "ds_read_b64_tr_b16 v[0:1], v235 offset:8192\n"
        "ds_read_b64_tr_b16 v[2:3], v235 offset:9216\n"
        "s_waitcnt lgkmcnt(10)\n"
        "v_mfma_f32_32x32x16_bf16 v[80:95], v[12:15], a[128:131], v[80:95]\n"
        "ds_read_b64_tr_b16 v[4:5], v238 offset:8192\n"
        "ds_read_b64_tr_b16 v[6:7], v238 offset:9216\n"
        "s_waitcnt lgkmcnt(10)\n"
        "v_mfma_f32_32x32x16_bf16 v[32:47], v[16:19], a[132:135], v[32:47]\n"
        "ds_read_b64_tr_b16 v[8:9], v241 offset:8192\n"
        "ds_read_b64_tr_b16 v[10:11], v241 offset:9216\n"
        "s_add_u32 s49, s49, 4096\n"
        "buffer_load_dwordx4 a[128:131], v232, s[40:43], s49 offen offset:0 sc1\n"
        "s_waitcnt lgkmcnt(10)\n"
        "v_mfma_f32_32x32x16_bf16 v[48:63], v[20:23], a[132:135], v[48:63]\n"
        "ds_read_b64_tr_b16 v[12:13], v244 offset:8192\n"
        "ds_read_b64_tr_b16 v[14:15], v244 offset:9216\n"
        "s_waitcnt lgkmcnt(10)\n"
        "v_mfma_f32_32x32x16_bf16 v[64:79], v[24:27], a[132:135], v[64:79]\n"
        "ds_read_b64_tr_b16 v[16:17], v235 offset:12288\n"
        "ds_read_b64_tr_b16 v[18:19], v235 offset:13312\n"
        "s_waitcnt lgkmcnt(10)\n"
        "v_mfma_f32_32x32x16_bf16 v[80:95], v[28:31], a[132:135], v[80:95]\n"
        "ds_read_b64_tr_b16 v[20:21], v238 offset:12288\n"
        "ds_read_b64_tr_b16 v[22:23], v238 offset:13312\n"
        "s_waitcnt lgkmcnt(10)\n"
        "v_mfma_f32_32x32x16_bf16 v[32:47], v[0:3], a[136:139], v[32:47]\n"
        "ds_read_b64_tr_b16 v[24:25], v241 offset:12288\n"
        "ds_read_b64_tr_b16 v[26:27], v241 offset:13312\n"
        "buffer_load_dwordx4 a[132:135], v232, s[40:43], s49 offen offset:1024 sc1\n"
        "s_waitcnt lgkmcnt(10)\n"
        "v_mfma_f32_32x32x16_bf16 v[48:63], v[4:7], a[136:139], v[48:63]\n"
        "ds_read_b64_tr_b16 v[28:29], v244 offset:12288\n"
        "ds_read_b64_tr_b16 v[30:31], v244 offset:13312\n"
        "s_waitcnt lgkmcnt(10)\n"
        "v_mfma_f32_32x32x16_bf16 v[64:79], v[8:11], a[136:139], v[64:79]\n"
        "s_waitcnt lgkmcnt(8)\n"
        "v_mfma_f32_32x32x16_bf16 v[80:95], v[12:15], a[136:139], v[80:95]\n"
        "s_waitcnt lgkmcnt(6)\n"
        "v_mfma_f32_32x32x16_bf16 v[32:47], v[16:19], a[140:143], v[32:47]\n"
        "buffer_load_dwordx4 a[136:139], v232, s[40:43], s49 offen offset:2048 sc1\n"
        "s_waitcnt lgkmcnt(4)\n"
        "v_mfma_f32_32x32x16_bf16 v[48:63], v[20:23], a[140:143], v[48:63]\n"
        "s_waitcnt lgkmcnt(2)\n"
        "v_mfma_f32_32x32x16_bf16 v[64:79], v[24:27], a[140:143], v[64:79]\n"
        "s_waitcnt lgkmcnt(0)\n"
        "v_mfma_f32_32x32x16_bf16 v[80:95], v[28:31], a[140:143], v[80:95]\n"
        "v_cvt_pk_bf16_f32 v96, v32, v36\n"
        "v_cvt_pk_bf16_f32 v97, v33, v37\n"
        "v_cvt_pk_bf16_f32 v98, v34, v38\n"
        "v_cvt_pk_bf16_f32 v99, v35, v39\n"
        "buffer_load_dwordx4 a[140:143], v232, s[40:43], s49 offen offset:3072 sc1\n"
        "s_add_u32 s49, s49, 114688\n"
        "v_cvt_pk_bf16_f32 v100, v40, v44\n"
        "v_cvt_pk_bf16_f32 v101, v41, v45\n"
        "v_cvt_pk_bf16_f32 v102, v42, v46\n"
        "v_cvt_pk_bf16_f32 v103, v43, v47\n"
        "s_waitcnt vmcnt(51)\n"
        "v_mfma_f32_32x32x16_bf16 v[128:143], a[144:147], v[96:99], 0\n"
        "v_cvt_pk_bf16_f32 v104, v48, v52\n"
        "v_cvt_pk_bf16_f32 v105, v49, v53\n"
        "v_cvt_pk_bf16_f32 v106, v50, v54\n"
        "v_cvt_pk_bf16_f32 v107, v51, v55\n"
        "s_waitcnt vmcnt(50)\n"
        "v_mfma_f32_32x32x16_bf16 v[128:143], a[148:151], v[100:103], v[128:143]\n"
        "v_cvt_pk_bf16_f32 v108, v56, v60\n"
        "v_cvt_pk_bf16_f32 v109, v57, v61\n"
        "v_cvt_pk_bf16_f32 v110, v58, v62\n"
        "v_cvt_pk_bf16_f32 v111, v59, v63\n"
        "s_waitcnt vmcnt(49)\n"
        "v_mfma_f32_32x32x16_bf16 v[128:143], a[152:155], v[104:107], v[128:143]\n"
        "v_cvt_pk_bf16_f32 v112, v64, v68\n"
        "v_cvt_pk_bf16_f32 v113, v65, v69\n"
        "v_cvt_pk_bf16_f32 v114, v66, v70\n"
        "v_cvt_pk_bf16_f32 v115, v67, v71\n"
        "s_waitcnt vmcnt(48)\n"
        "v_mfma_f32_32x32x16_bf16 v[128:143], a[156:159], v[108:111], v[128:143]\n"
        "v_cvt_pk_bf16_f32 v116, v72, v76\n"
        "v_cvt_pk_bf16_f32 v117, v73, v77\n"
        "v_cvt_pk_bf16_f32 v118, v74, v78\n"
        "v_cvt_pk_bf16_f32 v119, v75, v79\n"
        "s_waitcnt vmcnt(47)\n"
        "v_mfma_f32_32x32x16_bf16 v[128:143], a[160:163], v[112:115], v[128:143]\n"
        "v_cvt_pk_bf16_f32 v120, v80, v84\n"
        "v_cvt_pk_bf16_f32 v121, v81, v85\n"
        "v_cvt_pk_bf16_f32 v122, v82, v86\n"
        "v_cvt_pk_bf16_f32 v123, v83, v87\n"
        "s_waitcnt vmcnt(46)\n"
        "v_mfma_f32_32x32x16_bf16 v[128:143], a[164:167], v[116:119], v[128:143]\n"
        "v_cvt_pk_bf16_f32 v124, v88, v92\n"
        "v_cvt_pk_bf16_f32 v125, v89, v93\n"
        "v_cvt_pk_bf16_f32 v126, v90, v94\n"
        "v_cvt_pk_bf16_f32 v127, v91, v95\n"
        "s_waitcnt vmcnt(45)\n"
        "v_mfma_f32_32x32x16_bf16 v[128:143], a[168:171], v[120:123], v[128:143]\n"
        "s_waitcnt vmcnt(44)\n"
        "v_mfma_f32_32x32x16_bf16 v[128:143], a[172:175], v[124:127], v[128:143]\n"
        "s_nop 0\n"
        "s_nop 10\n"
        "v_cndmask_b32 v128, v248, v128, s[60:61]\n"
        "v_cndmask_b32 v129, v248, v129, s[62:63]\n"
        "v_cndmask_b32 v130, v248, v130, s[64:65]\n"
        "v_cndmask_b32 v131, v248, v131, s[66:67]\n"
        "v_cndmask_b32 v132, v248, v132, s[68:69]\n"
        "v_cndmask_b32 v133, v248, v133, s[70:71]\n"
        "v_cndmask_b32 v134, v248, v134, s[72:73]\n"
        "v_cndmask_b32 v135, v248, v135, s[74:75]\n"
        "v_cndmask_b32 v136, v248, v136, s[76:77]\n"
        "v_cndmask_b32 v137, v248, v137, s[78:79]\n"
        "v_cndmask_b32 v138, v248, v138, s[80:81]\n"
        "v_cndmask_b32 v139, v248, v139, s[82:83]\n"
        "v_cndmask_b32 v140, v248, v140, s[84:85]\n"
        "v_cndmask_b32 v141, v248, v141, s[86:87]\n"
        "v_cndmask_b32 v142, v248, v142, s[88:89]\n"
        "v_cndmask_b32 v143, v248, v143, s[90:91]\n"
        "v_max_f32 v249, v128, v129\n"
        "v_max3_f32 v249, v249, v130, v131\n"
        "v_max3_f32 v249, v249, v132, v133\n"
        "v_max3_f32 v249, v249, v134, v135\n"
        "v_max3_f32 v249, v249, v136, v137\n"
        "v_max3_f32 v249, v249, v138, v139\n"
        "v_max3_f32 v249, v249, v140, v141\n"
        "v_max3_f32 v249, v249, v142, v143\n"
        "v_mov_b32 v251, v249\n"
        "s_nop 1\n"
        "v_permlane32_swap_b32 v249, v251\n"
        "v_max_f32 v249, v249, v251\n"
        "v_mul_f32 v252, s58, v249\n"
        "v_fma_f32 v128, v128, s58, -v252\n"
        "v_fma_f32 v129, v129, s58, -v252\n"
        "v_fma_f32 v130, v130, s58, -v252\n"
        "v_fma_f32 v131, v131, s58, -v252\n"
        "v_fma_f32 v132, v132, s58, -v252\n"
        "v_fma_f32 v133, v133, s58, -v252\n"
        "v_fma_f32 v134, v134, s58, -v252\n"
        "v_fma_f32 v135, v135, s58, -v252\n"
        "v_fma_f32 v136, v136, s58, -v252\n"
        "v_fma_f32 v137, v137, s58, -v252\n"
        "v_fma_f32 v138, v138, s58, -v252\n"
        "v_fma_f32 v139, v139, s58, -v252\n"
        "v_fma_f32 v140, v140, s58, -v252\n"
        "v_fma_f32 v141, v141, s58, -v252\n"
        "v_fma_f32 v142, v142, s58, -v252\n"
        "v_fma_f32 v143, v143, s58, -v252\n"
        "v_exp_f32 v128, v128\n"
        "v_exp_f32 v129, v129\n"
        "v_exp_f32 v130, v130\n"
        "v_exp_f32 v131, v131\n"
        "v_exp_f32 v132, v132\n"
        "v_exp_f32 v133, v133\n"
        "v_exp_f32 v134, v134\n"
        "v_exp_f32 v135, v135\n"
        "v_exp_f32 v136, v136\n"
        "v_exp_f32 v137, v137\n"
        "v_exp_f32 v138, v138\n"
        "v_exp_f32 v139, v139\n"
        "v_exp_f32 v140, v140\n"
        "v_exp_f32 v141, v141\n"
        "v_exp_f32 v142, v142\n"
        "v_exp_f32 v143, v143\n"
        "v_cvt_pk_bf16_f32 v144, v128, v129\n"
        "v_cvt_pk_bf16_f32 v145, v130, v131\n"
        "v_cvt_pk_bf16_f32 v146, v132, v133\n"
        "v_cvt_pk_bf16_f32 v147, v134, v135\n"
        "v_cvt_pk_bf16_f32 v148, v136, v137\n"
        "v_cvt_pk_bf16_f32 v149, v138, v139\n"
        "v_cvt_pk_bf16_f32 v150, v140, v141\n"
        "v_cvt_pk_bf16_f32 v151, v142, v143\n"
        "v_add_f32 v250, v128, v129\n"
        "s_waitcnt vmcnt(43)\n"
        "v_mfma_f32_32x32x16_bf16 v[32:47], a[176:179], v[144:147], 0\n"
        "v_add_f32 v250, v250, v130\n"
        "v_add_f32 v250, v250, v131\n"
        "s_waitcnt vmcnt(42)\n"
        "v_mfma_f32_32x32x16_bf16 v[32:47], a[180:183], v[148:151], v[32:47]\n"
        "v_add_f32 v250, v250, v132\n"
        "v_add_f32 v250, v250, v133\n"
        "s_waitcnt vmcnt(41)\n"
        "v_mfma_f32_32x32x16_bf16 v[48:63], a[184:187], v[144:147], 0\n"
        "v_add_f32 v250, v250, v134\n"
        "v_add_f32 v250, v250, v135\n"
        "s_waitcnt vmcnt(40)\n"
        "v_mfma_f32_32x32x16_bf16 v[48:63], a[188:191], v[148:151], v[48:63]\n"
        "v_add_f32 v250, v250, v136\n"
        "v_add_f32 v250, v250, v137\n"
        "s_waitcnt vmcnt(39)\n"
        "v_mfma_f32_32x32x16_bf16 v[64:79], a[192:195], v[144:147], 0\n"
        "v_add_f32 v250, v250, v138\n"
        "v_add_f32 v250, v250, v139\n"
        "s_waitcnt vmcnt(38)\n"
        "v_mfma_f32_32x32x16_bf16 v[64:79], a[196:199], v[148:151], v[64:79]\n"
        "v_add_f32 v250, v250, v140\n"
        "v_add_f32 v250, v250, v141\n"
        "s_waitcnt vmcnt(37)\n"
        "v_mfma_f32_32x32x16_bf16 v[80:95], a[200:203], v[144:147], 0\n"
        "v_add_f32 v250, v250, v142\n"
        "v_add_f32 v250, v250, v143\n"
        "s_waitcnt vmcnt(36)\n"
        "v_mfma_f32_32x32x16_bf16 v[80:95], a[204:207], v[148:151], v[80:95]\n"
        "s_cmp_ge_u32 s48, 63\n"
        "s_cselect_b32 s54, 32768, 0\n"
        "s_sub_u32 s52, s56, s54\n"
        "4:\n"
        "buffer_load_dword v251, v247, s[40:43], s52 offen sc1\n"
        "s_waitcnt vmcnt(0)\n"
        "v_cmp_eq_u32 vcc, -1, v251\n"
        "s_cbranch_vccz 5f\n"
        "s_cmp_eq_u32 s55, 0\n"
        "s_cbranch_scc1 5f\n"
        "s_sub_u32 s55, s55, 1\n"
        "s_sleep 3\n"
        "s_branch 4b\n"
        "5:\n"
        "s_cmp_ge_u32 s48, 62\n"
        "s_cbranch_scc1 15f\n"
        "s_sleep 0\n"
        "s_or_b32 s53, s48, 0\n"
        "s_cmp_eq_u32 s53, 59\n"
        "s_cbranch_scc1 15f\n"
        "s_sleep 26\n"
        "15:\n"
        "s_cmp_eq_u32 s48, 63\n"
        "s_cbranch_scc0 8f\n"
        "s_sleep 4\n"
        "8:\n"
        "s_add_u32 s52, s51, 0x5c00000\n"
        "s_add_u32 s53, s51, 0x6000000\n"
        "buffer_load_dwordx4 v[0:3], v245, s[40:43], s52 offen offset:0 sc1\n"
        "buffer_load_dwordx4 v[4:7], v245, s[40:43], s52 offen offset:1024 sc1\n"
        "buffer_load_dwordx4 v[8:11], v245, s[40:43], s52 offen offset:256 sc1\n"
        "buffer_load_dwordx4 v[12:15], v245, s[40:43], s52 offen offset:1280 sc1\n"
        "buffer_load_dwordx4 v[16:19], v245, s[40:43], s52 offen offset:512 sc1\n"
        "buffer_load_dwordx4 v[20:23], v245, s[40:43], s52 offen offset:1536 sc1\n"
        "buffer_load_dwordx4 v[24:27], v245, s[40:43], s52 offen offset:768 sc1\n"
        "buffer_load_dwordx4 v[28:31], v245, s[40:43], s52 offen offset:1792 sc1\n"
        "buffer_load_dwordx2 v[128:129], v246, s[40:43], s53 offen offset:0 sc1\n"
        "buffer_load_dwordx2 v[130:131], v246, s[40:43], s53 offen offset:512 sc1\n"
        "buffer_load_dwordx2 v[132:133], v246, s[40:43], s53 offen offset:1024 sc1\n"
        "buffer_load_dwordx2 v[134:135], v246, s[40:43], s53 offen offset:1536 sc1\n"
        "s_add_u32 s53, s48, 1\n"
        "v_mov_b32 v251, 0\n"
        "v_mov_b32 v144, s53\n"
        "v_lshlrev_b32 v145, 5, v232\n"
        "s_waitcnt vmcnt(11)\n"
        "v_mfma_f32_32x32x16_bf16 v[152:167], v[0:3], v[96:99], 0\n"
        "v_max3_u32 v251, v251, v0, v1\n"
        "v_max3_u32 v251, v251, v2, v3\n"
        "s_waitcnt vmcnt(10)\n"
        "v_mfma_f32_32x32x16_bf16 v[152:167], v[4:7], v[100:103], v[152:167]\n"
        "v_max3_u32 v251, v251, v4, v5\n"
        "v_max3_u32 v251, v251, v6, v7\n"
        "s_waitcnt vmcnt(9)\n"
        "v_mfma_f32_32x32x16_bf16 v[152:167], v[8:11], v[104:107], v[152:167]\n"
        "v_max3_u32 v251, v251, v8, v9\n"
        "v_max3_u32 v251, v251, v10, v11\n"
        "s_waitcnt vmcnt(8)\n"
        "v_mfma_f32_32x32x16_bf16 v[152:167], v[12:15], v[108:111], v[152:167]\n"
        "v_max3_u32 v251, v251, v12, v13\n"
        "v_max3_u32 v251, v251, v14, v15\n"
        "s_waitcnt vmcnt(7)\n"
        "v_mfma_f32_32x32x16_bf16 v[152:167], v[16:19], v[112:115], v[152:167]\n"
        "v_max3_u32 v251, v251, v16, v17\n"
        "v_max3_u32 v251, v251, v18, v19\n"
        "s_waitcnt vmcnt(6)\n"
        "v_mfma_f32_32x32x16_bf16 v[152:167], v[20:23], v[116:119], v[152:167]\n"
        "v_max3_u32 v251, v251, v20, v21\n"
        "v_max3_u32 v251, v251, v22, v23\n"
        "s_waitcnt vmcnt(5)\n"
        "v_mfma_f32_32x32x16_bf16 v[152:167], v[24:27], v[120:123], v[152:167]\n"
        "v_max3_u32 v251, v251, v24, v25\n"
        "v_max3_u32 v251, v251, v26, v27\n"
        "s_waitcnt vmcnt(4)\n"
        "v_mfma_f32_32x32x16_bf16 v[152:167], v[28:31], v[124:127], v[152:167]\n"
        "v_max3_u32 v251, v251, v28, v29\n"
        "v_max3_u32 v251, v251, v30, v31\n"
        "s_waitcnt vmcnt(3)\n"
        "v_max3_u32 v251, v251, v128, v129\n"
        "s_waitcnt vmcnt(2)\n"
        "v_max3_u32 v251, v251, v130, v131\n"
        "s_waitcnt vmcnt(1)\n"
        "v_max3_u32 v251, v251, v132, v133\n"
        "s_waitcnt vmcnt(0)\n"
        "v_max3_u32 v251, v251, v134, v135\n"
        "v_cmp_eq_u32 vcc, -1, v251\n"
        "s_cbranch_vccnz 10f\n"
        "9:\n"
        "v_max_f32 v252, v152, v153\n"
        "v_max3_f32 v252, v252, v154, v155\n"
        "v_max_f32 v252, v252, v249\n"
        "v_mov_b32 v251, v252\n"
        "s_nop 1\n"
        "v_permlane32_swap_b32 v252, v251\n"
        "v_max_f32 v252, v252, v251\n"
        "v_mul_f32 v252, s58, v252\n"
        "v_fma_f32 v253, v249, s58, -v252\n"
        "v_fma_f32 v152, v152, s58, -v252\n"
        "v_fma_f32 v153, v153, s58, -v252\n"
        "v_fma_f32 v154, v154, s58, -v252\n"
        "v_fma_f32 v155, v155, s58, -v252\n"
        "v_exp_f32 v253, v253\n"
        "v_exp_f32 v152, v152\n"
        "v_exp_f32 v153, v153\n"
        "v_exp_f32 v154, v154\n"
        "v_exp_f32 v155, v155\n"
        "v_fma_f32 v250, v250, v253, v152\n"
        "v_add_f32 v250, v250, v153\n"
        "v_add_f32 v250, v250, v154\n"
        "v_add_f32 v250, v250, v155\n"
        "v_mov_b32 v251, v250\n"
        "s_nop 1\n"
        "v_permlane32_swap_b32 v250, v251\n"
        "v_add_f32 v250, v250, v251\n"
        "v_rcp_f32 v250, v250\n"
        "s_nop 0\n"
        "v_mul_f32 v152, v152, v250\n"
        "v_mul_f32 v153, v153, v250\n"
        "v_mul_f32 v154, v154, v250\n"
        "v_mul_f32 v155, v155, v250\n"
        "v_cvt_pk_bf16_f32 v152, v152, v153\n"
        "v_cvt_pk_bf16_f32 v153, v154, v155\n"
        "v_mul_f32 v253, v253, v250\n"
        "s_lshl_b32 s53, %2, 12\n"
        "s_sub_u32 s52, s50, s53\n"
        "s_add_u32 s52, s52, 0x3800000\n"
        "v_mfma_f32_32x32x8_bf16 v[168:183], v[128:129], v[152:153], 0\n"
        "s_cmp_ge_u32 s48, 62\n"
        "s_cselect_b64 exec, 3, 0\n"
        "buffer_store_dword v144, v145, s[40:43], s57 offen sc1\n"
        "s_mov_b64 exec, -1\n"
        "v_mfma_f32_32x32x8_bf16 v[184:199], v[130:131], v[152:153], 0\n"
        "s_nop 0\n"
        "s_nop 2\n"
        "v_fmac_f32 v168, v32, v253\n"
        "v_fmac_f32 v169, v33, v253\n"
        "v_fmac_f32 v170, v34, v253\n"
        "v_fmac_f32 v171, v35, v253\n"
        "v_fmac_f32 v172, v36, v253\n"
        "v_fmac_f32 v173, v37, v253\n"
        "v_fmac_f32 v174, v38, v253\n"
        "v_fmac_f32 v175, v39, v253\n"
        "v_mfma_f32_32x32x8_bf16 v[200:215], v[132:133], v[152:153], 0\n"
        "v_cvt_pk_bf16_f32 v0, v168, v169\n"
        "v_cvt_pk_bf16_f32 v1, v170, v171\n"
        "v_cvt_pk_bf16_f32 v2, v172, v173\n"
        "v_cvt_pk_bf16_f32 v3, v174, v175\n"
        "buffer_store_dwordx4 v[0:3], v255, s[40:43], s52 offen offset:0 sc1\n"
        "v_fmac_f32 v176, v40, v253\n"
        "v_fmac_f32 v177, v41, v253\n"
        "v_fmac_f32 v178, v42, v253\n"
        "v_fmac_f32 v179, v43, v253\n"
        "v_fmac_f32 v180, v44, v253\n"
        "v_fmac_f32 v181, v45, v253\n"
        "v_fmac_f32 v182, v46, v253\n"
        "v_fmac_f32 v183, v47, v253\n"
        "v_mfma_f32_32x32x8_bf16 v[216:231], v[134:135], v[152:153], 0\n"
        "v_cvt_pk_bf16_f32 v4, v176, v177\n"
        "v_cvt_pk_bf16_f32 v5, v178, v179\n"
        "v_cvt_pk_bf16_f32 v6, v180, v181\n"
        "v_cvt_pk_bf16_f32 v7, v182, v183\n"
        "buffer_store_dwordx4 v[4:7], v255, s[40:43], s52 offen offset:512 sc1\n"
        "v_fmac_f32 v184, v48, v253\n"
        "v_fmac_f32 v185, v49, v253\n"
        "v_fmac_f32 v186, v50, v253\n"
        "v_fmac_f32 v187, v51, v253\n"
        "v_fmac_f32 v188, v52, v253\n"
        "v_fmac_f32 v189, v53, v253\n"
        "v_fmac_f32 v190, v54, v253\n"
        "v_fmac_f32 v191, v55, v253\n"
        "v_cvt_pk_bf16_f32 v8, v184, v185\n"
        "v_cvt_pk_bf16_f32 v9, v186, v187\n"
        "v_cvt_pk_bf16_f32 v10, v188, v189\n"
        "v_cvt_pk_bf16_f32 v11, v190, v191\n"
        "buffer_store_dwordx4 v[8:11], v255, s[40:43], s52 offen offset:1024 sc1\n"
        "v_fmac_f32 v192, v56, v253\n"
        "v_fmac_f32 v193, v57, v253\n"
        "v_fmac_f32 v194, v58, v253\n"
        "v_fmac_f32 v195, v59, v253\n"
        "v_fmac_f32 v196, v60, v253\n"
        "v_fmac_f32 v197, v61, v253\n"
        "v_fmac_f32 v198, v62, v253\n"
        "v_fmac_f32 v199, v63, v253\n"
        "v_cvt_pk_bf16_f32 v12, v192, v193\n"
        "v_cvt_pk_bf16_f32 v13, v194, v195\n"
        "v_cvt_pk_bf16_f32 v14, v196, v197\n"
        "v_cvt_pk_bf16_f32 v15, v198, v199\n"
        "buffer_store_dwordx4 v[12:15], v255, s[40:43], s52 offen offset:1536 sc1\n"
        "v_fmac_f32 v200, v64, v253\n"
        "v_fmac_f32 v201, v65, v253\n"
        "v_fmac_f32 v202, v66, v253\n"
        "v_fmac_f32 v203, v67, v253\n"
        "v_fmac_f32 v204, v68, v253\n"
        "v_fmac_f32 v205, v69, v253\n"
        "v_fmac_f32 v206, v70, v253\n"
        "v_fmac_f32 v207, v71, v253\n"
        "v_cvt_pk_bf16_f32 v16, v200, v201\n"
        "v_cvt_pk_bf16_f32 v17, v202, v203\n"
        "v_cvt_pk_bf16_f32 v18, v204, v205\n"
        "v_cvt_pk_bf16_f32 v19, v206, v207\n"
        "buffer_store_dwordx4 v[16:19], v255, s[40:43], s52 offen offset:2048 sc1\n"
        "v_fmac_f32 v208, v72, v253\n"
        "v_fmac_f32 v209, v73, v253\n"
        "v_fmac_f32 v210, v74, v253\n"
        "v_fmac_f32 v211, v75, v253\n"
        "v_fmac_f32 v212, v76, v253\n"
        "v_fmac_f32 v213, v77, v253\n"
        "v_fmac_f32 v214, v78, v253\n"
        "v_fmac_f32 v215, v79, v253\n"
        "v_cvt_pk_bf16_f32 v20, v208, v209\n"
        "v_cvt_pk_bf16_f32 v21, v210, v211\n"
        "v_cvt_pk_bf16_f32 v22, v212, v213\n"
        "v_cvt_pk_bf16_f32 v23, v214, v215\n"
        "buffer_store_dwordx4 v[20:23], v255, s[40:43], s52 offen offset:2560 sc1\n"
        "v_fmac_f32 v216, v80, v253\n"
        "v_fmac_f32 v217, v81, v253\n"
        "v_fmac_f32 v218, v82, v253\n"
        "v_fmac_f32 v219, v83, v253\n"
        "v_fmac_f32 v220, v84, v253\n"
        "v_fmac_f32 v221, v85, v253\n"
        "v_fmac_f32 v222, v86, v253\n"
        "v_fmac_f32 v223, v87, v253\n"
        "v_cvt_pk_bf16_f32 v24, v216, v217\n"
        "v_cvt_pk_bf16_f32 v25, v218, v219\n"
        "v_cvt_pk_bf16_f32 v26, v220, v221\n"
        "v_cvt_pk_bf16_f32 v27, v222, v223\n"
        "buffer_store_dwordx4 v[24:27], v255, s[40:43], s52 offen offset:3072 sc1\n"
        "v_fmac_f32 v224, v88, v253\n"
        "v_fmac_f32 v225, v89, v253\n"
        "v_fmac_f32 v226, v90, v253\n"
        "v_fmac_f32 v227, v91, v253\n"
        "v_fmac_f32 v228, v92, v253\n"
        "v_fmac_f32 v229, v93, v253\n"
        "v_fmac_f32 v230, v94, v253\n"
        "v_fmac_f32 v231, v95, v253\n"
        "v_cvt_pk_bf16_f32 v28, v224, v225\n"
        "v_cvt_pk_bf16_f32 v29, v226, v227\n"
        "v_cvt_pk_bf16_f32 v30, v228, v229\n"
        "v_cvt_pk_bf16_f32 v31, v230, v231\n"
        "buffer_store_dwordx4 v[28:31], v255, s[40:43], s52 offen offset:3584 sc1\n"
        "s_add_u32 s48, s48, 1\n"
        "v_add_u32 v146, 64, v144\n"
        "s_waitcnt vmcnt(0)\n"
        "s_mov_b64 exec, 3\n"
        "buffer_store_dword v146, v145, s[40:43], s57 offen sc1\n"
        "s_mov_b64 exec, -1\n"
        "s_add_u32 s48, s48, 3\n"
        "s_add_u32 s50, s50, 1048576\n"
        "s_add_u32 s51, s51, 262144\n"
        "s_add_u32 s56, s56, 131072\n"
        "s_add_u32 s57, s57, 4096\n"
        "s_cmp_lt_u32 s48, 64\n"
        "s_cbranch_scc1 1b\n"
        "s_waitcnt vmcnt(0)\n"
        "s_branch 14f\n"
        "10:\n"
        "s_cmp_eq_u32 s54, 0\n"
        "s_cbranch_scc1 9b\n"
        "s_lshr_b32 s54, s54, 15\n"
        "s_cbranch_scc1 8b\n"
        "s_add_u32 s52, s59, 0x401000-0x400000\n"
        "s_cmp_eq_u32 s48, 63\n"
        "s_cselect_b32 s52, s52, s56\n"
        "s_cselect_b32 s53, 0, -1\n"
        "11:\n"
        "buffer_load_dword v251, v247, s[40:43], s52 offen sc1\n"
        "s_waitcnt vmcnt(0)\n"
        "v_cmp_eq_u32 vcc, s53, v251\n"
        "s_cbranch_vccz 12f\n"
        "s_cmp_eq_u32 s55, 0\n"
        "s_cbranch_scc1 12f\n"
        "s_sub_u32 s55, s55, 1\n"
        "s_sleep 1\n"
        "s_branch 11b\n"
        "12:\n"
        "s_branch 8b\n"
        "14:\n"
        :: "s"(ws), "s"(par), "s"(hd), "s"(w), "s"(from)
        : "v0", "v1", "v2", "v3", "v4", "v5", "v6", "v7", "v8", "v9", "v10", "v11", "v12", "v13", "v14", "v15", "v16", "v17", "v18", "v19", "v20", "v21", "v22", "v23", "v24", "v25", "v26", "v27", "v28", "v29", "v30", "v31", "v32", "v33", "v34", "v35", "v36", "v37", "v38", "v39", "v40", "v41", "v42", "v43", "v44", "v45", "v46", "v47", "v48", "v49", "v50", "v51", "v52", "v53", "v54", "v55", "v56", "v57", "v58", "v59", "v60", "v61", "v62", "v63", "v64", "v65", "v66", "v67", "v68", "v69", "v70", "v71", "v72", "v73", "v74", "v75", "v76", "v77", "v78", "v79", "v80", "v81", "v82", "v83", "v84", "v85", "v86", "v87", "v88", "v89", "v90", "v91", "v92", "v93", "v94", "v95", "v96", "v97", "v98", "v99", "v100", "v101", "v102", "v103", "v104", "v105", "v106", "v107", "v108", "v109", "v110", "v111", "v112", "v113", "v114", "v115", "v116", "v117", "v118", "v119", "v120", "v121", "v122", "v123", "v124", "v125", "v126", "v127", "v128", "v129", "v130", "v131", "v132", "v133", "v134", "v135", "v136", "v137", "v138", "v139", "v140", "v141", "v142", "v143", "v144", "v145", "v146", "v147", "v148", "v149", "v150", "v151", "v152", "v153", "v154", "v155", "v156", "v157", "v158", "v159", "v160", "v161", "v162", "v163", "v164", "v165", "v166", "v167", "v168", "v169", "v170", "v171", "v172", "v173", "v174", "v175", "v176", "v177", "v178", "v179", "v180", "v181", "v182", "v183", "v184", "v185", "v186", "v187", "v188", "v189", "v190", "v191", "v192", "v193", "v194", "v195", "v196", "v197", "v198", "v199", "v200", "v201", "v202", "v203", "v204", "v205", "v206", "v207", "v208", "v209", "v210", "v211", "v212", "v213", "v214", "v215", "v216", "v217", "v218", "v219", "v220", "v221", "v222", "v223", "v224", "v225", "v226", "v227", "v228", "v229", "v230", "v231", "v232", "v233", "v234", "v235", "v236", "v237", "v238", "v239", "v240", "v241", "v242", "v243", "v244", "v245", "v246", "v247", "v248", "v249", "v250", "v251", "v252", "v253", "v254", "v255", "a0", "a1", "a2", "a3", "a4", "a5", "a6", "a7", "a8", "a9", "a10", "a11", "a12", "a13", "a14", "a15", "a16", "a17", "a18", "a19", "a20", "a21", "a22", "a23", "a24", "a25", "a26", "a27", "a28", "a29", "a30", "a31", "a32", "a33", "a34", "a35", "a36", "a37", "a38", "a39", "a40", "a41", "a42", "a43", "a44", "a45", "a46", "a47", "a48", "a49", "a50", "a51", "a52", "a53", "a54", "a55", "a56", "a57", "a58", "a59", "a60", "a61", "a62", "a63", "a64", "a65", "a66", "a67", "a68", "a69", "a70", "a71", "a72", "a73", "a74", "a75", "a76", "a77", "a78", "a79", "a80", "a81", "a82", "a83", "a84", "a85", "a86", "a87", "a88", "a89", "a90", "a91", "a92", "a93", "a94", "a95", "a96", "a97", "a98", "a99", "a100", "a101", "a102", "a103", "a104", "a105", "a106", "a107", "a108", "a109", "a110", "a111", "a112", "a113", "a114", "a115", "a116", "a117", "a118", "a119", "a120", "a121", "a122", "a123", "a124", "a125", "a126", "a127", "a128", "a129", "a130", "a131", "a132", "a133", "a134", "a135", "a136", "a137", "a138", "a139", "a140", "a141", "a142", "a143", "a144", "a145", "a146", "a147", "a148", "a149", "a150", "a151", "a152", "a153", "a154", "a155", "a156", "a157", "a158", "a159", "a160", "a161", "a162", "a163", "a164", "a165", "a166", "a167", "a168", "a169", "a170", "a171", "a172", "a173", "a174", "a175", "a176", "a177", "a178", "a179", "a180", "a181", "a182", "a183", "a184", "a185", "a186", "a187", "a188", "a189", "a190", "a191", "a192", "a193", "a194", "a195", "a196", "a197", "a198", "a199", "a200", "a201", "a202", "a203", "a204", "a205", "a206", "a207", "a208", "a209", "a210", "a211", "a212", "a213", "a214", "a215", "a216", "a217", "a218", "a219", "a220", "a221", "a222", "a223", "a224", "a225", "a226", "a227", "a228", "a229", "a230", "a231", "a232", "a233", "a234", "a235", "a236", "a237", "a238", "a239", "a240", "a241", "a242", "a243", "a244", "a245", "a246", "a247", "a248", "a249", "a250", "a251", "a252", "a253", "a254", "a255", "s40", "s41", "s42", "s43", "s44", "s45", "s46", "s47", "s48", "s49", "s50", "s51", "s52", "s53", "s54", "s55", "s56", "s57", "s58", "s59", "s60", "s61", "s62", "s63", "s64", "s65", "s66", "s67", "s68", "s69", "s70", "s71", "s72", "s73", "s74", "s75", "s76", "s77", "s78", "s79", "s80", "s81", "s82", "s83", "s84", "s85", "s86", "s87", "s88", "s89", "s90", "s91", "s92", "s93", "vcc", "scc", "memory");
}

static_assert(FL_ACK == 4347904 && FL_G == 4202496 && FL_HF == 4280320 && FL_OF == 4210688 && OFF_GF == 41943040 && OFF_GO == 4456448 && OFF_OF == 58720256 && OFF_RDY == 4489216 && SPIN_MAX == 65536 && WS_SPAN == 109051904, "constants of the assembly block");
__device__ __forceinline__ void proj_out(unsigned char* ws, const u16* W, int src, int stride, const u16* x, u16* out, int cb, int grp, int w, int kind, int blk, unsigned go0, int from, int last) {
    asm volatile(
        "s_mov_b64 s[40:41], %0\n"
        "s_mov_b32 s42, 0x6800000\n"
        "s_mov_b32 s43, 0x20000\n"
        "s_mov_b64 s[44:45], %1\n"
        "s_mov_b32 s46, 1327104\n"
        "s_mov_b32 s47, 0x20000\n"
        "s_mov_b64 s[60:61], %4\n"
        "s_mov_b32 s62, 2359296\n"
        "s_mov_b32 s63, 0x20000\n"
        "s_mul_i32 s55, %8, 40960\n"
        "s_mov_b32 s54, 0x10000\n"
        "s_mov_b32 s49, %2\n"
        "s_lshr_b32 s48, %7, 1\n"
        "s_mul_i32 s52, %7, 9\n"
        "s_add_u32 s52, s52, %6\n"
        "s_lshl_b32 s57, s52, 7\n"
        "s_lshl2_add_u32 s57, %8, s57\n"
        "s_add_u32 s57, s57, 0x402000\n"
        "s_lshl2_add_u32 s52, s52, %8\n"
        "s_lshl_b32 s51, s52, 9\n"
        "s_add_u32 s51, s51, 0x2800000\n"
        "s_lshl2_add_u32 s52, %7, %8\n"
        "s_lshl_b32 s50, s52, 7\n"
        "s_mul_i32 s56, s52, 4608\n"
        "s_lshl_b32 s52, %6, 7\n"
        "s_add_u32 s56, s56, s52\n"
        "s_lshl1_add_u32 s48, %12, s48\n"
        "s_mul_i32 s52, %12, 73728\n"
        "s_add_u32 s51, s51, s52\n"
        "s_add_u32 s56, s56, s52\n"
        "s_lshl_b32 s52, %12, 11\n"
        "s_add_u32 s50, s50, s52\n"
        "v_mbcnt_lo_u32_b32 v11, -1, 0\n"
        "v_mbcnt_hi_u32_b32 v11, -1, v11\n"
        "v_lshlrev_b32 v0, 4, v11\n"
        "v_add_u32 v1, s55, v0\n"
        "v_lshrrev_b32 v8, 4, v11\n"
        "v_lshlrev_b32 v2, 5, v8\n"
        "v_xor_b32 v2, v2, v0\n"
        "v_and_b32 v2, 0x70, v2\n"
        "v_lshrrev_b32 v9, 3, v11\n"
        "v_mul_u32_u24 v9, 1152, v9\n"
        "v_add_u32 v2, v9, v2\n"
        "v_lshrrev_b32 v9, 5, v11\n"
        "v_bfe_u32 v3, v11, 2, 3\n"
        "v_lshl_add_u32 v3, v9, 4, v3\n"
        "v_lshlrev_b32 v3, 7, v3\n"
        "v_and_b32 v9, 3, v11\n"
        "v_lshl_add_u32 v3, v9, 3, v3\n"
        "v_bfe_u32 v9, v11, 3, 2\n"
        "v_lshl_add_u32 v3, v9, 5, v3\n"
        "v_add_u32 v3, s55, v3\n"
        "v_xor_b32 v4, 32, v3\n"
        "v_xor_b32 v5, 64, v3\n"
        "v_xor_b32 v6, 96, v3\n"
        "s_lshl_b32 s58, %10, 5\n"
        "s_add_u32 s58, s58, %8\n"
        "s_lshl2_add_u32 s58, s58, 0x448000\n"
        "v_mov_b32 v7, %11\n"
        "v_add_u32 v8, %12, v7\n"
        "s_mov_b32 s52, 0\n"
        "s_cmp_eq_u32 %9, 1\n"
        "s_cbranch_scc1 30f\n"
        "s_add_u32 m0, s55, 20480\n"
        "s_nop 0\n"
        "buffer_load_dwordx4 v2, s[44:47], s52 offen lds\n"
        "s_add_u32 s52, s52, 9216\n"
        "s_add_u32 m0, s55, 21504\n"
        "s_nop 0\n"
        "buffer_load_dwordx4 v2, s[44:47], s52 offen lds\n"
        "s_add_u32 s52, s52, 9216\n"
        "s_add_u32 m0, s55, 22528\n"
        "s_nop 0\n"
        "buffer_load_dwordx4 v2, s[44:47], s52 offen lds\n"
        "s_add_u32 s52, s52, 9216\n"
        "s_add_u32 m0, s55, 23552\n"
        "s_nop 0\n"
        "buffer_load_dwordx4 v2, s[44:47], s52 offen lds\n"
        "s_add_u32 s52, s52, 9216\n"
        "s_add_u32 m0, s55, 24576\n"
        "s_nop 0\n"
        "buffer_load_dwordx4 v2, s[44:47], s52 offen lds\n"
        "s_add_u32 s52, s52, 9216\n"
        "s_add_u32 m0, s55, 25600\n"
        "s_nop 0\n"
        "buffer_load_dwordx4 v2, s[44:47], s52 offen lds\n"
        "s_add_u32 s52, s52, 9216\n"
        "s_add_u32 m0, s55, 26624\n"
        "s_nop 0\n"
        "buffer_load_dwordx4 v2, s[44:47], s52 offen lds\n"
        "s_add_u32 s52, s52, 9216\n"
        "s_add_u32 m0, s55, 27648\n"
        "s_nop 0\n"
        "buffer_load_dwordx4 v2, s[44:47], s52 offen lds\n"
        "s_add_u32 s52, s52, 9216\n"
        "s_add_u32 m0, s55, 28672\n"
        "s_nop 0\n"
        "buffer_load_dwordx4 v2, s[44:47], s52 offen lds\n"
        "s_add_u32 s52, s52, 9216\n"
        "s_add_u32 m0, s55, 29696\n"
        "s_nop 0\n"
        "buffer_load_dwordx4 v2, s[44:47], s52 offen lds\n"
        "s_add_u32 s52, s52, 9216\n"
        "s_add_u32 m0, s55, 30720\n"
        "s_nop 0\n"
        "buffer_load_dwordx4 v2, s[44:47], s52 offen lds\n"
        "s_add_u32 s52, s52, 9216\n"
        "s_add_u32 m0, s55, 31744\n"
        "s_nop 0\n"
        "buffer_load_dwordx4 v2, s[44:47], s52 offen lds\n"
        "s_add_u32 s52, s52, 9216\n"
        "s_add_u32 m0, s55, 32768\n"
        "s_nop 0\n"
        "buffer_load_dwordx4 v2, s[44:47], s52 offen lds\n"
        "s_add_u32 s52, s52, 9216\n"
        "s_add_u32 m0, s55, 33792\n"
        "s_nop 0\n"
        "buffer_load_dwordx4 v2, s[44:47], s52 offen lds\n"
        "s_add_u32 s52, s52, 9216\n"
        "s_add_u32 m0, s55, 34816\n"
        "s_nop 0\n"
        "buffer_load_dwordx4 v2, s[44:47], s52 offen lds\n"
        "s_add_u32 s52, s52, 9216\n"
        "s_add_u32 m0, s55, 35840\n"
        "s_nop 0\n"
        "buffer_load_dwordx4 v2, s[44:47], s52 offen lds\n"
        "s_add_u32 s52, s52, 9216\n"
        "s_add_u32 m0, s55, 36864\n"
        "s_nop 0\n"
        "buffer_load_dwordx4 v2, s[44:47], s52 offen lds\n"
        "s_add_u32 s52, s52, 9216\n"
        "s_add_u32 m0, s55, 37888\n"
        "s_nop 0\n"
        "buffer_load_dwordx4 v2, s[44:47], s52 offen lds\n"
        "s_add_u32 s52, s52, 9216\n"
        "s_add_u32 m0, s55, 38912\n"
        "s_nop 0\n"
        "buffer_load_dwordx4 v2, s[44:47], s52 offen lds\n"
        "s_add_u32 s52, s52, 9216\n"
        "s_add_u32 m0, s55, 39936\n"
        "s_nop 0\n"
        "buffer_load_dwordx4 v2, s[44:47], s52 offen lds\n"
        "s_add_u32 s52, s52, 9216\n"
        "s_waitcnt vmcnt(16)\n"
        "ds_read_b64_tr_b16 a[0:1], v3 offset:20480\n"
        "ds_read_b64_tr_b16 a[2:3], v3 offset:21504\n"
        "ds_read_b64_tr_b16 a[4:5], v4 offset:20480\n"
        "ds_read_b64_tr_b16 a[6:7], v4 offset:21504\n"
        "ds_read_b64_tr_b16 a[8:9], v5 offset:20480\n"
        "ds_read_b64_tr_b16 a[10:11], v5 offset:21504\n"
        "ds_read_b64_tr_b16 v[12:13], v6 offset:20480\n"
        "ds_read_b64_tr_b16 v[14:15], v6 offset:21504\n"
        "s_waitcnt lgkmcnt(0)\n"
        "ds_write_b128 v1, v[12:15] offset:0\n"
        "s_memrealtime vcc\n"
        "s_waitcnt lgkmcnt(0)\n"
        "v_mov_b32 v10, vcc_lo\n"
        "buffer_store_dword v10, off, s[40:43], s58 sc1\n"
        "s_add_u32 m0, s55, 20480\n"
        "s_nop 0\n"
        "buffer_load_dwordx4 v2, s[44:47], s52 offen lds\n"
        "s_add_u32 s52, s52, 9216\n"
        "s_add_u32 m0, s55, 21504\n"
        "s_nop 0\n"
        "buffer_load_dwordx4 v2, s[44:47], s52 offen lds\n"
        "s_add_u32 s52, s52, 9216\n"
        "s_add_u32 m0, s55, 22528\n"
        "s_nop 0\n"
        "buffer_load_dwordx4 v2, s[44:47], s52 offen lds\n"
        "s_add_u32 s52, s52, 9216\n"
        "s_add_u32 m0, s55, 23552\n"
        "s_nop 0\n"
        "buffer_load_dwordx4 v2, s[44:47], s52 offen lds\n"
        "s_add_u32 s52, s52, 9216\n"
        "s_waitcnt vmcnt(16)\n"
        "ds_read_b64_tr_b16 a[12:13], v3 offset:24576\n"
        "ds_read_b64_tr_b16 a[14:15], v3 offset:25600\n"
        "ds_read_b64_tr_b16 a[16:17], v4 offset:24576\n"
        "ds_read_b64_tr_b16 a[18:19], v4 offset:25600\n"
        "ds_read_b64_tr_b16 a[20:21], v5 offset:24576\n"
        "ds_read_b64_tr_b16 a[22:23], v5 offset:25600\n"
        "ds_read_b64_tr_b16 v[12:13], v6 offset:24576\n"
        "ds_read_b64_tr_b16 v[14:15], v6 offset:25600\n"
        "s_waitcnt lgkmcnt(0)\n"
        "ds_write_b128 v1, v[12:15] offset:1024\n"
        "s_add_u32 m0, s55, 24576\n"
        "s_nop 0\n"
        "buffer_load_dwordx4 v2, s[44:47], s52 offen lds\n"
        "s_add_u32 s52, s52, 9216\n"
        "s_add_u32 m0, s55, 25600\n"
        "s_nop 0\n"
        "buffer_load_dwordx4 v2, s[44:47], s52 offen lds\n"
        "s_add_u32 s52, s52, 9216\n"
        "s_add_u32 m0, s55, 26624\n"
        "s_nop 0\n"
        "buffer_load_dwordx4 v2, s[44:47], s52 offen lds\n"
        "s_add_u32 s52, s52, 9216\n"
        "s_add_u32 m0, s55, 27648\n"
        "s_nop 0\n"
        "buffer_load_dwordx4 v2, s[44:47], s52 offen lds\n"
        "s_add_u32 s52, s52, 9216\n"
        "s_waitcnt vmcnt(16)\n"
        "ds_read_b64_tr_b16 a[24:25], v3 offset:28672\n"
        "ds_read_b64_tr_b16 a[26:27], v3 offset:29696\n"
        "ds_read_b64_tr_b16 a[28:29], v4 offset:28672\n"
        "ds_read_b64_tr_b16 a[30:31], v4 offset:29696\n"
        "ds_read_b64_tr_b16 a[32:33], v5 offset:28672\n"
        "ds_read_b64_tr_b16 a[34:35], v5 offset:29696\n"
        "ds_read_b64_tr_b16 v[12:13], v6 offset:28672\n"
        "ds_read_b64_tr_b16 v[14:15], v6 offset:29696\n"
        "s_waitcnt lgkmcnt(0)\n"
        "ds_write_b128 v1, v[12:15] offset:2048\n"
        "s_add_u32 m0, s55, 28672\n"
        "s_nop 0\n"
        "buffer_load_dwordx4 v2, s[44:47], s52 offen lds\n"
        "s_add_u32 s52, s52, 9216\n"
        "s_add_u32 m0, s55, 29696\n"
        "s_nop 0\n"
        "buffer_load_dwordx4 v2, s[44:47], s52 offen lds\n"
        "s_add_u32 s52, s52, 9216\n"
        "s_add_u32 m0, s55, 30720\n"
        "s_nop 0\n"
        "buffer_load_dwordx4 v2, s[44:47], s52 offen lds\n"
        "s_add_u32 s52, s52, 9216\n"
        "s_add_u32 m0, s55, 31744\n"
        "s_nop 0\n"
        "buffer_load_dwordx4 v2, s[44:47], s52 offen lds\n"
        "s_add_u32 s52, s52, 9216\n"
        "s_waitcnt vmcnt(16)\n"
        "ds_read_b64_tr_b16 a[36:37], v3 offset:32768\n"
        "ds_read_b64_tr_b16 a[38:39], v3 offset:33792\n"
        "ds_read_b64_tr_b16 a[40:41], v4 offset:32768\n"
        "ds_read_b64_tr_b16 a[42:43], v4 offset:33792\n"
        "ds_read_b64_tr_b16 a[44:45], v5 offset:32768\n"
        "ds_read_b64_tr_b16 a[46:47], v5 offset:33792\n"
        "ds_read_b64_tr_b16 v[12:13], v6 offset:32768\n"
        "ds_read_b64_tr_b16 v[14:15], v6 offset:33792\n"
        "s_waitcnt lgkmcnt(0)\n"
        "ds_write_b128 v1, v[12:15] offset:3072\n"
        "s_add_u32 m0, s55, 32768\n"
        "s_nop 0\n"
        "buffer_load_dwordx4 v2, s[44:47], s52 offen lds\n"
        "s_add_u32 s52, s52, 9216\n"
        "s_add_u32 m0, s55, 33792\n"
        "s_nop 0\n"
        "buffer_load_dwordx4 v2, s[44:47], s52 offen lds\n"
        "s_add_u32 s52, s52, 9216\n"
        "s_add_u32 m0, s55, 34816\n"
        "s_nop 0\n"
        "buffer_load_dwordx4 v2, s[44:47], s52 offen lds\n"
        "s_add_u32 s52, s52, 9216\n"
        "s_add_u32 m0, s55, 35840\n"
        "s_nop 0\n"
        "buffer_load_dwordx4 v2, s[44:47], s52 offen lds\n"
        "s_add_u32 s52, s52, 9216\n"
        "s_waitcnt vmcnt(16)\n"
        "ds_read_b64_tr_b16 a[48:49], v3 offset:36864\n"
        "ds_read_b64_tr_b16 a[50:51], v3 offset:37888\n"
        "ds_read_b64_tr_b16 a[52:53], v4 offset:36864\n"
        "ds_read_b64_tr_b16 a[54:55], v4 offset:37888\n"
        "ds_read_b64_tr_b16 a[56:57], v5 offset:36864\n"
        "ds_read_b64_tr_b16 a[58:59], v5 offset:37888\n"
        "ds_read_b64_tr_b16 v[12:13], v6 offset:36864\n"
        "ds_read_b64_tr_b16 v[14:15], v6 offset:37888\n"
        "s_waitcnt lgkmcnt(0)\n"
        "ds_write_b128 v1, v[12:15] offset:4096\n"
        "s_memrealtime vcc\n"
        "s_waitcnt lgkmcnt(0)\n"
        "v_mov_b32 v10, vcc_lo\n"
        "buffer_store_dword v10, off, s[40:43], s58 sc1\n"
        "s_add_u32 m0, s55, 36864\n"
        "s_nop 0\n"
        "buffer_load_dwordx4 v2, s[44:47], s52 offen lds\n"
        "s_add_u32 s52, s52, 9216\n"
        "s_add_u32 m0, s55, 37888\n"
        "s_nop 0\n"
        "buffer_load_dwordx4 v2, s[44:47], s52 offen lds\n"
        "s_add_u32 s52, s52, 9216\n"
        "s_add_u32 m0, s55, 38912\n"
        "s_nop 0\n"
        "buffer_load_dwordx4 v2, s[44:47], s52 offen lds\n"
        "s_add_u32 s52, s52, 9216\n"
        "s_add_u32 m0, s55, 39936\n"
        "s_nop 0\n"
        "buffer_load_dwordx4 v2, s[44:47], s52 offen lds\n"
        "s_add_u32 s52, s52, 9216\n"
        "s_waitcnt vmcnt(16)\n"
        "ds_read_b64_tr_b16 a[60:61], v3 offset:20480\n"
        "ds_read_b64_tr_b16 a[62:63], v3 offset:21504\n"
        "ds_read_b64_tr_b16 a[64:65], v4 offset:20480\n"
        "ds_read_b64_tr_b16 a[66:67], v4 offset:21504\n"
        "ds_read_b64_tr_b16 a[68:69], v5 offset:20480\n"
        "ds_read_b64_tr_b16 a[70:71], v5 offset:21504\n"
        "ds_read_b64_tr_b16 v[12:13], v6 offset:20480\n"
        "ds_read_b64_tr_b16 v[14:15], v6 offset:21504\n"
        "s_waitcnt lgkmcnt(0)\n"
        "ds_write_b128 v1, v[12:15] offset:5120\n"
        "s_add_u32 m0, s55, 20480\n"
        "s_nop 0\n"
        "buffer_load_dwordx4 v2, s[44:47], s52 offen lds\n"
        "s_add_u32 s52, s52, 9216\n"
        "s_add_u32 m0, s55, 21504\n"
        "s_nop 0\n"
        "buffer_load_dwordx4 v2, s[44:47], s52 offen lds\n"
        "s_add_u32 s52, s52, 9216\n"
        "s_add_u32 m0, s55, 22528\n"
        "s_nop 0\n"
        "buffer_load_dwordx4 v2, s[44:47], s52 offen lds\n"
        "s_add_u32 s52, s52, 9216\n"
        "s_add_u32 m0, s55, 23552\n"
        "s_nop 0\n"
        "buffer_load_dwordx4 v2, s[44:47], s52 offen lds\n"
        "s_add_u32 s52, s52, 9216\n"
        "s_waitcnt vmcnt(16)\n"
        "ds_read_b64_tr_b16 a[72:73], v3 offset:24576\n"
        "ds_read_b64_tr_b16 a[74:75], v3 offset:25600\n"
        "ds_read_b64_tr_b16 a[76:77], v4 offset:24576\n"
        "ds_read_b64_tr_b16 a[78:79], v4 offset:25600\n"
        "ds_read_b64_tr_b16 a[80:81], v5 offset:24576\n"
        "ds_read_b64_tr_b16 a[82:83], v5 offset:25600\n"
        "ds_read_b64_tr_b16 v[12:13], v6 offset:24576\n"
        "ds_read_b64_tr_b16 v[14:15], v6 offset:25600\n"
        "s_waitcnt lgkmcnt(0)\n"
        "ds_write_b128 v1, v[12:15] offset:6144\n"
        "s_add_u32 m0, s55, 24576\n"
        "s_nop 0\n"
        "buffer_load_dwordx4 v2, s[44:47], s52 offen lds\n"
        "s_add_u32 s52, s52, 9216\n"
        "s_add_u32 m0, s55, 25600\n"
        "s_nop 0\n"
        "buffer_load_dwordx4 v2, s[44:47], s52 offen lds\n"
        "s_add_u32 s52, s52, 9216\n"
        "s_add_u32 m0, s55, 26624\n"
        "s_nop 0\n"
        "buffer_load_dwordx4 v2, s[44:47], s52 offen lds\n"
        "s_add_u32 s52, s52, 9216\n"
        "s_add_u32 m0, s55, 27648\n"
        "s_nop 0\n"
        "buffer_load_dwordx4 v2, s[44:47], s52 offen lds\n"
        "s_add_u32 s52, s52, 9216\n"
        "s_waitcnt vmcnt(16)\n"
        "ds_read_b64_tr_b16 a[84:85], v3 offset:28672\n"
        "ds_read_b64_tr_b16 a[86:87], v3 offset:29696\n"
        "ds_read_b64_tr_b16 a[88:89], v4 offset:28672\n"
        "ds_read_b64_tr_b16 a[90:91], v4 offset:29696\n"
        "ds_read_b64_tr_b16 a[92:93], v5 offset:28672\n"
        "ds_read_b64_tr_b16 a[94:95], v5 offset:29696\n"
        "ds_read_b64_tr_b16 v[12:13], v6 offset:28672\n"
        "ds_read_b64_tr_b16 v[14:15], v6 offset:29696\n"
        "s_waitcnt lgkmcnt(0)\n"
        "ds_write_b128 v1, v[12:15] offset:7168\n"
        "s_add_u32 m0, s55, 28672\n"
        "s_nop 0\n"
        "buffer_load_dwordx4 v2, s[44:47], s52 offen lds\n"
        "s_add_u32 s52, s52, 9216\n"
        "s_add_u32 m0, s55, 29696\n"
        "s_nop 0\n"
        "buffer_load_dwordx4 v2, s[44:47], s52 offen lds\n"
        "s_add_u32 s52, s52, 9216\n"
        "s_add_u32 m0, s55, 30720\n"
        "s_nop 0\n"
        "buffer_load_dwordx4 v2, s[44:47], s52 offen lds\n"
        "s_add_u32 s52, s52, 9216\n"
        "s_add_u32 m0, s55, 31744\n"
        "s_nop 0\n"
        "buffer_load_dwordx4 v2, s[44:47], s52 offen lds\n"
        "s_add_u32 s52, s52, 9216\n"
        "s_waitcnt vmcnt(16)\n"
        "ds_read_b64_tr_b16 a[96:97], v3 offset:32768\n"
        "ds_read_b64_tr_b16 a[98:99], v3 offset:33792\n"
        "ds_read_b64_tr_b16 a[100:101], v4 offset:32768\n"
        "ds_read_b64_tr_b16 a[102:103], v4 offset:33792\n"
        "ds_read_b64_tr_b16 a[104:105], v5 offset:32768\n"
        "ds_read_b64_tr_b16 a[106:107], v5 offset:33792\n"
        "ds_read_b64_tr_b16 v[12:13], v6 offset:32768\n"
        "ds_read_b64_tr_b16 v[14:15], v6 offset:33792\n"
        "s_waitcnt lgkmcnt(0)\n"
        "ds_write_b128 v1, v[12:15] offset:8192\n"
        "s_add_u32 m0, s55, 32768\n"
        "s_nop 0\n"
        "buffer_load_dwordx4 v2, s[44:47], s52 offen lds\n"
        "s_add_u32 s52, s52, 9216\n"
        "s_add_u32 m0, s55, 33792\n"
        "s_nop 0\n"
        "buffer_load_dwordx4 v2, s[44:47], s52 offen lds\n"
        "s_add_u32 s52, s52, 9216\n"
        "s_add_u32 m0, s55, 34816\n"
        "s_nop 0\n"
        "buffer_load_dwordx4 v2, s[44:47], s52 offen lds\n"
        "s_add_u32 s52, s52, 9216\n"
        "s_add_u32 m0, s55, 35840\n"
        "s_nop 0\n"
        "buffer_load_dwordx4 v2, s[44:47], s52 offen lds\n"
        "s_add_u32 s52, s52, 9216\n"
        "s_waitcnt vmcnt(16)\n"
        "ds_read_b64_tr_b16 a[108:109], v3 offset:36864\n"
        "ds_read_b64_tr_b16 a[110:111], v3 offset:37888\n"
        "ds_read_b64_tr_b16 a[112:113], v4 offset:36864\n"
        "ds_read_b64_tr_b16 a[114:115], v4 offset:37888\n"
        "ds_read_b64_tr_b16 a[116:117], v5 offset:36864\n"
        "ds_read_b64_tr_b16 a[118:119], v5 offset:37888\n"
        "ds_read_b64_tr_b16 v[12:13], v6 offset:36864\n"
        "ds_read_b64_tr_b16 v[14:15], v6 offset:37888\n"
        "s_waitcnt lgkmcnt(0)\n"
        "ds_write_b128 v1, v[12:15] offset:9216\n"
        "s_add_u32 m0, s55, 36864\n"
        "s_nop 0\n"
        "buffer_load_dwordx4 v2, s[44:47], s52 offen lds\n"
        "s_add_u32 s52, s52, 9216\n"
        "s_add_u32 m0, s55, 37888\n"
        "s_nop 0\n"
        "buffer_load_dwordx4 v2, s[44:47], s52 offen lds\n"
        "s_add_u32 s52, s52, 9216\n"
        "s_add_u32 m0, s55, 38912\n"
        "s_nop 0\n"
        "buffer_load_dwordx4 v2, s[44:47], s52 offen lds\n"
        "s_add_u32 s52, s52, 9216\n"
        "s_add_u32 m0, s55, 39936\n"
        "s_nop 0\n"
        "buffer_load_dwordx4 v2, s[44:47], s52 offen lds\n"
        "s_add_u32 s52, s52, 9216\n"
        "s_waitcnt vmcnt(16)\n"
        "ds_read_b64_tr_b16 a[120:121], v3 offset:20480\n"
        "ds_read_b64_tr_b16 a[122:123], v3 offset:21504\n"
        "ds_read_b64_tr_b16 a[124:125], v4 offset:20480\n"
        "ds_read_b64_tr_b16 a[126:127], v4 offset:21504\n"
        "ds_read_b64_tr_b16 a[128:129], v5 offset:20480\n"
        "ds_read_b64_tr_b16 a[130:131], v5 offset:21504\n"
        "ds_read_b64_tr_b16 v[12:13], v6 offset:20480\n"
        "ds_read_b64_tr_b16 v[14:15], v6 offset:21504\n"
        "s_waitcnt lgkmcnt(0)\n"
        "ds_write_b128 v1, v[12:15] offset:10240\n"
        "s_add_u32 m0, s55, 20480\n"
        "s_nop 0\n"
        "buffer_load_dwordx4 v2, s[44:47], s52 offen lds\n"
        "s_add_u32 s52, s52, 9216\n"
        "s_add_u32 m0, s55, 21504\n"
        "s_nop 0\n"
        "buffer_load_dwordx4 v2, s[44:47], s52 offen lds\n"
        "s_add_u32 s52, s52, 9216\n"
        "s_add_u32 m0, s55, 22528\n"
        "s_nop 0\n"
        "buffer_load_dwordx4 v2, s[44:47], s52 offen lds\n"
        "s_add_u32 s52, s52, 9216\n"
        "s_add_u32 m0, s55, 23552\n"
        "s_nop 0\n"
        "buffer_load_dwordx4 v2, s[44:47], s52 offen lds\n"
        "s_add_u32 s52, s52, 9216\n"
        "s_waitcnt vmcnt(16)\n"
        "ds_read_b64_tr_b16 a[132:133], v3 offset:24576\n"
        "ds_read_b64_tr_b16 a[134:135], v3 offset:25600\n"
        "ds_read_b64_tr_b16 a[136:137], v4 offset:24576\n"
        "ds_read_b64_tr_b16 a[138:139], v4 offset:25600\n"
        "ds_read_b64_tr_b16 a[140:141], v5 offset:24576\n"
        "ds_read_b64_tr_b16 a[142:143], v5 offset:25600\n"
        "ds_read_b64_tr_b16 v[12:13], v6 offset:24576\n"
        "ds_read_b64_tr_b16 v[14:15], v6 offset:25600\n"
        "s_waitcnt lgkmcnt(0)\n"
        "ds_write_b128 v1, v[12:15] offset:11264\n"
        "s_add_u32 m0, s55, 24576\n"
        "s_nop 0\n"
        "buffer_load_dwordx4 v2, s[44:47], s52 offen lds\n"
        "s_add_u32 s52, s52, 9216\n"
        "s_add_u32 m0, s55, 25600\n"
        "s_nop 0\n"
        "buffer_load_dwordx4 v2, s[44:47], s52 offen lds\n"
        "s_add_u32 s52, s52, 9216\n"
        "s_add_u32 m0, s55, 26624\n"
        "s_nop 0\n"
        "buffer_load_dwordx4 v2, s[44:47], s52 offen lds\n"
        "s_add_u32 s52, s52, 9216\n"
        "s_add_u32 m0, s55, 27648\n"
        "s_nop 0\n"
        "buffer_load_dwordx4 v2, s[44:47], s52 offen lds\n"
        "s_add_u32 s52, s52, 9216\n"
        "s_waitcnt vmcnt(16)\n"
        "ds_read_b64_tr_b16 a[144:145], v3 offset:28672\n"
        "ds_read_b64_tr_b16 a[146:147], v3 offset:29696\n"
        "ds_read_b64_tr_b16 a[148:149], v4 offset:28672\n"
        "ds_read_b64_tr_b16 a[150:151], v4 offset:29696\n"
        "ds_read_b64_tr_b16 a[152:153], v5 offset:28672\n"
        "ds_read_b64_tr_b16 a[154:155], v5 offset:29696\n"
        "ds_read_b64_tr_b16 v[12:13], v6 offset:28672\n"
        "ds_read_b64_tr_b16 v[14:15], v6 offset:29696\n"
        "s_waitcnt lgkmcnt(0)\n"
        "ds_write_b128 v1, v[12:15] offset:12288\n"
        "s_memrealtime vcc\n"
        "s_waitcnt lgkmcnt(0)\n"
        "v_mov_b32 v10, vcc_lo\n"
        "buffer_store_dword v10, off, s[40:43], s58 sc1\n"
        "s_add_u32 m0, s55, 28672\n"
        "s_nop 0\n"
        "buffer_load_dwordx4 v2, s[44:47], s52 offen lds\n"
        "s_add_u32 s52, s52, 9216\n"
        "s_add_u32 m0, s55, 29696\n"
        "s_nop 0\n"
        "buffer_load_dwordx4 v2, s[44:47], s52 offen lds\n"
        "s_add_u32 s52, s52, 9216\n"
        "s_add_u32 m0, s55, 30720\n"
        "s_nop 0\n"
        "buffer_load_dwordx4 v2, s[44:47], s52 offen lds\n"
        "s_add_u32 s52, s52, 9216\n"
        "s_add_u32 m0, s55, 31744\n"
        "s_nop 0\n"
        "buffer_load_dwordx4 v2, s[44:47], s52 offen lds\n"
        "s_add_u32 s52, s52, 9216\n"
        "s_waitcnt vmcnt(16)\n"
        "ds_read_b64_tr_b16 a[156:157], v3 offset:32768\n"
        "ds_read_b64_tr_b16 a[158:159], v3 offset:33792\n"
        "ds_read_b64_tr_b16 a[160:161], v4 offset:32768\n"
        "ds_read_b64_tr_b16 a[162:163], v4 offset:33792\n"
        "ds_read_b64_tr_b16 a[164:165], v5 offset:32768\n"
        "ds_read_b64_tr_b16 a[166:167], v5 offset:33792\n"
        "ds_read_b64_tr_b16 v[12:13], v6 offset:32768\n"
        "ds_read_b64_tr_b16 v[14:15], v6 offset:33792\n"
        "s_waitcnt lgkmcnt(0)\n"
        "ds_write_b128 v1, v[12:15] offset:13312\n"
        "s_add_u32 m0, s55, 32768\n"
        "s_nop 0\n"
        "buffer_load_dwordx4 v2, s[44:47], s52 offen lds\n"
        "s_add_u32 s52, s52, 9216\n"
        "s_add_u32 m0, s55, 33792\n"
        "s_nop 0\n"
        "buffer_load_dwordx4 v2, s[44:47], s52 offen lds\n"
        "s_add_u32 s52, s52, 9216\n"
        "s_add_u32 m0, s55, 34816\n"
        "s_nop 0\n"
        "buffer_load_dwordx4 v2, s[44:47], s52 offen lds\n"
        "s_add_u32 s52, s52, 9216\n"
        "s_add_u32 m0, s55, 35840\n"
        "s_nop 0\n"
        "buffer_load_dwordx4 v2, s[44:47], s52 offen lds\n"
        "s_add_u32 s52, s52, 9216\n"
        "s_waitcnt vmcnt(16)\n"
        "ds_read_b64_tr_b16 a[168:169], v3 offset:36864\n"
        "ds_read_b64_tr_b16 a[170:171], v3 offset:37888\n"
        "ds_read_b64_tr_b16 a[172:173], v4 offset:36864\n"
        "ds_read_b64_tr_b16 a[174:175], v4 offset:37888\n"
        "ds_read_b64_tr_b16 a[176:177], v5 offset:36864\n"
        "ds_read_b64_tr_b16 a[178:179], v5 offset:37888\n"
        "ds_read_b64_tr_b16 v[12:13], v6 offset:36864\n"
        "ds_read_b64_tr_b16 v[14:15], v6 offset:37888\n"
        "s_waitcnt lgkmcnt(0)\n"
        "ds_write_b128 v1, v[12:15] offset:14336\n"
        "s_add_u32 m0, s55, 36864\n"
        "s_nop 0\n"
        "buffer_load_dwordx4 v2, s[44:47], s52 offen lds\n"
        "s_add_u32 s52, s52, 9216\n"
        "s_add_u32 m0, s55, 37888\n"
        "s_nop 0\n"
        "buffer_load_dwordx4 v2, s[44:47], s52 offen lds\n"
        "s_add_u32 s52, s52, 9216\n"
        "s_add_u32 m0, s55, 38912\n"
        "s_nop 0\n"
        "buffer_load_dwordx4 v2, s[44:47], s52 offen lds\n"
        "s_add_u32 s52, s52, 9216\n"
        "s_add_u32 m0, s55, 39936\n"
        "s_nop 0\n"
        "buffer_load_dwordx4 v2, s[44:47], s52 offen lds\n"
        "s_add_u32 s52, s52, 9216\n"
        "s_waitcnt vmcnt(16)\n"
        "ds_read_b64_tr_b16 a[180:181], v3 offset:20480\n"
        "ds_read_b64_tr_b16 a[182:183], v3 offset:21504\n"
        "ds_read_b64_tr_b16 a[184:185], v4 offset:20480\n"
        "ds_read_b64_tr_b16 a[186:187], v4 offset:21504\n"
        "ds_read_b64_tr_b16 a[188:189], v5 offset:20480\n"
        "ds_read_b64_tr_b16 a[190:191], v5 offset:21504\n"
        "ds_read_b64_tr_b16 v[12:13], v6 offset:20480\n"
        "ds_read_b64_tr_b16 v[14:15], v6 offset:21504\n"
        "s_waitcnt lgkmcnt(0)\n"
        "ds_write_b128 v1, v[12:15] offset:15360\n"
        "s_add_u32 m0, s55, 20480\n"
        "s_nop 0\n"
        "buffer_load_dwordx4 v2, s[44:47], s52 offen lds\n"
        "s_add_u32 s52, s52, 9216\n"
        "s_add_u32 m0, s55, 21504\n"
        "s_nop 0\n"
        "buffer_load_dwordx4 v2, s[44:47], s52 offen lds\n"
        "s_add_u32 s52, s52, 9216\n"
        "s_add_u32 m0, s55, 22528\n"
        "s_nop 0\n"
        "buffer_load_dwordx4 v2, s[44:47], s52 offen lds\n"
        "s_add_u32 s52, s52, 9216\n"
        "s_add_u32 m0, s55, 23552\n"
        "s_nop 0\n"
        "buffer_load_dwordx4 v2, s[44:47], s52 offen lds\n"
        "s_add_u32 s52, s52, 9216\n"
        "s_waitcnt vmcnt(16)\n"
        "ds_read_b64_tr_b16 a[192:193], v3 offset:24576\n"
        "ds_read_b64_tr_b16 a[194:195], v3 offset:25600\n"
        "ds_read_b64_tr_b16 a[196:197], v4 offset:24576\n"
        "ds_read_b64_tr_b16 a[198:199], v4 offset:25600\n"
        "ds_read_b64_tr_b16 a[200:201], v5 offset:24576\n"
        "ds_read_b64_tr_b16 a[202:203], v5 offset:25600\n"
        "ds_read_b64_tr_b16 v[12:13], v6 offset:24576\n"
        "ds_read_b64_tr_b16 v[14:15], v6 offset:25600\n"
        "s_waitcnt lgkmcnt(0)\n"
        "ds_write_b128 v1, v[12:15] offset:16384\n"
        "s_add_u32 m0, s55, 24576\n"
        "s_nop 0\n"
        "buffer_load_dwordx4 v2, s[44:47], s52 offen lds\n"
        "s_add_u32 s52, s52, 9216\n"
        "s_add_u32 m0, s55, 25600\n"
        "s_nop 0\n"
        "buffer_load_dwordx4 v2, s[44:47], s52 offen lds\n"
        "s_add_u32 s52, s52, 9216\n"
        "s_add_u32 m0, s55, 26624\n"
        "s_nop 0\n"
        "buffer_load_dwordx4 v2, s[44:47], s52 offen lds\n"
        "s_add_u32 s52, s52, 9216\n"
        "s_add_u32 m0, s55, 27648\n"
        "s_nop 0\n"
        "buffer_load_dwordx4 v2, s[44:47], s52 offen lds\n"
        "s_add_u32 s52, s52, 9216\n"
        "s_waitcnt vmcnt(16)\n"
        "ds_read_b64_tr_b16 a[204:205], v3 offset:28672\n"
        "ds_read_b64_tr_b16 a[206:207], v3 offset:29696\n"
        "ds_read_b64_tr_b16 a[208:209], v4 offset:28672\n"
        "ds_read_b64_tr_b16 a[210:211], v4 offset:29696\n"
        "ds_read_b64_tr_b16 a[212:213], v5 offset:28672\n"
        "ds_read_b64_tr_b16 a[214:215], v5 offset:29696\n"
        "ds_read_b64_tr_b16 v[12:13], v6 offset:28672\n"
        "ds_read_b64_tr_b16 v[14:15], v6 offset:29696\n"
        "s_waitcnt lgkmcnt(0)\n"
        "ds_write_b128 v1, v[12:15] offset:17408\n"
        "s_add_u32 m0, s55, 28672\n"
        "s_nop 0\n"
        "buffer_load_dwordx4 v2, s[44:47], s52 offen lds\n"
        "s_add_u32 s52, s52, 9216\n"
        "s_add_u32 m0, s55, 29696\n"
        "s_nop 0\n"
        "buffer_load_dwordx4 v2, s[44:47], s52 offen lds\n"
        "s_add_u32 s52, s52, 9216\n"
        "s_add_u32 m0, s55, 30720\n"
        "s_nop 0\n"
        "buffer_load_dwordx4 v2, s[44:47], s52 offen lds\n"
        "s_add_u32 s52, s52, 9216\n"
        "s_add_u32 m0, s55, 31744\n"
        "s_nop 0\n"
        "buffer_load_dwordx4 v2, s[44:47], s52 offen lds\n"
        "s_add_u32 s52, s52, 9216\n"
        "s_waitcnt vmcnt(16)\n"
        "ds_read_b64_tr_b16 a[216:217], v3 offset:32768\n"
        "ds_read_b64_tr_b16 a[218:219], v3 offset:33792\n"
        "ds_read_b64_tr_b16 a[220:221], v4 offset:32768\n"
        "ds_read_b64_tr_b16 a[222:223], v4 offset:33792\n"
        "ds_read_b64_tr_b16 a[224:225], v5 offset:32768\n"
        "ds_read_b64_tr_b16 a[226:227], v5 offset:33792\n"
        "ds_read_b64_tr_b16 v[12:13], v6 offset:32768\n"
        "ds_read_b64_tr_b16 v[14:15], v6 offset:33792\n"
        "s_waitcnt lgkmcnt(0)\n"
        "ds_write_b128 v1, v[12:15] offset:18432\n"
        "s_add_u32 m0, s55, 32768\n"
        "s_nop 0\n"
        "buffer_load_dwordx4 v2, s[44:47], s52 offen lds\n"
        "s_add_u32 s52, s52, 9216\n"
        "s_add_u32 m0, s55, 33792\n"
        "s_nop 0\n"
        "buffer_load_dwordx4 v2, s[44:47], s52 offen lds\n"
        "s_add_u32 s52, s52, 9216\n"
        "s_add_u32 m0, s55, 34816\n"
        "s_nop 0\n"
        "buffer_load_dwordx4 v2, s[44:47], s52 offen lds\n"
        "s_add_u32 s52, s52, 9216\n"
        "s_add_u32 m0, s55, 35840\n"
        "s_nop 0\n"
        "buffer_load_dwordx4 v2, s[44:47], s52 offen lds\n"
        "s_add_u32 s52, s52, 9216\n"
        "s_waitcnt vmcnt(16)\n"
        "ds_read_b64_tr_b16 a[228:229], v3 offset:36864\n"
        "ds_read_b64_tr_b16 a[230:231], v3 offset:37888\n"
        "ds_read_b64_tr_b16 a[232:233], v4 offset:36864\n"
        "ds_read_b64_tr_b16 a[234:235], v4 offset:37888\n"
        "ds_read_b64_tr_b16 a[236:237], v5 offset:36864\n"
        "ds_read_b64_tr_b16 a[238:239], v5 offset:37888\n"
        "ds_read_b64_tr_b16 v[12:13], v6 offset:36864\n"
        "ds_read_b64_tr_b16 v[14:15], v6 offset:37888\n"
        "s_waitcnt lgkmcnt(0)\n"
        "ds_write_b128 v1, v[12:15] offset:19456\n"
        "s_add_u32 m0, s55, 36864\n"
        "s_nop 0\n"
        "buffer_load_dwordx4 v2, s[44:47], s52 offen lds\n"
        "s_add_u32 s52, s52, 9216\n"
        "s_add_u32 m0, s55, 37888\n"
        "s_nop 0\n"
        "buffer_load_dwordx4 v2, s[44:47], s52 offen lds\n"
        "s_add_u32 s52, s52, 9216\n"
        "s_add_u32 m0, s55, 38912\n"
        "s_nop 0\n"
        "buffer_load_dwordx4 v2, s[44:47], s52 offen lds\n"
        "s_add_u32 s52, s52, 9216\n"
        "s_add_u32 m0, s55, 39936\n"
        "s_nop 0\n"
        "buffer_load_dwordx4 v2, s[44:47], s52 offen lds\n"
        "s_add_u32 s52, s52, 9216\n"
        "s_waitcnt vmcnt(16)\n"
        "ds_read_b64_tr_b16 a[240:241], v3 offset:20480\n"
        "ds_read_b64_tr_b16 a[242:243], v3 offset:21504\n"
        "ds_read_b64_tr_b16 a[244:245], v4 offset:20480\n"
        "ds_read_b64_tr_b16 a[246:247], v4 offset:21504\n"
        "ds_read_b64_tr_b16 a[248:249], v5 offset:20480\n"
        "ds_read_b64_tr_b16 a[250:251], v5 offset:21504\n"
        "ds_read_b64_tr_b16 v[16:17], v6 offset:20480\n"
        "ds_read_b64_tr_b16 v[18:19], v6 offset:21504\n"
        "s_waitcnt lgkmcnt(0)\n"
        "s_add_u32 m0, s55, 20480\n"
        "s_nop 0\n"
        "buffer_load_dwordx4 v2, s[44:47], s52 offen lds\n"
        "s_add_u32 s52, s52, 9216\n"
        "s_add_u32 m0, s55, 21504\n"
        "s_nop 0\n"
        "buffer_load_dwordx4 v2, s[44:47], s52 offen lds\n"
        "s_add_u32 s52, s52, 9216\n"
        "s_add_u32 m0, s55, 22528\n"
        "s_nop 0\n"
        "buffer_load_dwordx4 v2, s[44:47], s52 offen lds\n"
        "s_add_u32 s52, s52, 9216\n"
        "s_add_u32 m0, s55, 23552\n"
        "s_nop 0\n"
        "buffer_load_dwordx4 v2, s[44:47], s52 offen lds\n"
        "s_add_u32 s52, s52, 9216\n"
        "s_waitcnt vmcnt(16)\n"
        "ds_read_b64_tr_b16 a[252:253], v3 offset:24576\n"
        "ds_read_b64_tr_b16 a[254:255], v3 offset:25600\n"
        "ds_read_b64_tr_b16 v[80:81], v4 offset:24576\n"
        "ds_read_b64_tr_b16 v[82:83], v4 offset:25600\n"
        "ds_read_b64_tr_b16 v[84:85], v5 offset:24576\n"
        "ds_read_b64_tr_b16 v[86:87], v5 offset:25600\n"
        "ds_read_b64_tr_b16 v[20:21], v6 offset:24576\n"
        "ds_read_b64_tr_b16 v[22:23], v6 offset:25600\n"
        "s_waitcnt lgkmcnt(0)\n"
        "s_add_u32 m0, s55, 24576\n"
        "s_nop 0\n"
        "buffer_load_dwordx4 v2, s[44:47], s52 offen lds\n"
        "s_add_u32 s52, s52, 9216\n"
        "s_add_u32 m0, s55, 25600\n"
        "s_nop 0\n"
        "buffer_load_dwordx4 v2, s[44:47], s52 offen lds\n"
        "s_add_u32 s52, s52, 9216\n"
        "s_add_u32 m0, s55, 26624\n"
        "s_nop 0\n"
        "buffer_load_dwordx4 v2, s[44:47], s52 offen lds\n"
        "s_add_u32 s52, s52, 9216\n"
        "s_add_u32 m0, s55, 27648\n"
        "s_nop 0\n"
        "buffer_load_dwordx4 v2, s[44:47], s52 offen lds\n"
        "s_add_u32 s52, s52, 9216\n"
        "s_waitcnt vmcnt(16)\n"
        "ds_read_b64_tr_b16 v[88:89], v3 offset:28672\n"
        "ds_read_b64_tr_b16 v[90:91], v3 offset:29696\n"
        "ds_read_b64_tr_b16 v[92:93], v4 offset:28672\n"
        "ds_read_b64_tr_b16 v[94:95], v4 offset:29696\n"
        "ds_read_b64_tr_b16 v[96:97], v5 offset:28672\n"
        "ds_read_b64_tr_b16 v[98:99], v5 offset:29696\n"
        "ds_read_b64_tr_b16 v[24:25], v6 offset:28672\n"
        "ds_read_b64_tr_b16 v[26:27], v6 offset:29696\n"
        "s_waitcnt lgkmcnt(0)\n"
        "s_add_u32 m0, s55, 28672\n"
        "s_nop 0\n"
        "buffer_load_dwordx4 v2, s[44:47], s52 offen lds\n"
        "s_add_u32 s52, s52, 9216\n"
        "s_add_u32 m0, s55, 29696\n"
        "s_nop 0\n"
        "buffer_load_dwordx4 v2, s[44:47], s52 offen lds\n"
        "s_add_u32 s52, s52, 9216\n"
        "s_add_u32 m0, s55, 30720\n"
        "s_nop 0\n"
        "buffer_load_dwordx4 v2, s[44:47], s52 offen lds\n"
        "s_add_u32 s52, s52, 9216\n"
        "s_add_u32 m0, s55, 31744\n"
        "s_nop 0\n"
        "buffer_load_dwordx4 v2, s[44:47], s52 offen lds\n"
        "s_add_u32 s52, s52, 9216\n"
        "s_waitcnt vmcnt(16)\n"
        "ds_read_b64_tr_b16 v[100:101], v3 offset:32768\n"
        "ds_read_b64_tr_b16 v[102:103], v3 offset:33792\n"
        "ds_read_b64_tr_b16 v[104:105], v4 offset:32768\n"
        "ds_read_b64_tr_b16 v[106:107], v4 offset:33792\n"
        "ds_read_b64_tr_b16 v[108:109], v5 offset:32768\n"
        "ds_read_b64_tr_b16 v[110:111], v5 offset:33792\n"
        "ds_read_b64_tr_b16 v[28:29], v6 offset:32768\n"
        "ds_read_b64_tr_b16 v[30:31], v6 offset:33792\n"
        "s_waitcnt lgkmcnt(0)\n"
        "s_add_u32 m0, s55, 32768\n"
        "s_nop 0\n"
        "buffer_load_dwordx4 v2, s[44:47], s52 offen lds\n"
        "s_add_u32 s52, s52, 9216\n"
        "s_add_u32 m0, s55, 33792\n"
        "s_nop 0\n"
        "buffer_load_dwordx4 v2, s[44:47], s52 offen lds\n"
        "s_add_u32 s52, s52, 9216\n"
        "s_add_u32 m0, s55, 34816\n"
        "s_nop 0\n"
        "buffer_load_dwordx4 v2, s[44:47], s52 offen lds\n"
        "s_add_u32 s52, s52, 9216\n"
        "s_add_u32 m0, s55, 35840\n"
        "s_nop 0\n"
        "buffer_load_dwordx4 v2, s[44:47], s52 offen lds\n"
        "s_add_u32 s52, s52, 9216\n"
        "s_waitcnt vmcnt(16)\n"
        "ds_read_b64_tr_b16 v[112:113], v3 offset:36864\n"
        "ds_read_b64_tr_b16 v[114:115], v3 offset:37888\n"
        "ds_read_b64_tr_b16 v[116:117], v4 offset:36864\n"
        "ds_read_b64_tr_b16 v[118:119], v4 offset:37888\n"
        "ds_read_b64_tr_b16 v[120:121], v5 offset:36864\n"
        "ds_read_b64_tr_b16 v[122:123], v5 offset:37888\n"
        "ds_read_b64_tr_b16 v[32:33], v6 offset:36864\n"
        "ds_read_b64_tr_b16 v[34:35], v6 offset:37888\n"
        "s_waitcnt lgkmcnt(0)\n"
        "s_add_u32 m0, s55, 36864\n"
        "s_nop 0\n"
        "buffer_load_dwordx4 v2, s[44:47], s52 offen lds\n"
        "s_add_u32 s52, s52, 9216\n"
        "s_add_u32 m0, s55, 37888\n"
        "s_nop 0\n"
        "buffer_load_dwordx4 v2, s[44:47], s52 offen lds\n"
        "s_add_u32 s52, s52, 9216\n"
        "s_add_u32 m0, s55, 38912\n"
        "s_nop 0\n"
        "buffer_load_dwordx4 v2, s[44:47], s52 offen lds\n"
        "s_add_u32 s52, s52, 9216\n"
        "s_add_u32 m0, s55, 39936\n"
        "s_nop 0\n"
        "buffer_load_dwordx4 v2, s[44:47], s52 offen lds\n"
        "s_add_u32 s52, s52, 9216\n"
        "s_waitcnt vmcnt(16)\n"
        "ds_read_b64_tr_b16 v[124:125], v3 offset:20480\n"
        "ds_read_b64_tr_b16 v[126:127], v3 offset:21504\n"
        "ds_read_b64_tr_b16 v[128:129], v4 offset:20480\n"
        "ds_read_b64_tr_b16 v[130:131], v4 offset:21504\n"
        "ds_read_b64_tr_b16 v[132:133], v5 offset:20480\n"
        "ds_read_b64_tr_b16 v[134:135], v5 offset:21504\n"
        "ds_read_b64_tr_b16 v[36:37], v6 offset:20480\n"
        "ds_read_b64_tr_b16 v[38:39], v6 offset:21504\n"
        "s_waitcnt lgkmcnt(0)\n"
        "s_add_u32 m0, s55, 20480\n"
        "s_nop 0\n"
        "buffer_load_dwordx4 v2, s[44:47], s52 offen lds\n"
        "s_add_u32 s52, s52, 9216\n"
        "s_add_u32 m0, s55, 21504\n"
        "s_nop 0\n"
        "buffer_load_dwordx4 v2, s[44:47], s52 offen lds\n"
        "s_add_u32 s52, s52, 9216\n"
        "s_add_u32 m0, s55, 22528\n"
        "s_nop 0\n"
        "buffer_load_dwordx4 v2, s[44:47], s52 offen lds\n"
        "s_add_u32 s52, s52, 9216\n"
        "s_add_u32 m0, s55, 23552\n"
        "s_nop 0\n"
        "buffer_load_dwordx4 v2, s[44:47], s52 offen lds\n"
        "s_add_u32 s52, s52, 9216\n"
        "s_waitcnt vmcnt(16)\n"
        "ds_read_b64_tr_b16 v[136:137], v3 offset:24576\n"
        "ds_read_b64_tr_b16 v[138:139], v3 offset:25600\n"
        "ds_read_b64_tr_b16 v[140:141], v4 offset:24576\n"
        "ds_read_b64_tr_b16 v[142:143], v4 offset:25600\n"
        "ds_read_b64_tr_b16 v[144:145], v5 offset:24576\n"
        "ds_read_b64_tr_b16 v[146:147], v5 offset:25600\n"
        "ds_read_b64_tr_b16 v[40:41], v6 offset:24576\n"
        "ds_read_b64_tr_b16 v[42:43], v6 offset:25600\n"
        "s_waitcnt lgkmcnt(0)\n"
        "s_add_u32 m0, s55, 24576\n"
        "s_nop 0\n"
        "buffer_load_dwordx4 v2, s[44:47], s52 offen lds\n"
        "s_add_u32 s52, s52, 9216\n"
        "s_add_u32 m0, s55, 25600\n"
        "s_nop 0\n"
        "buffer_load_dwordx4 v2, s[44:47], s52 offen lds\n"
        "s_add_u32 s52, s52, 9216\n"
        "s_add_u32 m0, s55, 26624\n"
        "s_nop 0\n"
        "buffer_load_dwordx4 v2, s[44:47], s52 offen lds\n"
        "s_add_u32 s52, s52, 9216\n"
        "s_add_u32 m0, s55, 27648\n"
        "s_nop 0\n"
        "buffer_load_dwordx4 v2, s[44:47], s52 offen lds\n"
        "s_add_u32 s52, s52, 9216\n"
        "s_waitcnt vmcnt(16)\n"
        "ds_read_b64_tr_b16 v[148:149], v3 offset:28672\n"
        "ds_read_b64_tr_b16 v[150:151], v3 offset:29696\n"
        "ds_read_b64_tr_b16 v[152:153], v4 offset:28672\n"
        "ds_read_b64_tr_b16 v[154:155], v4 offset:29696\n"
        "ds_read_b64_tr_b16 v[156:157], v5 offset:28672\n"
        "ds_read_b64_tr_b16 v[158:159], v5 offset:29696\n"
        "ds_read_b64_tr_b16 v[44:45], v6 offset:28672\n"
        "ds_read_b64_tr_b16 v[46:47], v6 offset:29696\n"
        "s_waitcnt lgkmcnt(0)\n"
        "s_add_u32 m0, s55, 28672\n"
        "s_nop 0\n"
        "buffer_load_dwordx4 v2, s[44:47], s52 offen lds\n"
        "s_add_u32 s52, s52, 9216\n"
        "s_add_u32 m0, s55, 29696\n"
        "s_nop 0\n"
        "buffer_load_dwordx4 v2, s[44:47], s52 offen lds\n"
        "s_add_u32 s52, s52, 9216\n"
        "s_add_u32 m0, s55, 30720\n"
        "s_nop 0\n"
        "buffer_load_dwordx4 v2, s[44:47], s52 offen lds\n"
        "s_add_u32 s52, s52, 9216\n"
        "s_add_u32 m0, s55, 31744\n"
        "s_nop 0\n"
        "buffer_load_dwordx4 v2, s[44:47], s52 offen lds\n"
        "s_add_u32 s52, s52, 9216\n"
        "s_waitcnt vmcnt(16)\n"
        "ds_read_b64_tr_b16 v[160:161], v3 offset:32768\n"
        "ds_read_b64_tr_b16 v[162:163], v3 offset:33792\n"
        "ds_read_b64_tr_b16 v[164:165], v4 offset:32768\n"
        "ds_read_b64_tr_b16 v[166:167], v4 offset:33792\n"
        "ds_read_b64_tr_b16 v[168:169], v5 offset:32768\n"
        "ds_read_b64_tr_b16 v[170:171], v5 offset:33792\n"
        "ds_read_b64_tr_b16 v[48:49], v6 offset:32768\n"
        "ds_read_b64_tr_b16 v[50:51], v6 offset:33792\n"
        "s_waitcnt lgkmcnt(0)\n"
        "s_add_u32 m0, s55, 32768\n"
        "s_nop 0\n"
        "buffer_load_dwordx4 v2, s[44:47], s52 offen lds\n"
        "s_add_u32 s52, s52, 9216\n"
        "s_add_u32 m0, s55, 33792\n"
        "s_nop 0\n"
        "buffer_load_dwordx4 v2, s[44:47], s52 offen lds\n"
        "s_add_u32 s52, s52, 9216\n"
        "s_add_u32 m0, s55, 34816\n"
        "s_nop 0\n"
        "buffer_load_dwordx4 v2, s[44:47], s52 offen lds\n"
        "s_add_u32 s52, s52, 9216\n"
        "s_add_u32 m0, s55, 35840\n"
        "s_nop 0\n"
        "buffer_load_dwordx4 v2, s[44:47], s52 offen lds\n"
        "s_add_u32 s52, s52, 9216\n"
        "s_waitcnt vmcnt(16)\n"
        "ds_read_b64_tr_b16 v[172:173], v3 offset:36864\n"
        "ds_read_b64_tr_b16 v[174:175], v3 offset:37888\n"
        "ds_read_b64_tr_b16 v[176:177], v4 offset:36864\n"
        "ds_read_b64_tr_b16 v[178:179], v4 offset:37888\n"
        "ds_read_b64_tr_b16 v[180:181], v5 offset:36864\n"
        "ds_read_b64_tr_b16 v[182:183], v5 offset:37888\n"
        "ds_read_b64_tr_b16 v[52:53], v6 offset:36864\n"
        "ds_read_b64_tr_b16 v[54:55], v6 offset:37888\n"
        "s_waitcnt lgkmcnt(0)\n"
        "s_add_u32 m0, s55, 36864\n"
        "s_nop 0\n"
        "buffer_load_dwordx4 v2, s[44:47], s52 offen lds\n"
        "s_add_u32 s52, s52, 9216\n"
        "s_add_u32 m0, s55, 37888\n"
        "s_nop 0\n"
        "buffer_load_dwordx4 v2, s[44:47], s52 offen lds\n"
        "s_add_u32 s52, s52, 9216\n"
        "s_add_u32 m0, s55, 38912\n"
        "s_nop 0\n"
        "buffer_load_dwordx4 v2, s[44:47], s52 offen lds\n"
        "s_add_u32 s52, s52, 9216\n"
        "s_add_u32 m0, s55, 39936\n"
        "s_nop 0\n"
        "buffer_load_dwordx4 v2, s[44:47], s52 offen lds\n"
        "s_add_u32 s52, s52, 9216\n"
        "s_waitcnt vmcnt(16)\n"
        "ds_read_b64_tr_b16 v[184:185], v3 offset:20480\n"
        "ds_read_b64_tr_b16 v[186:187], v3 offset:21504\n"
        "ds_read_b64_tr_b16 v[188:189], v4 offset:20480\n"
        "ds_read_b64_tr_b16 v[190:191], v4 offset:21504\n"
        "ds_read_b64_tr_b16 v[192:193], v5 offset:20480\n"
        "ds_read_b64_tr_b16 v[194:195], v5 offset:21504\n"
        "ds_read_b64_tr_b16 v[56:57], v6 offset:20480\n"
        "ds_read_b64_tr_b16 v[58:59], v6 offset:21504\n"
        "s_waitcnt lgkmcnt(0)\n"
        "s_add_u32 m0, s55, 20480\n"
        "s_nop 0\n"
        "buffer_load_dwordx4 v2, s[44:47], s52 offen lds\n"
        "s_add_u32 s52, s52, 9216\n"
        "s_add_u32 m0, s55, 21504\n"
        "s_nop 0\n"
        "buffer_load_dwordx4 v2, s[44:47], s52 offen lds\n"
        "s_add_u32 s52, s52, 9216\n"
        "s_add_u32 m0, s55, 22528\n"
        "s_nop 0\n"
        "buffer_load_dwordx4 v2, s[44:47], s52 offen lds\n"
        "s_add_u32 s52, s52, 9216\n"
        "s_add_u32 m0, s55, 23552\n"
        "s_nop 0\n"
        "buffer_load_dwordx4 v2, s[44:47], s52 offen lds\n"
        "s_add_u32 s52, s52, 9216\n"
        "s_waitcnt vmcnt(16)\n"
        "ds_read_b64_tr_b16 v[196:197], v3 offset:24576\n"
        "ds_read_b64_tr_b16 v[198:199], v3 offset:25600\n"
        "ds_read_b64_tr_b16 v[200:201], v4 offset:24576\n"
        "ds_read_b64_tr_b16 v[202:203], v4 offset:25600\n"
        "ds_read_b64_tr_b16 v[204:205], v5 offset:24576\n"
        "ds_read_b64_tr_b16 v[206:207], v5 offset:25600\n"
        "ds_read_b64_tr_b16 v[60:61], v6 offset:24576\n"
        "ds_read_b64_tr_b16 v[62:63], v6 offset:25600\n"
        "s_waitcnt lgkmcnt(0)\n"
        "s_waitcnt vmcnt(12)\n"
        "ds_read_b64_tr_b16 v[208:209], v3 offset:28672\n"
        "ds_read_b64_tr_b16 v[210:211], v3 offset:29696\n"
        "ds_read_b64_tr_b16 v[212:213], v4 offset:28672\n"
        "ds_read_b64_tr_b16 v[214:215], v4 offset:29696\n"
        "ds_read_b64_tr_b16 v[216:217], v5 offset:28672\n"
        "ds_read_b64_tr_b16 v[218:219], v5 offset:29696\n"
        "ds_read_b64_tr_b16 v[64:65], v6 offset:28672\n"
        "ds_read_b64_tr_b16 v[66:67], v6 offset:29696\n"
        "s_waitcnt lgkmcnt(0)\n"
        "s_waitcnt vmcnt(8)\n"
        "ds_read_b64_tr_b16 v[220:221], v3 offset:32768\n"
        "ds_read_b64_tr_b16 v[222:223], v3 offset:33792\n"
        "ds_read_b64_tr_b16 v[224:225], v4 offset:32768\n"
        "ds_read_b64_tr_b16 v[226:227], v4 offset:33792\n"
        "ds_read_b64_tr_b16 v[228:229], v5 offset:32768\n"
        "ds_read_b64_tr_b16 v[230:231], v5 offset:33792\n"
        "ds_read_b64_tr_b16 v[68:69], v6 offset:32768\n"
        "ds_read_b64_tr_b16 v[70:71], v6 offset:33792\n"
        "s_waitcnt lgkmcnt(0)\n"
        "s_waitcnt vmcnt(4)\n"
        "ds_read_b64_tr_b16 v[232:233], v3 offset:36864\n"
        "ds_read_b64_tr_b16 v[234:235], v3 offset:37888\n"
        "ds_read_b64_tr_b16 v[236:237], v4 offset:36864\n"
        "ds_read_b64_tr_b16 v[238:239], v4 offset:37888\n"
        "ds_read_b64_tr_b16 v[240:241], v5 offset:36864\n"
        "ds_read_b64_tr_b16 v[242:243], v5 offset:37888\n"
        "ds_read_b64_tr_b16 v[72:73], v6 offset:36864\n"
        "ds_read_b64_tr_b16 v[74:75], v6 offset:37888\n"
        "s_waitcnt lgkmcnt(0)\n"
        "s_waitcnt vmcnt(0)\n"
        "ds_read_b64_tr_b16 v[244:245], v3 offset:20480\n"
        "ds_read_b64_tr_b16 v[246:247], v3 offset:21504\n"
        "ds_read_b64_tr_b16 v[248:249], v4 offset:20480\n"
        "ds_read_b64_tr_b16 v[250:251], v4 offset:21504\n"
        "ds_read_b64_tr_b16 v[252:253], v5 offset:20480\n"
        "ds_read_b64_tr_b16 v[254:255], v5 offset:21504\n"
        "ds_read_b64_tr_b16 v[76:77], v6 offset:20480\n"
        "ds_read_b64_tr_b16 v[78:79], v6 offset:21504\n"
        "s_waitcnt lgkmcnt(0)\n"
        "ds_write_b128 v1, v[16:19] offset:20480\n"
        "ds_write_b128 v1, v[20:23] offset:21504\n"
        "ds_write_b128 v1, v[24:27] offset:22528\n"
        "ds_write_b128 v1, v[28:31] offset:23552\n"
        "ds_write_b128 v1, v[32:35] offset:24576\n"
        "s_waitcnt lgkmcnt(0)\n"
        "ds_write_b128 v1, v[36:39] offset:25600\n"
        "ds_write_b128 v1, v[40:43] offset:26624\n"
        "ds_write_b128 v1, v[44:47] offset:27648\n"
        "ds_write_b128 v1, v[48:51] offset:28672\n"
        "ds_write_b128 v1, v[52:55] offset:29696\n"
        "ds_write_b128 v1, v[56:59] offset:30720\n"
        "ds_write_b128 v1, v[60:63] offset:31744\n"
        "ds_write_b128 v1, v[64:67] offset:32768\n"
        "s_waitcnt lgkmcnt(0)\n"
        "ds_write_b128 v1, v[68:71] offset:33792\n"
        "ds_write_b128 v1, v[72:75] offset:34816\n"
        "ds_write_b128 v1, v[76:79] offset:35840\n"
        "s_mov_b32 s53, 0x10000\n"
        "s_lshl_b32 s52, %10, 7\n"
        "s_add_u32 s52, s52, 0x440000\n"
        "60:\n"
        "v_cmp_ne_u32 vcc, v7, v8\n"
        "s_cbranch_vccnz 61f\n"
        "s_sub_u32 s53, s53, 1\n"
        "s_cbranch_scc1 61f\n"
        "s_and_b32 vcc_lo, s53, 15\n"
        "s_cbranch_scc1 62f\n"
        "s_memrealtime vcc\n"
        "s_waitcnt lgkmcnt(0)\n"
        "v_mov_b32 v10, vcc_lo\n"
        "buffer_store_dword v10, off, s[40:43], s58 sc1\n"
        "62:\n"
        "buffer_load_dwordx2 v[8:9], off, s[40:43], s52 sc1\n"
        "s_waitcnt vmcnt(0)\n"
        "s_branch 60b\n"
        "61:\n"
        "s_lshl2_add_u32 s52, %10, %8\n"
        "s_lshl2_add_u32 s52, s52, 0x425800\n"
        "s_memrealtime vcc\n"
        "s_waitcnt lgkmcnt(0)\n"
        "v_or_b32 v7, vcc_lo, 1\n"
        "buffer_store_dword v7, off, s[40:43], s52 sc1\n"
        "v_lshrrev_b32 v8, 4, v11\n"
        "v_and_b32 v9, 15, v11\n"
        "v_lshlrev_b32 v7, 3, v11\n"
        "v_mul_u32_u24 v10, 1152, v8\n"
        "v_lshl_add_u32 v10, v9, 3, v10\n"
        "v_xor_b32 v5, v8, v9\n"
        "v_lshlrev_b32 v5, 4, v5\n"
        "s_lshl_b32 s52, %8, 6\n"
        "v_xor_b32 v6, s52, v5\n"
        "v_lshl_add_u32 v6, v9, 8, v6\n"
        "v_add_u32 v6, 36864, v6\n"
        "v_lshl_add_u32 v5, v8, 8, v5\n"
        "v_add_u32 v5, s55, v5\n"
        "v_lshrrev_b32 v8, 5, v11\n"
        "v_lshl_add_u32 v4, v8, 1, 1\n"
        "v_add_u32 v4, s48, v4\n"
        "v_and_b32 v2, 31, v11\n"
        "s_and_b32 s52, %6, 15\n"
        "s_lshl_b32 s52, s52, 12\n"
        "v_lshl_add_u32 v2, v2, 7, s52\n"
        "v_mad_u32_u24 v2, v8, %3, v2\n"
        "s_add_u32 s50, s50, 0x415000\n"
        "v_and_b32 v9, 31, v11\n"
        "v_min_u32 v9, 17, v9\n"
        "v_lshl_add_u32 v9, v9, 2, s50\n"
        "v_lshl_add_u32 v3, v8, 11, v9\n"
        "s_waitcnt lgkmcnt(0)\n"
        "10:\n"
        "buffer_load_dword v8, v3, s[40:43], 0 offen sc1\n"
        "s_waitcnt vmcnt(0)\n"
        "v_cmp_gt_u32 vcc, v4, v8\n"
        "s_cmp_eq_u32 vcc_lo, 0\n"
        "s_cbranch_scc1 11f\n"
        "s_cmp_eq_u32 s54, 0\n"
        "s_cbranch_scc1 11f\n"
        "s_sub_u32 s54, s54, 1\n"
        "s_sleep 8\n"
        "s_branch 10b\n"
        "11:\n"
        "s_cmp_eq_u32 vcc_hi, 0\n"
        "s_cselect_b32 exec_hi, -1, 0\n"
        "s_nop 0\n"
        "s_mov_b32 exec_hi, -1\n"
        "s_add_u32 s52, s49, 0\n"
        "buffer_load_dwordx4 v[36:39], v0, s[40:43], s52 offen offset:0 sc1\n"
        "buffer_load_dwordx4 v[40:43], v0, s[40:43], s52 offen offset:1024 sc1\n"
        "buffer_load_dwordx4 v[44:47], v0, s[40:43], s52 offen offset:2048 sc1\n"
        "buffer_load_dwordx4 v[48:51], v0, s[40:43], s52 offen offset:3072 sc1\n"
        "s_add_u32 s52, s49, 4096\n"
        "buffer_load_dwordx4 v[52:55], v0, s[40:43], s52 offen offset:0 sc1\n"
        "buffer_load_dwordx4 v[56:59], v0, s[40:43], s52 offen offset:1024 sc1\n"
        "buffer_load_dwordx4 v[60:63], v0, s[40:43], s52 offen offset:2048 sc1\n"
        "buffer_load_dwordx4 v[64:67], v0, s[40:43], s52 offen offset:3072 sc1\n"
        "s_add_u32 s52, s49, 8192\n"
        "buffer_load_dwordx4 v[68:71], v0, s[40:43], s52 offen offset:0 sc1\n"
        "buffer_load_dwordx4 v[72:75], v0, s[40:43], s52 offen offset:1024 sc1\n"
        "v_add_u32 v4, 2, v4\n"
        "v_add_u32 v3, 2048, v3\n"
        "12:\n"
        "ds_read_b128 v[28:31], v1 offset:0\n"
        "s_waitcnt vmcnt(9)\n"
        "v_mfma_f32_16x16x32_bf16 v[12:15], a[0:3], v[36:39], 0\n"
        "buffer_load_dwordx4 v[76:79], v0, s[40:43], s52 offen offset:2048 sc1\n"
        "v_mfma_f32_16x16x32_bf16 v[16:19], a[4:7], v[36:39], 0\n"
        "ds_read_b128 v[32:35], v1 offset:1024\n"
        "v_mfma_f32_16x16x32_bf16 v[20:23], a[8:11], v[36:39], 0\n"
        "s_waitcnt lgkmcnt(1)\n"
        "v_mfma_f32_16x16x32_bf16 v[24:27], v[28:31], v[36:39], 0\n"
        "s_waitcnt vmcnt(9)\n"
        "v_mfma_f32_16x16x32_bf16 v[12:15], a[12:15], v[40:43], v[12:15]\n"
        "buffer_load_dwordx4 v[36:39], v0, s[40:43], s52 offen offset:3072 sc1\n"
        "v_mfma_f32_16x16x32_bf16 v[16:19], a[16:19], v[40:43], v[16:19]\n"
        "ds_read_b128 v[28:31], v1 offset:2048\n"
        "v_mfma_f32_16x16x32_bf16 v[20:23], a[20:23], v[40:43], v[20:23]\n"
        "s_waitcnt lgkmcnt(1)\n"
        "v_mfma_f32_16x16x32_bf16 v[24:27], v[32:35], v[40:43], v[24:27]\n"
        "s_waitcnt vmcnt(9)\n"
        "v_mfma_f32_16x16x32_bf16 v[12:15], a[24:27], v[44:47], v[12:15]\n"
        "s_add_u32 s52, s49, 12288\n"
        "buffer_load_dwordx4 v[40:43], v0, s[40:43], s52 offen offset:0 sc1\n"
        "v_mfma_f32_16x16x32_bf16 v[16:19], a[28:31], v[44:47], v[16:19]\n"
        "ds_read_b128 v[32:35], v1 offset:3072\n"
        "v_mfma_f32_16x16x32_bf16 v[20:23], a[32:35], v[44:47], v[20:23]\n"
        "s_waitcnt lgkmcnt(1)\n"
        "v_mfma_f32_16x16x32_bf16 v[24:27], v[28:31], v[44:47], v[24:27]\n"
        "s_waitcnt vmcnt(9)\n"
        "v_mfma_f32_16x16x32_bf16 v[12:15], a[36:39], v[48:51], v[12:15]\n"
        "buffer_load_dwordx4 v[44:47], v0, s[40:43], s52 offen offset:1024 sc1\n"
        "v_mfma_f32_16x16x32_bf16 v[16:19], a[40:43], v[48:51], v[16:19]\n"
        "ds_read_b128 v[28:31], v1 offset:4096\n"
        "v_mfma_f32_16x16x32_bf16 v[20:23], a[44:47], v[48:51], v[20:23]\n"
        "s_waitcnt lgkmcnt(1)\n"
        "v_mfma_f32_16x16x32_bf16 v[24:27], v[32:35], v[48:51], v[24:27]\n"
        "s_waitcnt vmcnt(9)\n"
        "v_mfma_f32_16x16x32_bf16 v[12:15], a[48:51], v[52:55], v[12:15]\n"
        "buffer_load_dwordx4 v[48:51], v0, s[40:43], s52 offen offset:2048 sc1\n"
        "v_mfma_f32_16x16x32_bf16 v[16:19], a[52:55], v[52:55], v[16:19]\n"
        "ds_read_b128 v[32:35], v1 offset:5120\n"
        "v_mfma_f32_16x16x32_bf16 v[20:23], a[56:59], v[52:55], v[20:23]\n"
        "s_waitcnt lgkmcnt(1)\n"
        "v_mfma_f32_16x16x32_bf16 v[24:27], v[28:31], v[52:55], v[24:27]\n"
        "s_waitcnt vmcnt(9)\n"
        "v_mfma_f32_16x16x32_bf16 v[12:15], a[60:63], v[56:59], v[12:15]\n"
        "buffer_load_dwordx4 v[52:55], v0, s[40:43], s52 offen offset:3072 sc1\n"
        "v_mfma_f32_16x16x32_bf16 v[16:19], a[64:67], v[56:59], v[16:19]\n"
        "ds_read_b128 v[28:31], v1 offset:6144\n"
        "v_mfma_f32_16x16x32_bf16 v[20:23], a[68:71], v[56:59], v[20:23]\n"
        "s_waitcnt lgkmcnt(1)\n"
        "v_mfma_f32_16x16x32_bf16 v[24:27], v[32:35], v[56:59], v[24:27]\n"
        "s_waitcnt vmcnt(9)\n"
        "v_mfma_f32_16x16x32_bf16 v[12:15], a[72:75], v[60:63], v[12:15]\n"
        "s_add_u32 s52, s49, 16384\n"
        "buffer_load_dwordx4 v[56:59], v0, s[40:43], s52 offen offset:0 sc1\n"
        "v_mfma_f32_16x16x32_bf16 v[16:19], a[76:79], v[60:63], v[16:19]\n"
        "ds_read_b128 v[32:35], v1 offset:7168\n"
        "v_mfma_f32_16x16x32_bf16 v[20:23], a[80:83], v[60:63], v[20:23]\n"
        "s_waitcnt lgkmcnt(1)\n"
        "v_mfma_f32_16x16x32_bf16 v[24:27], v[28:31], v[60:63], v[24:27]\n"
        "s_waitcnt vmcnt(9)\n"
        "v_mfma_f32_16x16x32_bf16 v[12:15], a[84:87], v[64:67], v[12:15]\n"
        "buffer_load_dwordx4 v[60:63], v0, s[40:43], s52 offen offset:1024 sc1\n"
        "v_mfma_f32_16x16x32_bf16 v[16:19], a[88:91], v[64:67], v[16:19]\n"
        "ds_read_b128 v[28:31], v1 offset:8192\n"
        "v_mfma_f32_16x16x32_bf16 v[20:23], a[92:95], v[64:67], v[20:23]\n"
        "s_waitcnt lgkmcnt(1)\n"
        "v_mfma_f32_16x16x32_bf16 v[24:27], v[32:35], v[64:67], v[24:27]\n"
        "s_waitcnt vmcnt(9)\n"
        "v_mfma_f32_16x16x32_bf16 v[12:15], a[96:99], v[68:71], v[12:15]\n"
        "buffer_load_dwordx4 v[64:67], v0, s[40:43], s52 offen offset:2048 sc1\n"
        "v_mfma_f32_16x16x32_bf16 v[16:19], a[100:103], v[68:71], v[16:19]\n"
        "ds_read_b128 v[32:35], v1 offset:9216\n"
        "v_mfma_f32_16x16x32_bf16 v[20:23], a[104:107], v[68:71], v[20:23]\n"
        "s_waitcnt lgkmcnt(1)\n"
        "v_mfma_f32_16x16x32_bf16 v[24:27], v[28:31], v[68:71], v[24:27]\n"
        "s_waitcnt vmcnt(9)\n"
        "v_mfma_f32_16x16x32_bf16 v[12:15], a[108:111], v[72:75], v[12:15]\n"
        "buffer_load_dwordx4 v[68:71], v0, s[40:43], s52 offen offset:3072 sc1\n"
        "v_mfma_f32_16x16x32_bf16 v[16:19], a[112:115], v[72:75], v[16:19]\n"
        "ds_read_b128 v[28:31], v1 offset:10240\n"
        "v_mfma_f32_16x16x32_bf16 v[20:23], a[116:119], v[72:75], v[20:23]\n"
        "s_waitcnt lgkmcnt(1)\n"
        "v_mfma_f32_16x16x32_bf16 v[24:27], v[32:35], v[72:75], v[24:27]\n"
        "s_waitcnt vmcnt(9)\n"
        "v_mfma_f32_16x16x32_bf16 v[12:15], a[120:123], v[76:79], v[12:15]\n"
        "s_add_u32 s52, s49, 20480\n"
        "buffer_load_dwordx4 v[72:75], v0, s[40:43], s52 offen offset:0 sc1\n"
        "v_mfma_f32_16x16x32_bf16 v[16:19], a[124:127], v[76:79], v[16:19]\n"
        "ds_read_b128 v[32:35], v1 offset:11264\n"
        "v_mfma_f32_16x16x32_bf16 v[20:23], a[128:131], v[76:79], v[20:23]\n"
        "s_waitcnt lgkmcnt(1)\n"
        "v_mfma_f32_16x16x32_bf16 v[24:27], v[28:31], v[76:79], v[24:27]\n"
        "s_waitcnt vmcnt(9)\n"
        "v_mfma_f32_16x16x32_bf16 v[12:15], a[132:135], v[36:39], v[12:15]\n"
        "buffer_load_dwordx4 v[76:79], v0, s[40:43], s52 offen offset:1024 sc1\n"
        "v_mfma_f32_16x16x32_bf16 v[16:19], a[136:139], v[36:39], v[16:19]\n"
        "ds_read_b128 v[28:31], v1 offset:12288\n"
        "v_mfma_f32_16x16x32_bf16 v[20:23], a[140:143], v[36:39], v[20:23]\n"
        "s_waitcnt lgkmcnt(1)\n"
        "v_mfma_f32_16x16x32_bf16 v[24:27], v[32:35], v[36:39], v[24:27]\n"
        "s_waitcnt vmcnt(9)\n"
        "v_mfma_f32_16x16x32_bf16 v[12:15], a[144:147], v[40:43], v[12:15]\n"
        "buffer_load_dwordx4 v[36:39], v0, s[40:43], s52 offen offset:2048 sc1\n"
        "v_mfma_f32_16x16x32_bf16 v[16:19], a[148:151], v[40:43], v[16:19]\n"
        "ds_read_b128 v[32:35], v1 offset:13312\n"
        "v_mfma_f32_16x16x32_bf16 v[20:23], a[152:155], v[40:43], v[20:23]\n"
        "s_waitcnt lgkmcnt(1)\n"
        "v_mfma_f32_16x16x32_bf16 v[24:27], v[28:31], v[40:43], v[24:27]\n"
        "s_waitcnt vmcnt(9)\n"
        "v_mfma_f32_16x16x32_bf16 v[12:15], a[156:159], v[44:47], v[12:15]\n"
        "buffer_load_dwordx4 v[40:43], v0, s[40:43], s52 offen offset:3072 sc1\n"
        "v_mfma_f32_16x16x32_bf16 v[16:19], a[160:163], v[44:47], v[16:19]\n"
        "ds_read_b128 v[28:31], v1 offset:14336\n"
        "v_mfma_f32_16x16x32_bf16 v[20:23], a[164:167], v[44:47], v[20:23]\n"
        "s_waitcnt lgkmcnt(1)\n"
        "v_mfma_f32_16x16x32_bf16 v[24:27], v[32:35], v[44:47], v[24:27]\n"
        "s_waitcnt vmcnt(9)\n"
        "v_mfma_f32_16x16x32_bf16 v[12:15], a[168:171], v[48:51], v[12:15]\n"
        "s_add_u32 s52, s49, 24576\n"
        "buffer_load_dwordx4 v[44:47], v0, s[40:43], s52 offen offset:0 sc1\n"
        "v_mfma_f32_16x16x32_bf16 v[16:19], a[172:175], v[48:51], v[16:19]\n"
        "ds_read_b128 v[32:35], v1 offset:15360\n"
        "v_mfma_f32_16x16x32_bf16 v[20:23], a[176:179], v[48:51], v[20:23]\n"
        "s_waitcnt lgkmcnt(1)\n"
        "v_mfma_f32_16x16x32_bf16 v[24:27], v[28:31], v[48:51], v[24:27]\n"
        "s_waitcnt vmcnt(9)\n"
        "v_mfma_f32_16x16x32_bf16 v[12:15], a[180:183], v[52:55], v[12:15]\n"
        "buffer_load_dwordx4 v[48:51], v0, s[40:43], s52 offen offset:1024 sc1\n"
        "v_mfma_f32_16x16x32_bf16 v[16:19], a[184:187], v[52:55], v[16:19]\n"
        "ds_read_b128 v[28:31], v1 offset:16384\n"
        "v_mfma_f32_16x16x32_bf16 v[20:23], a[188:191], v[52:55], v[20:23]\n"
        "s_waitcnt lgkmcnt(1)\n"
        "v_mfma_f32_16x16x32_bf16 v[24:27], v[32:35], v[52:55], v[24:27]\n"
        "s_waitcnt vmcnt(9)\n"
        "v_mfma_f32_16x16x32_bf16 v[12:15], a[192:195], v[56:59], v[12:15]\n"
        "buffer_load_dwordx4 v[52:55], v0, s[40:43], s52 offen offset:2048 sc1\n"
        "v_mfma_f32_16x16x32_bf16 v[16:19], a[196:199], v[56:59], v[16:19]\n"
        "ds_read_b128 v[32:35], v1 offset:17408\n"
        "v_mfma_f32_16x16x32_bf16 v[20:23], a[200:203], v[56:59], v[20:23]\n"
        "s_waitcnt lgkmcnt(1)\n"
        "v_mfma_f32_16x16x32_bf16 v[24:27], v[28:31], v[56:59], v[24:27]\n"
        "s_waitcnt vmcnt(9)\n"
        "v_mfma_f32_16x16x32_bf16 v[12:15], a[204:207], v[60:63], v[12:15]\n"
        "buffer_load_dwordx4 v[56:59], v0, s[40:43], s52 offen offset:3072 sc1\n"
        "v_mfma_f32_16x16x32_bf16 v[16:19], a[208:211], v[60:63], v[16:19]\n"
        "ds_read_b128 v[28:31], v1 offset:18432\n"
        "v_mfma_f32_16x16x32_bf16 v[20:23], a[212:215], v[60:63], v[20:23]\n"
        "s_waitcnt lgkmcnt(1)\n"
        "v_mfma_f32_16x16x32_bf16 v[24:27], v[32:35], v[60:63], v[24:27]\n"
        "s_waitcnt vmcnt(9)\n"
        "v_mfma_f32_16x16x32_bf16 v[12:15], a[216:219], v[64:67], v[12:15]\n"
        "s_add_u32 s52, s49, 28672\n"
        "buffer_load_dwordx4 v[60:63], v0, s[40:43], s52 offen offset:0 sc1\n"
        "v_mfma_f32_16x16x32_bf16 v[16:19], a[220:223], v[64:67], v[16:19]\n"
        "ds_read_b128 v[32:35], v1 offset:19456\n"
        "v_mfma_f32_16x16x32_bf16 v[20:23], a[224:227], v[64:67], v[20:23]\n"
        "s_waitcnt lgkmcnt(1)\n"
        "v_mfma_f32_16x16x32_bf16 v[24:27], v[28:31], v[64:67], v[24:27]\n"
        "s_waitcnt vmcnt(9)\n"
        "v_mfma_f32_16x16x32_bf16 v[12:15], a[228:231], v[68:71], v[12:15]\n"
        "buffer_load_dwordx4 v[64:67], v0, s[40:43], s52 offen offset:1024 sc1\n"
        "v_mfma_f32_16x16x32_bf16 v[16:19], a[232:235], v[68:71], v[16:19]\n"
        "ds_read_b128 v[28:31], v1 offset:20480\n"
        "v_mfma_f32_16x16x32_bf16 v[20:23], a[236:239], v[68:71], v[20:23]\n"
        "s_waitcnt lgkmcnt(1)\n"
        "v_mfma_f32_16x16x32_bf16 v[24:27], v[32:35], v[68:71], v[24:27]\n"
        "s_waitcnt vmcnt(9)\n"
        "v_mfma_f32_16x16x32_bf16 v[12:15], a[240:243], v[72:75], v[12:15]\n"
        "buffer_load_dwordx4 v[68:71], v0, s[40:43], s52 offen offset:2048 sc1\n"
        "v_mfma_f32_16x16x32_bf16 v[16:19], a[244:247], v[72:75], v[16:19]\n"
        "ds_read_b128 v[32:35], v1 offset:21504\n"
        "v_mfma_f32_16x16x32_bf16 v[20:23], a[248:251], v[72:75], v[20:23]\n"
        "s_waitcnt lgkmcnt(1)\n"
        "v_mfma_f32_16x16x32_bf16 v[24:27], v[28:31], v[72:75], v[24:27]\n"
        "s_waitcnt vmcnt(9)\n"
        "v_mfma_f32_16x16x32_bf16 v[12:15], a[252:255], v[76:79], v[12:15]\n"
        "buffer_load_dwordx4 v[72:75], v0, s[40:43], s52 offen offset:3072 sc1\n"
        "v_mfma_f32_16x16x32_bf16 v[16:19], v[80:83], v[76:79], v[16:19]\n"
        "ds_read_b128 v[28:31], v1 offset:22528\n"
        "v_mfma_f32_16x16x32_bf16 v[20:23], v[84:87], v[76:79], v[20:23]\n"
        "s_waitcnt lgkmcnt(1)\n"
        "v_mfma_f32_16x16x32_bf16 v[24:27], v[32:35], v[76:79], v[24:27]\n"
        "s_waitcnt vmcnt(9)\n"
        "v_mfma_f32_16x16x32_bf16 v[12:15], v[88:91], v[36:39], v[12:15]\n"
        "s_add_u32 s52, s49, 32768\n"
        "buffer_load_dwordx4 v[76:79], v0, s[40:43], s52 offen offset:0 sc1\n"
        "v_mfma_f32_16x16x32_bf16 v[16:19], v[92:95], v[36:39], v[16:19]\n"
        "ds_read_b128 v[32:35], v1 offset:23552\n"
        "v_mfma_f32_16x16x32_bf16 v[20:23], v[96:99], v[36:39], v[20:23]\n"
        "s_waitcnt lgkmcnt(1)\n"
        "v_mfma_f32_16x16x32_bf16 v[24:27], v[28:31], v[36:39], v[24:27]\n"
        "s_waitcnt vmcnt(9)\n"
        "v_mfma_f32_16x16x32_bf16 v[12:15], v[100:103], v[40:43], v[12:15]\n"
        "buffer_load_dwordx4 v[36:39], v0, s[40:43], s52 offen offset:1024 sc1\n"
        "v_mfma_f32_16x16x32_bf16 v[16:19], v[104:107], v[40:43], v[16:19]\n"
        "ds_read_b128 v[28:31], v1 offset:24576\n"
        "v_mfma_f32_16x16x32_bf16 v[20:23], v[108:111], v[40:43], v[20:23]\n"
        "s_waitcnt lgkmcnt(1)\n"
        "v_mfma_f32_16x16x32_bf16 v[24:27], v[32:35], v[40:43], v[24:27]\n"
        "s_waitcnt vmcnt(9)\n"
        "v_mfma_f32_16x16x32_bf16 v[12:15], v[112:115], v[44:47], v[12:15]\n"
        "buffer_load_dwordx4 v[40:43], v0, s[40:43], s52 offen offset:2048 sc1\n"
        "v_mfma_f32_16x16x32_bf16 v[16:19], v[116:119], v[44:47], v[16:19]\n"
        "ds_read_b128 v[32:35], v1 offset:25600\n"
        "v_mfma_f32_16x16x32_bf16 v[20:23], v[120:123], v[44:47], v[20:23]\n"
        "s_waitcnt lgkmcnt(1)\n"
        "v_mfma_f32_16x16x32_bf16 v[24:27], v[28:31], v[44:47], v[24:27]\n"
        "s_waitcnt vmcnt(9)\n"
        "v_mfma_f32_16x16x32_bf16 v[12:15], v[124:127], v[48:51], v[12:15]\n"
        "buffer_load_dwordx4 v[44:47], v0, s[40:43], s52 offen offset:3072 sc1\n"
        "v_mfma_f32_16x16x32_bf16 v[16:19], v[128:131], v[48:51], v[16:19]\n"
        "ds_read_b128 v[28:31], v1 offset:26624\n"
        "v_mfma_f32_16x16x32_bf16 v[20:23], v[132:135], v[48:51], v[20:23]\n"
        "s_waitcnt lgkmcnt(1)\n"
        "v_mfma_f32_16x16x32_bf16 v[24:27], v[32:35], v[48:51], v[24:27]\n"
        "s_waitcnt vmcnt(9)\n"
        "v_mfma_f32_16x16x32_bf16 v[12:15], v[136:139], v[52:55], v[12:15]\n"
        "buffer_load_dword v8, v3, s[40:43], 0 offen sc1\n"
        "s_add_u32 s49, s49, %3\n"
        "v_mfma_f32_16x16x32_bf16 v[16:19], v[140:143], v[52:55], v[16:19]\n"
        "ds_read_b128 v[32:35], v1 offset:27648\n"
        "v_mfma_f32_16x16x32_bf16 v[20:23], v[144:147], v[52:55], v[20:23]\n"
        "s_waitcnt lgkmcnt(1)\n"
        "v_mfma_f32_16x16x32_bf16 v[24:27], v[28:31], v[52:55], v[24:27]\n"
        "s_waitcnt vmcnt(9)\n"
        "v_mfma_f32_16x16x32_bf16 v[12:15], v[148:151], v[56:59], v[12:15]\n"
        "v_mfma_f32_16x16x32_bf16 v[16:19], v[152:155], v[56:59], v[16:19]\n"
        "ds_read_b128 v[28:31], v1 offset:28672\n"
        "v_mfma_f32_16x16x32_bf16 v[20:23], v[156:159], v[56:59], v[20:23]\n"
        "s_waitcnt lgkmcnt(1)\n"
        "v_mfma_f32_16x16x32_bf16 v[24:27], v[32:35], v[56:59], v[24:27]\n"
        "s_waitcnt vmcnt(8)\n"
        "v_mfma_f32_16x16x32_bf16 v[12:15], v[160:163], v[60:63], v[12:15]\n"
        "v_mfma_f32_16x16x32_bf16 v[16:19], v[164:167], v[60:63], v[16:19]\n"
        "ds_read_b128 v[32:35], v1 offset:29696\n"
        "v_mfma_f32_16x16x32_bf16 v[20:23], v[168:171], v[60:63], v[20:23]\n"
        "s_waitcnt lgkmcnt(1)\n"
        "v_mfma_f32_16x16x32_bf16 v[24:27], v[28:31], v[60:63], v[24:27]\n"
        "s_waitcnt vmcnt(7)\n"
        "v_mfma_f32_16x16x32_bf16 v[12:15], v[172:175], v[64:67], v[12:15]\n"
        "v_mfma_f32_16x16x32_bf16 v[16:19], v[176:179], v[64:67], v[16:19]\n"
        "ds_read_b128 v[28:31], v1 offset:30720\n"
        "v_mfma_f32_16x16x32_bf16 v[20:23], v[180:183], v[64:67], v[20:23]\n"
        "s_waitcnt lgkmcnt(1)\n"
        "v_mfma_f32_16x16x32_bf16 v[24:27], v[32:35], v[64:67], v[24:27]\n"
        "s_waitcnt vmcnt(6)\n"
        "v_mfma_f32_16x16x32_bf16 v[12:15], v[184:187], v[68:71], v[12:15]\n"
        "v_mfma_f32_16x16x32_bf16 v[16:19], v[188:191], v[68:71], v[16:19]\n"
        "ds_read_b128 v[32:35], v1 offset:31744\n"
        "v_mfma_f32_16x16x32_bf16 v[20:23], v[192:195], v[68:71], v[20:23]\n"
        "s_waitcnt lgkmcnt(1)\n"
        "v_mfma_f32_16x16x32_bf16 v[24:27], v[28:31], v[68:71], v[24:27]\n"
        "s_waitcnt vmcnt(5)\n"
        "v_mfma_f32_16x16x32_bf16 v[12:15], v[196:199], v[72:75], v[12:15]\n"
        "v_mfma_f32_16x16x32_bf16 v[16:19], v[200:203], v[72:75], v[16:19]\n"
        "ds_read_b128 v[28:31], v1 offset:32768\n"
        "v_mfma_f32_16x16x32_bf16 v[20:23], v[204:207], v[72:75], v[20:23]\n"
        "s_waitcnt lgkmcnt(1)\n"
        "v_mfma_f32_16x16x32_bf16 v[24:27], v[32:35], v[72:75], v[24:27]\n"
        "s_waitcnt vmcnt(4)\n"
        "v_mfma_f32_16x16x32_bf16 v[12:15], v[208:211], v[76:79], v[12:15]\n"
        "v_mfma_f32_16x16x32_bf16 v[16:19], v[212:215], v[76:79], v[16:19]\n"
        "ds_read_b128 v[32:35], v1 offset:33792\n"
        "v_mfma_f32_16x16x32_bf16 v[20:23], v[216:219], v[76:79], v[20:23]\n"
        "s_waitcnt lgkmcnt(1)\n"
        "v_mfma_f32_16x16x32_bf16 v[24:27], v[28:31], v[76:79], v[24:27]\n"
        "s_waitcnt vmcnt(3)\n"
        "v_mfma_f32_16x16x32_bf16 v[12:15], v[220:223], v[36:39], v[12:15]\n"
        "v_mfma_f32_16x16x32_bf16 v[16:19], v[224:227], v[36:39], v[16:19]\n"
        "ds_read_b128 v[28:31], v1 offset:34816\n"
        "v_mfma_f32_16x16x32_bf16 v[20:23], v[228:231], v[36:39], v[20:23]\n"
        "s_waitcnt lgkmcnt(1)\n"
        "v_mfma_f32_16x16x32_bf16 v[24:27], v[32:35], v[36:39], v[24:27]\n"
        "s_waitcnt vmcnt(2)\n"
        "v_mfma_f32_16x16x32_bf16 v[12:15], v[232:235], v[40:43], v[12:15]\n"
        "v_mfma_f32_16x16x32_bf16 v[16:19], v[236:239], v[40:43], v[16:19]\n"
        "ds_read_b128 v[32:35], v1 offset:35840\n"
        "v_mfma_f32_16x16x32_bf16 v[20:23], v[240:243], v[40:43], v[20:23]\n"
        "s_waitcnt lgkmcnt(1)\n"
        "v_mfma_f32_16x16x32_bf16 v[24:27], v[28:31], v[40:43], v[24:27]\n"
        "s_waitcnt vmcnt(1)\n"
        "v_mfma_f32_16x16x32_bf16 v[12:15], v[244:247], v[44:47], v[12:15]\n"
        "v_xor_b32 v28, 64, v5\n"
        "v_mfma_f32_16x16x32_bf16 v[16:19], v[248:251], v[44:47], v[16:19]\n"
        "v_xor_b32 v29, 128, v5\n"
        "v_mfma_f32_16x16x32_bf16 v[20:23], v[252:255], v[44:47], v[20:23]\n"
        "s_waitcnt lgkmcnt(0)\n"
        "v_xor_b32 v30, 192, v5\n"
        "v_mfma_f32_16x16x32_bf16 v[24:27], v[32:35], v[44:47], v[24:27]\n"
        "s_waitcnt vmcnt(0)\n"
        "v_cmp_gt_u32 vcc, v4, v8\n"
        "ds_write_b128 v5, v[12:15] offset:36864\n"
        "ds_write_b128 v28, v[16:19] offset:37888\n"
        "ds_write_b128 v29, v[20:23] offset:38912\n"
        "s_nop 2\n"
        "ds_write_b128 v30, v[24:27] offset:39936\n"
        "s_cmp_eq_u32 vcc_lo, 0\n"
        "s_cbranch_scc0 13f\n"
        "s_cmp_eq_u32 vcc_hi, 0\n"
        "s_cselect_b32 exec_hi, -1, 0\n"
        "s_nop 0\n"
        "s_mov_b32 exec_hi, -1\n"
        "s_add_u32 s52, s49, 0\n"
        "buffer_load_dwordx4 v[36:39], v0, s[40:43], s52 offen offset:0 sc1\n"
        "buffer_load_dwordx4 v[40:43], v0, s[40:43], s52 offen offset:1024 sc1\n"
        "buffer_load_dwordx4 v[44:47], v0, s[40:43], s52 offen offset:2048 sc1\n"
        "buffer_load_dwordx4 v[48:51], v0, s[40:43], s52 offen offset:3072 sc1\n"
        "s_add_u32 s52, s49, 4096\n"
        "buffer_load_dwordx4 v[52:55], v0, s[40:43], s52 offen offset:0 sc1\n"
        "buffer_load_dwordx4 v[56:59], v0, s[40:43], s52 offen offset:1024 sc1\n"
        "buffer_load_dwordx4 v[60:63], v0, s[40:43], s52 offen offset:2048 sc1\n"
        "buffer_load_dwordx4 v[64:67], v0, s[40:43], s52 offen offset:3072 sc1\n"
        "s_add_u32 s52, s49, 8192\n"
        "buffer_load_dwordx4 v[68:71], v0, s[40:43], s52 offen offset:0 sc1\n"
        "buffer_load_dwordx4 v[72:75], v0, s[40:43], s52 offen offset:1024 sc1\n"
        "v_add_u32 v4, 2, v4\n"
        "v_add_u32 v3, 2048, v3\n"
        "v_add_u32 v31, 81920, v6\n"
        "s_waitcnt lgkmcnt(0)\n"
        "s_barrier\n"
        "ds_read_b128 v[12:15], v6 offset:0\n"
        "ds_read_b128 v[16:19], v6 offset:40960\n"
        "ds_read_b128 v[20:23], v31 offset:0\n"
        "ds_read_b128 v[24:27], v31 offset:40960\n"
        "s_waitcnt lgkmcnt(2)\n"
        "v_add_f32 v12, v12, v16\n"
        "v_add_f32 v13, v13, v17\n"
        "v_add_f32 v14, v14, v18\n"
        "v_add_f32 v15, v15, v19\n"
        "s_waitcnt lgkmcnt(0)\n"
        "v_add_f32 v20, v20, v24\n"
        "v_add_f32 v21, v21, v25\n"
        "v_add_f32 v22, v22, v26\n"
        "v_add_f32 v23, v23, v27\n"
        "s_barrier\n"
        "v_add_f32 v12, v12, v20\n"
        "v_add_f32 v13, v13, v21\n"
        "v_add_f32 v14, v14, v22\n"
        "v_add_f32 v15, v15, v23\n"
        "v_cvt_pk_bf16_f32 v16, v12, v13\n"
        "v_cvt_pk_bf16_f32 v17, v14, v15\n"
        "v_and_b32 v13, 0xffff0000, v16\n"
        "v_lshlrev_b32 v12, 16, v16\n"
        "v_and_b32 v15, 0xffff0000, v17\n"
        "v_lshlrev_b32 v14, 16, v17\n"
        "v_mul_f32 v20, 0xbfb8aa3b, v12\n"
        "v_mul_f32 v21, 0xbfb8aa3b, v13\n"
        "v_mul_f32 v22, 0xbfb8aa3b, v14\n"
        "v_mul_f32 v23, 0xbfb8aa3b, v15\n"
        "v_exp_f32 v20, v20\n"
        "v_exp_f32 v21, v21\n"
        "v_exp_f32 v22, v22\n"
        "v_exp_f32 v23, v23\n"
        "v_add_f32 v20, 1.0, v20\n"
        "v_add_f32 v21, 1.0, v21\n"
        "v_add_f32 v22, 1.0, v22\n"
        "v_add_f32 v23, 1.0, v23\n"
        "v_rcp_f32 v20, v20\n"
        "v_rcp_f32 v21, v21\n"
        "v_rcp_f32 v22, v22\n"
        "v_rcp_f32 v23, v23\n"
        "v_mul_f32 v12, v12, v20\n"
        "v_mul_f32 v13, v13, v21\n"
        "v_mul_f32 v14, v14, v22\n"
        "v_mul_f32 v15, v15, v23\n"
        "v_cvt_pk_bf16_f32 v16, v12, v13\n"
        "v_cvt_pk_bf16_f32 v17, v14, v15\n"
        "s_max_u32 s53, s48, 1\n"
        "v_subrev_u32 v28, 1, s53\n"
        "s_mov_b64 exec, 1\n"
        "buffer_store_dword v28, off, s[40:43], s57 sc1\n"
        "s_mov_b64 exec, -1\n"
        "buffer_store_dwordx2 v[16:17], v7, s[40:43], s51 offen sc1\n"
        "s_add_u32 s51, s51, 73728\n"
        "s_add_u32 s56, s56, 73728\n"
        "s_add_u32 s48, s48, 2\n"
        "s_branch 12b\n"
        "13:\n"
        "v_add_u32 v31, 81920, v6\n"
        "s_waitcnt lgkmcnt(0)\n"
        "s_barrier\n"
        "ds_read_b128 v[12:15], v6 offset:0\n"
        "ds_read_b128 v[16:19], v6 offset:40960\n"
        "ds_read_b128 v[20:23], v31 offset:0\n"
        "ds_read_b128 v[24:27], v31 offset:40960\n"
        "s_waitcnt lgkmcnt(2)\n"
        "v_add_f32 v12, v12, v16\n"
        "v_add_f32 v13, v13, v17\n"
        "v_add_f32 v14, v14, v18\n"
        "v_add_f32 v15, v15, v19\n"
        "s_waitcnt lgkmcnt(0)\n"
        "v_add_f32 v20, v20, v24\n"
        "v_add_f32 v21, v21, v25\n"
        "v_add_f32 v22, v22, v26\n"
        "v_add_f32 v23, v23, v27\n"
        "s_barrier\n"
        "v_add_f32 v12, v12, v20\n"
        "v_add_f32 v13, v13, v21\n"
        "v_add_f32 v14, v14, v22\n"
        "v_add_f32 v15, v15, v23\n"
        "v_cvt_pk_bf16_f32 v16, v12, v13\n"
        "v_cvt_pk_bf16_f32 v17, v14, v15\n"
        "v_and_b32 v13, 0xffff0000, v16\n"
        "v_lshlrev_b32 v12, 16, v16\n"
        "v_and_b32 v15, 0xffff0000, v17\n"
        "v_lshlrev_b32 v14, 16, v17\n"
        "v_mul_f32 v20, 0xbfb8aa3b, v12\n"
        "v_mul_f32 v21, 0xbfb8aa3b, v13\n"
        "v_mul_f32 v22, 0xbfb8aa3b, v14\n"
        "v_mul_f32 v23, 0xbfb8aa3b, v15\n"
        "v_exp_f32 v20, v20\n"
        "v_exp_f32 v21, v21\n"
        "v_exp_f32 v22, v22\n"
        "v_exp_f32 v23, v23\n"
        "v_add_f32 v20, 1.0, v20\n"
        "v_add_f32 v21, 1.0, v21\n"
        "v_add_f32 v22, 1.0, v22\n"
        "v_add_f32 v23, 1.0, v23\n"
        "v_rcp_f32 v20, v20\n"
        "v_rcp_f32 v21, v21\n"
        "v_rcp_f32 v22, v22\n"
        "v_rcp_f32 v23, v23\n"
        "v_mul_f32 v12, v12, v20\n"
        "v_mul_f32 v13, v13, v21\n"
        "v_mul_f32 v14, v14, v22\n"
        "v_mul_f32 v15, v15, v23\n"
        "v_cvt_pk_bf16_f32 v16, v12, v13\n"
        "v_cvt_pk_bf16_f32 v17, v14, v15\n"
        "s_max_u32 s53, s48, 1\n"
        "v_subrev_u32 v28, 1, s53\n"
        "s_mov_b64 exec, 1\n"
        "buffer_store_dword v28, off, s[40:43], s57 sc1\n"
        "s_mov_b64 exec, -1\n"
        "buffer_store_dwordx2 v[16:17], v7, s[40:43], s51 offen sc1\n"
        "s_add_u32 s51, s51, 73728\n"
        "s_add_u32 s56, s56, 73728\n"
        "s_add_u32 s48, s48, 2\n"
        "s_waitcnt vmcnt(0)\n"
        "s_max_u32 s53, s48, 1\n"
        "v_subrev_u32 v28, 1, s53\n"
        "s_mov_b64 exec, 1\n"
        "buffer_store_dword v28, off, s[40:43], s57 sc1\n"
        "s_mov_b64 exec, -1\n"
        "s_cmp_le_u32 s48, %13\n"
        "s_cbranch_scc1 10b\n"
        "s_waitcnt vmcnt(0)\n"
        "s_branch 40f\n"
        "30:\n"
        "s_add_u32 m0, s55, 20480\n"
        "s_nop 0\n"
        "buffer_load_dwordx4 v2, s[44:47], s52 offen lds\n"
        "s_add_u32 s52, s52, 9216\n"
        "s_add_u32 m0, s55, 21504\n"
        "s_nop 0\n"
        "buffer_load_dwordx4 v2, s[44:47], s52 offen lds\n"
        "s_add_u32 s52, s52, 9216\n"
        "s_add_u32 m0, s55, 22528\n"
        "s_nop 0\n"
        "buffer_load_dwordx4 v2, s[44:47], s52 offen lds\n"
        "s_add_u32 s52, s52, 9216\n"
        "s_add_u32 m0, s55, 23552\n"
        "s_nop 0\n"
        "buffer_load_dwordx4 v2, s[44:47], s52 offen lds\n"
        "s_add_u32 s52, s52, 9216\n"
        "s_add_u32 m0, s55, 24576\n"
        "s_nop 0\n"
        "buffer_load_dwordx4 v2, s[44:47], s52 offen lds\n"
        "s_add_u32 s52, s52, 9216\n"
        "s_add_u32 m0, s55, 25600\n"
        "s_nop 0\n"
        "buffer_load_dwordx4 v2, s[44:47], s52 offen lds\n"
        "s_add_u32 s52, s52, 9216\n"
        "s_add_u32 m0, s55, 26624\n"
        "s_nop 0\n"
        "buffer_load_dwordx4 v2, s[44:47], s52 offen lds\n"
        "s_add_u32 s52, s52, 9216\n"
        "s_add_u32 m0, s55, 27648\n"
        "s_nop 0\n"
        "buffer_load_dwordx4 v2, s[44:47], s52 offen lds\n"
        "s_add_u32 s52, s52, 9216\n"
        "s_add_u32 m0, s55, 28672\n"
        "s_nop 0\n"
        "buffer_load_dwordx4 v2, s[44:47], s52 offen lds\n"
        "s_add_u32 s52, s52, 9216\n"
        "s_add_u32 m0, s55, 29696\n"
        "s_nop 0\n"
        "buffer_load_dwordx4 v2, s[44:47], s52 offen lds\n"
        "s_add_u32 s52, s52, 9216\n"
        "s_add_u32 m0, s55, 30720\n"
        "s_nop 0\n"
        "buffer_load_dwordx4 v2, s[44:47], s52 offen lds\n"
        "s_add_u32 s52, s52, 9216\n"
        "s_add_u32 m0, s55, 31744\n"
        "s_nop 0\n"
        "buffer_load_dwordx4 v2, s[44:47], s52 offen lds\n"
        "s_add_u32 s52, s52, 9216\n"
        "s_add_u32 m0, s55, 32768\n"
        "s_nop 0\n"
        "buffer_load_dwordx4 v2, s[44:47], s52 offen lds\n"
        "s_add_u32 s52, s52, 9216\n"
        "s_add_u32 m0, s55, 33792\n"
        "s_nop 0\n"
        "buffer_load_dwordx4 v2, s[44:47], s52 offen lds\n"
        "s_add_u32 s52, s52, 9216\n"
        "s_add_u32 m0, s55, 34816\n"
        "s_nop 0\n"
        "buffer_load_dwordx4 v2, s[44:47], s52 offen lds\n"
        "s_add_u32 s52, s52, 9216\n"
        "s_add_u32 m0, s55, 35840\n"
        "s_nop 0\n"
        "buffer_load_dwordx4 v2, s[44:47], s52 offen lds\n"
        "s_add_u32 s52, s52, 9216\n"
        "s_add_u32 m0, s55, 36864\n"
        "s_nop 0\n"
        "buffer_load_dwordx4 v2, s[44:47], s52 offen lds\n"
        "s_add_u32 s52, s52, 9216\n"
        "s_add_u32 m0, s55, 37888\n"
        "s_nop 0\n"
        "buffer_load_dwordx4 v2, s[44:47], s52 offen lds\n"
        "s_add_u32 s52, s52, 9216\n"
        "s_add_u32 m0, s55, 38912\n"
        "s_nop 0\n"
        "buffer_load_dwordx4 v2, s[44:47], s52 offen lds\n"
        "s_add_u32 s52, s52, 9216\n"
        "s_add_u32 m0, s55, 39936\n"
        "s_nop 0\n"
        "buffer_load_dwordx4 v2, s[44:47], s52 offen lds\n"
        "s_add_u32 s52, s52, 9216\n"
        "s_waitcnt vmcnt(16)\n"
        "ds_read_b64_tr_b16 a[0:1], v3 offset:20480\n"
        "ds_read_b64_tr_b16 a[2:3], v3 offset:21504\n"
        "ds_read_b64_tr_b16 a[4:5], v4 offset:20480\n"
        "ds_read_b64_tr_b16 a[6:7], v4 offset:21504\n"
        "ds_read_b64_tr_b16 a[8:9], v5 offset:20480\n"
        "ds_read_b64_tr_b16 a[10:11], v5 offset:21504\n"
        "ds_read_b64_tr_b16 v[12:13], v6 offset:20480\n"
        "ds_read_b64_tr_b16 v[14:15], v6 offset:21504\n"
        "s_waitcnt lgkmcnt(0)\n"
        "ds_write_b128 v1, v[12:15] offset:0\n"
        "s_memrealtime vcc\n"
        "s_waitcnt lgkmcnt(0)\n"
        "v_mov_b32 v10, vcc_lo\n"
        "buffer_store_dword v10, off, s[40:43], s58 sc1\n"
        "s_add_u32 m0, s55, 20480\n"
        "s_nop 0\n"
        "buffer_load_dwordx4 v2, s[44:47], s52 offen lds\n"
        "s_add_u32 s52, s52, 9216\n"
        "s_add_u32 m0, s55, 21504\n"
        "s_nop 0\n"
        "buffer_load_dwordx4 v2, s[44:47], s52 offen lds\n"
        "s_add_u32 s52, s52, 9216\n"
        "s_add_u32 m0, s55, 22528\n"
        "s_nop 0\n"
        "buffer_load_dwordx4 v2, s[44:47], s52 offen lds\n"
        "s_add_u32 s52, s52, 9216\n"
        "s_add_u32 m0, s55, 23552\n"
        "s_nop 0\n"
        "buffer_load_dwordx4 v2, s[44:47], s52 offen lds\n"
        "s_add_u32 s52, s52, 9216\n"
        "s_waitcnt vmcnt(16)\n"
        "ds_read_b64_tr_b16 a[12:13], v3 offset:24576\n"
        "ds_read_b64_tr_b16 a[14:15], v3 offset:25600\n"
        "ds_read_b64_tr_b16 a[16:17], v4 offset:24576\n"
        "ds_read_b64_tr_b16 a[18:19], v4 offset:25600\n"
        "ds_read_b64_tr_b16 a[20:21], v5 offset:24576\n"
        "ds_read_b64_tr_b16 a[22:23], v5 offset:25600\n"
        "ds_read_b64_tr_b16 v[12:13], v6 offset:24576\n"
        "ds_read_b64_tr_b16 v[14:15], v6 offset:25600\n"
        "s_waitcnt lgkmcnt(0)\n"
        "ds_write_b128 v1, v[12:15] offset:1024\n"
        "s_add_u32 m0, s55, 24576\n"
        "s_nop 0\n"
        "buffer_load_dwordx4 v2, s[44:47], s52 offen lds\n"
        "s_add_u32 s52, s52, 9216\n"
        "s_add_u32 m0, s55, 25600\n"
        "s_nop 0\n"
        "buffer_load_dwordx4 v2, s[44:47], s52 offen lds\n"
        "s_add_u32 s52, s52, 9216\n"
        "s_add_u32 m0, s55, 26624\n"
        "s_nop 0\n"
        "buffer_load_dwordx4 v2, s[44:47], s52 offen lds\n"
        "s_add_u32 s52, s52, 9216\n"
        "s_add_u32 m0, s55, 27648\n"
        "s_nop 0\n"
        "buffer_load_dwordx4 v2, s[44:47], s52 offen lds\n"
        "s_add_u32 s52, s52, 9216\n"
        "s_waitcnt vmcnt(16)\n"
        "ds_read_b64_tr_b16 a[24:25], v3 offset:28672\n"
        "ds_read_b64_tr_b16 a[26:27], v3 offset:29696\n"
        "ds_read_b64_tr_b16 a[28:29], v4 offset:28672\n"
        "ds_read_b64_tr_b16 a[30:31], v4 offset:29696\n"
        "ds_read_b64_tr_b16 a[32:33], v5 offset:28672\n"
        "ds_read_b64_tr_b16 a[34:35], v5 offset:29696\n"
        "ds_read_b64_tr_b16 v[12:13], v6 offset:28672\n"
        "ds_read_b64_tr_b16 v[14:15], v6 offset:29696\n"
        "s_waitcnt lgkmcnt(0)\n"
        "ds_write_b128 v1, v[12:15] offset:2048\n"
        "s_add_u32 m0, s55, 28672\n"
        "s_nop 0\n"
        "buffer_load_dwordx4 v2, s[44:47], s52 offen lds\n"
        "s_add_u32 s52, s52, 9216\n"
        "s_add_u32 m0, s55, 29696\n"
        "s_nop 0\n"
        "buffer_load_dwordx4 v2, s[44:47], s52 offen lds\n"
        "s_add_u32 s52, s52, 9216\n"
        "s_add_u32 m0, s55, 30720\n"
        "s_nop 0\n"
        "buffer_load_dwordx4 v2, s[44:47], s52 offen lds\n"
        "s_add_u32 s52, s52, 9216\n"
        "s_add_u32 m0, s55, 31744\n"
        "s_nop 0\n"
        "buffer_load_dwordx4 v2, s[44:47], s52 offen lds\n"
        "s_add_u32 s52, s52, 9216\n"
        "s_waitcnt vmcnt(16)\n"
        "ds_read_b64_tr_b16 a[36:37], v3 offset:32768\n"
        "ds_read_b64_tr_b16 a[38:39], v3 offset:33792\n"
        "ds_read_b64_tr_b16 a[40:41], v4 offset:32768\n"
        "ds_read_b64_tr_b16 a[42:43], v4 offset:33792\n"
        "ds_read_b64_tr_b16 a[44:45], v5 offset:32768\n"
        "ds_read_b64_tr_b16 a[46:47], v5 offset:33792\n"
        "ds_read_b64_tr_b16 v[12:13], v6 offset:32768\n"
        "ds_read_b64_tr_b16 v[14:15], v6 offset:33792\n"
        "s_waitcnt lgkmcnt(0)\n"
        "ds_write_b128 v1, v[12:15] offset:3072\n"
        "s_add_u32 m0, s55, 32768\n"
        "s_nop 0\n"
        "buffer_load_dwordx4 v2, s[44:47], s52 offen lds\n"
        "s_add_u32 s52, s52, 9216\n"
        "s_add_u32 m0, s55, 33792\n"
        "s_nop 0\n"
        "buffer_load_dwordx4 v2, s[44:47], s52 offen lds\n"
        "s_add_u32 s52, s52, 9216\n"
        "s_add_u32 m0, s55, 34816\n"
        "s_nop 0\n"
        "buffer_load_dwordx4 v2, s[44:47], s52 offen lds\n"
        "s_add_u32 s52, s52, 9216\n"
        "s_add_u32 m0, s55, 35840\n"
        "s_nop 0\n"
        "buffer_load_dwordx4 v2, s[44:47], s52 offen lds\n"
        "s_add_u32 s52, s52, 9216\n"
        "s_waitcnt vmcnt(16)\n"
        "ds_read_b64_tr_b16 a[48:49], v3 offset:36864\n"
        "ds_read_b64_tr_b16 a[50:51], v3 offset:37888\n"
        "ds_read_b64_tr_b16 a[52:53], v4 offset:36864\n"
        "ds_read_b64_tr_b16 a[54:55], v4 offset:37888\n"
        "ds_read_b64_tr_b16 a[56:57], v5 offset:36864\n"
        "ds_read_b64_tr_b16 a[58:59], v5 offset:37888\n"
        "ds_read_b64_tr_b16 v[12:13], v6 offset:36864\n"
        "ds_read_b64_tr_b16 v[14:15], v6 offset:37888\n"
        "s_waitcnt lgkmcnt(0)\n"
        "ds_write_b128 v1, v[12:15] offset:4096\n"
        "s_memrealtime vcc\n"
        "s_waitcnt lgkmcnt(0)\n"
        "v_mov_b32 v10, vcc_lo\n"
        "buffer_store_dword v10, off, s[40:43], s58 sc1\n"
        "s_add_u32 m0, s55, 36864\n"
        "s_nop 0\n"
        "buffer_load_dwordx4 v2, s[44:47], s52 offen lds\n"
        "s_add_u32 s52, s52, 9216\n"
        "s_add_u32 m0, s55, 37888\n"
        "s_nop 0\n"
        "buffer_load_dwordx4 v2, s[44:47], s52 offen lds\n"
        "s_add_u32 s52, s52, 9216\n"
        "s_add_u32 m0, s55, 38912\n"
        "s_nop 0\n"
        "buffer_load_dwordx4 v2, s[44:47], s52 offen lds\n"
        "s_add_u32 s52, s52, 9216\n"
        "s_add_u32 m0, s55, 39936\n"
        "s_nop 0\n"
        "buffer_load_dwordx4 v2, s[44:47], s52 offen lds\n"
        "s_add_u32 s52, s52, 9216\n"
        "s_waitcnt vmcnt(16)\n"
        "ds_read_b64_tr_b16 a[60:61], v3 offset:20480\n"
        "ds_read_b64_tr_b16 a[62:63], v3 offset:21504\n"
        "ds_read_b64_tr_b16 a[64:65], v4 offset:20480\n"
        "ds_read_b64_tr_b16 a[66:67], v4 offset:21504\n"
        "ds_read_b64_tr_b16 a[68:69], v5 offset:20480\n"
        "ds_read_b64_tr_b16 a[70:71], v5 offset:21504\n"
        "ds_read_b64_tr_b16 v[12:13], v6 offset:20480\n"
        "ds_read_b64_tr_b16 v[14:15], v6 offset:21504\n"
        "s_waitcnt lgkmcnt(0)\n"
        "ds_write_b128 v1, v[12:15] offset:5120\n"
        "s_add_u32 m0, s55, 20480\n"
        "s_nop 0\n"
        "buffer_load_dwordx4 v2, s[44:47], s52 offen lds\n"
        "s_add_u32 s52, s52, 9216\n"
        "s_add_u32 m0, s55, 21504\n"
        "s_nop 0\n"
        "buffer_load_dwordx4 v2, s[44:47], s52 offen lds\n"
        "s_add_u32 s52, s52, 9216\n"
        "s_add_u32 m0, s55, 22528\n"
        "s_nop 0\n"
        "buffer_load_dwordx4 v2, s[44:47], s52 offen lds\n"
        "s_add_u32 s52, s52, 9216\n"
        "s_add_u32 m0, s55, 23552\n"
        "s_nop 0\n"
        "buffer_load_dwordx4 v2, s[44:47], s52 offen lds\n"
        "s_add_u32 s52, s52, 9216\n"
        "s_waitcnt vmcnt(16)\n"
        "ds_read_b64_tr_b16 a[72:73], v3 offset:24576\n"
        "ds_read_b64_tr_b16 a[74:75], v3 offset:25600\n"
        "ds_read_b64_tr_b16 a[76:77], v4 offset:24576\n"
        "ds_read_b64_tr_b16 a[78:79], v4 offset:25600\n"
        "ds_read_b64_tr_b16 a[80:81], v5 offset:24576\n"
        "ds_read_b64_tr_b16 a[82:83], v5 offset:25600\n"
        "ds_read_b64_tr_b16 v[12:13], v6 offset:24576\n"
        "ds_read_b64_tr_b16 v[14:15], v6 offset:25600\n"
        "s_waitcnt lgkmcnt(0)\n"
        "ds_write_b128 v1, v[12:15] offset:6144\n"
        "s_add_u32 m0, s55, 24576\n"
        "s_nop 0\n"
        "buffer_load_dwordx4 v2, s[44:47], s52 offen lds\n"
        "s_add_u32 s52, s52, 9216\n"
        "s_add_u32 m0, s55, 25600\n"
        "s_nop 0\n"
        "buffer_load_dwordx4 v2, s[44:47], s52 offen lds\n"
        "s_add_u32 s52, s52, 9216\n"
        "s_add_u32 m0, s55, 26624\n"
        "s_nop 0\n"
        "buffer_load_dwordx4 v2, s[44:47], s52 offen lds\n"
        "s_add_u32 s52, s52, 9216\n"
        "s_add_u32 m0, s55, 27648\n"
        "s_nop 0\n"
        "buffer_load_dwordx4 v2, s[44:47], s52 offen lds\n"
        "s_add_u32 s52, s52, 9216\n"
        "s_waitcnt vmcnt(16)\n"
        "ds_read_b64_tr_b16 a[84:85], v3 offset:28672\n"
        "ds_read_b64_tr_b16 a[86:87], v3 offset:29696\n"
        "ds_read_b64_tr_b16 a[88:89], v4 offset:28672\n"
        "ds_read_b64_tr_b16 a[90:91], v4 offset:29696\n"
        "ds_read_b64_tr_b16 a[92:93], v5 offset:28672\n"
        "ds_read_b64_tr_b16 a[94:95], v5 offset:29696\n"
        "ds_read_b64_tr_b16 v[12:13], v6 offset:28672\n"
        "ds_read_b64_tr_b16 v[14:15], v6 offset:29696\n"
        "s_waitcnt lgkmcnt(0)\n"
        "ds_write_b128 v1, v[12:15] offset:7168\n"
        "s_add_u32 m0, s55, 28672\n"
        "s_nop 0\n"
        "buffer_load_dwordx4 v2, s[44:47], s52 offen lds\n"
        "s_add_u32 s52, s52, 9216\n"
        "s_add_u32 m0, s55, 29696\n"
        "s_nop 0\n"
        "buffer_load_dwordx4 v2, s[44:47], s52 offen lds\n"
        "s_add_u32 s52, s52, 9216\n"
        "s_add_u32 m0, s55, 30720\n"
        "s_nop 0\n"
        "buffer_load_dwordx4 v2, s[44:47], s52 offen lds\n"
        "s_add_u32 s52, s52, 9216\n"
        "s_add_u32 m0, s55, 31744\n"
        "s_nop 0\n"
        "buffer_load_dwordx4 v2, s[44:47], s52 offen lds\n"
        "s_add_u32 s52, s52, 9216\n"
        "s_waitcnt vmcnt(16)\n"
        "ds_read_b64_tr_b16 a[96:97], v3 offset:32768\n"
        "ds_read_b64_tr_b16 a[98:99], v3 offset:33792\n"
        "ds_read_b64_tr_b16 a[100:101], v4 offset:32768\n"
        "ds_read_b64_tr_b16 a[102:103], v4 offset:33792\n"
        "ds_read_b64_tr_b16 a[104:105], v5 offset:32768\n"
        "ds_read_b64_tr_b16 a[106:107], v5 offset:33792\n"
        "ds_read_b64_tr_b16 v[12:13], v6 offset:32768\n"
        "ds_read_b64_tr_b16 v[14:15], v6 offset:33792\n"
        "s_waitcnt lgkmcnt(0)\n"
        "ds_write_b128 v1, v[12:15] offset:8192\n"
        "s_add_u32 m0, s55, 32768\n"
        "s_nop 0\n"
        "buffer_load_dwordx4 v2, s[44:47], s52 offen lds\n"
        "s_add_u32 s52, s52, 9216\n"
        "s_add_u32 m0, s55, 33792\n"
        "s_nop 0\n"
        "buffer_load_dwordx4 v2, s[44:47], s52 offen lds\n"
        "s_add_u32 s52, s52, 9216\n"
        "s_add_u32 m0, s55, 34816\n"
        "s_nop 0\n"
        "buffer_load_dwordx4 v2, s[44:47], s52 offen lds\n"
        "s_add_u32 s52, s52, 9216\n"
        "s_add_u32 m0, s55, 35840\n"
        "s_nop 0\n"
        "buffer_load_dwordx4 v2, s[44:47], s52 offen lds\n"
        "s_add_u32 s52, s52, 9216\n"
        "s_waitcnt vmcnt(16)\n"
        "ds_read_b64_tr_b16 a[108:109], v3 offset:36864\n"
        "ds_read_b64_tr_b16 a[110:111], v3 offset:37888\n"
        "ds_read_b64_tr_b16 a[112:113], v4 offset:36864\n"
        "ds_read_b64_tr_b16 a[114:115], v4 offset:37888\n"
        "ds_read_b64_tr_b16 a[116:117], v5 offset:36864\n"
        "ds_read_b64_tr_b16 a[118:119], v5 offset:37888\n"
        "ds_read_b64_tr_b16 v[12:13], v6 offset:36864\n"
        "ds_read_b64_tr_b16 v[14:15], v6 offset:37888\n"
        "s_waitcnt lgkmcnt(0)\n"
        "ds_write_b128 v1, v[12:15] offset:9216\n"
        "s_add_u32 m0, s55, 36864\n"
        "s_nop 0\n"
        "buffer_load_dwordx4 v2, s[44:47], s52 offen lds\n"
        "s_add_u32 s52, s52, 9216\n"
        "s_add_u32 m0, s55, 37888\n"
        "s_nop 0\n"
        "buffer_load_dwordx4 v2, s[44:47], s52 offen lds\n"
        "s_add_u32 s52, s52, 9216\n"
        "s_add_u32 m0, s55, 38912\n"
        "s_nop 0\n"
        "buffer_load_dwordx4 v2, s[44:47], s52 offen lds\n"
        "s_add_u32 s52, s52, 9216\n"
        "s_add_u32 m0, s55, 39936\n"
        "s_nop 0\n"
        "buffer_load_dwordx4 v2, s[44:47], s52 offen lds\n"
        "s_add_u32 s52, s52, 9216\n"
        "s_waitcnt vmcnt(16)\n"
        "ds_read_b64_tr_b16 a[120:121], v3 offset:20480\n"
        "ds_read_b64_tr_b16 a[122:123], v3 offset:21504\n"
        "ds_read_b64_tr_b16 a[124:125], v4 offset:20480\n"
        "ds_read_b64_tr_b16 a[126:127], v4 offset:21504\n"
        "ds_read_b64_tr_b16 a[128:129], v5 offset:20480\n"
        "ds_read_b64_tr_b16 a[130:131], v5 offset:21504\n"
        "ds_read_b64_tr_b16 v[12:13], v6 offset:20480\n"
        "ds_read_b64_tr_b16 v[14:15], v6 offset:21504\n"
        "s_waitcnt lgkmcnt(0)\n"
        "ds_write_b128 v1, v[12:15] offset:10240\n"
        "s_add_u32 m0, s55, 20480\n"
        "s_nop 0\n"
        "buffer_load_dwordx4 v2, s[44:47], s52 offen lds\n"
        "s_add_u32 s52, s52, 9216\n"
        "s_add_u32 m0, s55, 21504\n"
        "s_nop 0\n"
        "buffer_load_dwordx4 v2, s[44:47], s52 offen lds\n"
        "s_add_u32 s52, s52, 9216\n"
        "s_add_u32 m0, s55, 22528\n"
        "s_nop 0\n"
        "buffer_load_dwordx4 v2, s[44:47], s52 offen lds\n"
        "s_add_u32 s52, s52, 9216\n"
        "s_add_u32 m0, s55, 23552\n"
        "s_nop 0\n"
        "buffer_load_dwordx4 v2, s[44:47], s52 offen lds\n"
        "s_add_u32 s52, s52, 9216\n"
        "s_waitcnt vmcnt(16)\n"
        "ds_read_b64_tr_b16 a[132:133], v3 offset:24576\n"
        "ds_read_b64_tr_b16 a[134:135], v3 offset:25600\n"
        "ds_read_b64_tr_b16 a[136:137], v4 offset:24576\n"
        "ds_read_b64_tr_b16 a[138:139], v4 offset:25600\n"
        "ds_read_b64_tr_b16 a[140:141], v5 offset:24576\n"
        "ds_read_b64_tr_b16 a[142:143], v5 offset:25600\n"
        "ds_read_b64_tr_b16 v[12:13], v6 offset:24576\n"
        "ds_read_b64_tr_b16 v[14:15], v6 offset:25600\n"
        "s_waitcnt lgkmcnt(0)\n"
        "ds_write_b128 v1, v[12:15] offset:11264\n"
        "s_add_u32 m0, s55, 24576\n"
        "s_nop 0\n"
        "buffer_load_dwordx4 v2, s[44:47], s52 offen lds\n"
        "s_add_u32 s52, s52, 9216\n"
        "s_add_u32 m0, s55, 25600\n"
        "s_nop 0\n"
        "buffer_load_dwordx4 v2, s[44:47], s52 offen lds\n"
        "s_add_u32 s52, s52, 9216\n"
        "s_add_u32 m0, s55, 26624\n"
        "s_nop 0\n"
        "buffer_load_dwordx4 v2, s[44:47], s52 offen lds\n"
        "s_add_u32 s52, s52, 9216\n"
        "s_add_u32 m0, s55, 27648\n"
        "s_nop 0\n"
        "buffer_load_dwordx4 v2, s[44:47], s52 offen lds\n"
        "s_add_u32 s52, s52, 9216\n"
        "s_waitcnt vmcnt(16)\n"
        "ds_read_b64_tr_b16 a[144:145], v3 offset:28672\n"
        "ds_read_b64_tr_b16 a[146:147], v3 offset:29696\n"
        "ds_read_b64_tr_b16 a[148:149], v4 offset:28672\n"
        "ds_read_b64_tr_b16 a[150:151], v4 offset:29696\n"
        "ds_read_b64_tr_b16 a[152:153], v5 offset:28672\n"
        "ds_read_b64_tr_b16 a[154:155], v5 offset:29696\n"
        "ds_read_b64_tr_b16 v[12:13], v6 offset:28672\n"
        "ds_read_b64_tr_b16 v[14:15], v6 offset:29696\n"
        "s_waitcnt lgkmcnt(0)\n"
        "ds_write_b128 v1, v[12:15] offset:12288\n"
        "s_memrealtime vcc\n"
        "s_waitcnt lgkmcnt(0)\n"
        "v_mov_b32 v10, vcc_lo\n"
        "buffer_store_dword v10, off, s[40:43], s58 sc1\n"
        "s_add_u32 m0, s55, 28672\n"
        "s_nop 0\n"
        "buffer_load_dwordx4 v2, s[44:47], s52 offen lds\n"
        "s_add_u32 s52, s52, 9216\n"
        "s_add_u32 m0, s55, 29696\n"
        "s_nop 0\n"
        "buffer_load_dwordx4 v2, s[44:47], s52 offen lds\n"
        "s_add_u32 s52, s52, 9216\n"
        "s_add_u32 m0, s55, 30720\n"
        "s_nop 0\n"
        "buffer_load_dwordx4 v2, s[44:47], s52 offen lds\n"
        "s_add_u32 s52, s52, 9216\n"
        "s_add_u32 m0, s55, 31744\n"
        "s_nop 0\n"
        "buffer_load_dwordx4 v2, s[44:47], s52 offen lds\n"
        "s_add_u32 s52, s52, 9216\n"
        "s_waitcnt vmcnt(16)\n"
        "ds_read_b64_tr_b16 a[156:157], v3 offset:32768\n"
        "ds_read_b64_tr_b16 a[158:159], v3 offset:33792\n"
        "ds_read_b64_tr_b16 a[160:161], v4 offset:32768\n"
        "ds_read_b64_tr_b16 a[162:163], v4 offset:33792\n"
        "ds_read_b64_tr_b16 a[164:165], v5 offset:32768\n"
        "ds_read_b64_tr_b16 a[166:167], v5 offset:33792\n"
        "ds_read_b64_tr_b16 v[12:13], v6 offset:32768\n"
        "ds_read_b64_tr_b16 v[14:15], v6 offset:33792\n"
        "s_waitcnt lgkmcnt(0)\n"
        "ds_write_b128 v1, v[12:15] offset:13312\n"
        "s_add_u32 m0, s55, 32768\n"
        "s_nop 0\n"
        "buffer_load_dwordx4 v2, s[44:47], s52 offen lds\n"
        "s_add_u32 s52, s52, 9216\n"
        "s_add_u32 m0, s55, 33792\n"
        "s_nop 0\n"
        "buffer_load_dwordx4 v2, s[44:47], s52 offen lds\n"
        "s_add_u32 s52, s52, 9216\n"
        "s_add_u32 m0, s55, 34816\n"
        "s_nop 0\n"
        "buffer_load_dwordx4 v2, s[44:47], s52 offen lds\n"
        "s_add_u32 s52, s52, 9216\n"
        "s_add_u32 m0, s55, 35840\n"
        "s_nop 0\n"
        "buffer_load_dwordx4 v2, s[44:47], s52 offen lds\n"
        "s_add_u32 s52, s52, 9216\n"
        "s_waitcnt vmcnt(16)\n"
        "ds_read_b64_tr_b16 a[168:169], v3 offset:36864\n"
        "ds_read_b64_tr_b16 a[170:171], v3 offset:37888\n"
        "ds_read_b64_tr_b16 a[172:173], v4 offset:36864\n"
        "ds_read_b64_tr_b16 a[174:175], v4 offset:37888\n"
        "ds_read_b64_tr_b16 a[176:177], v5 offset:36864\n"
        "ds_read_b64_tr_b16 a[178:179], v5 offset:37888\n"
        "ds_read_b64_tr_b16 v[12:13], v6 offset:36864\n"
        "ds_read_b64_tr_b16 v[14:15], v6 offset:37888\n"
        "s_waitcnt lgkmcnt(0)\n"
        "ds_write_b128 v1, v[12:15] offset:14336\n"
        "s_add_u32 m0, s55, 36864\n"
        "s_nop 0\n"
        "buffer_load_dwordx4 v2, s[44:47], s52 offen lds\n"
        "s_add_u32 s52, s52, 9216\n"
        "s_add_u32 m0, s55, 37888\n"
        "s_nop 0\n"
        "buffer_load_dwordx4 v2, s[44:47], s52 offen lds\n"
        "s_add_u32 s52, s52, 9216\n"
        "s_add_u32 m0, s55, 38912\n"
        "s_nop 0\n"
        "buffer_load_dwordx4 v2, s[44:47], s52 offen lds\n"
        "s_add_u32 s52, s52, 9216\n"
        "s_add_u32 m0, s55, 39936\n"
        "s_nop 0\n"
        "buffer_load_dwordx4 v2, s[44:47], s52 offen lds\n"
        "s_add_u32 s52, s52, 9216\n"
        "s_waitcnt vmcnt(16)\n"
        "ds_read_b64_tr_b16 a[180:181], v3 offset:20480\n"
        "ds_read_b64_tr_b16 a[182:183], v3 offset:21504\n"
        "ds_read_b64_tr_b16 a[184:185], v4 offset:20480\n"
        "ds_read_b64_tr_b16 a[186:187], v4 offset:21504\n"
        "ds_read_b64_tr_b16 a[188:189], v5 offset:20480\n"
        "ds_read_b64_tr_b16 a[190:191], v5 offset:21504\n"
        "ds_read_b64_tr_b16 v[12:13], v6 offset:20480\n"
        "ds_read_b64_tr_b16 v[14:15], v6 offset:21504\n"
        "s_waitcnt lgkmcnt(0)\n"
        "ds_write_b128 v1, v[12:15] offset:15360\n"
        "s_add_u32 m0, s55, 20480\n"
        "s_nop 0\n"
        "buffer_load_dwordx4 v2, s[44:47], s52 offen lds\n"
        "s_add_u32 s52, s52, 9216\n"
        "s_add_u32 m0, s55, 21504\n"
        "s_nop 0\n"
        "buffer_load_dwordx4 v2, s[44:47], s52 offen lds\n"
        "s_add_u32 s52, s52, 9216\n"
        "s_add_u32 m0, s55, 22528\n"
        "s_nop 0\n"
        "buffer_load_dwordx4 v2, s[44:47], s52 offen lds\n"
        "s_add_u32 s52, s52, 9216\n"
        "s_add_u32 m0, s55, 23552\n"
        "s_nop 0\n"
        "buffer_load_dwordx4 v2, s[44:47], s52 offen lds\n"
        "s_add_u32 s52, s52, 9216\n"
        "s_waitcnt vmcnt(16)\n"
        "ds_read_b64_tr_b16 a[192:193], v3 offset:24576\n"
        "ds_read_b64_tr_b16 a[194:195], v3 offset:25600\n"
        "ds_read_b64_tr_b16 a[196:197], v4 offset:24576\n"
        "ds_read_b64_tr_b16 a[198:199], v4 offset:25600\n"
        "ds_read_b64_tr_b16 a[200:201], v5 offset:24576\n"
        "ds_read_b64_tr_b16 a[202:203], v5 offset:25600\n"
        "ds_read_b64_tr_b16 v[12:13], v6 offset:24576\n"
        "ds_read_b64_tr_b16 v[14:15], v6 offset:25600\n"
        "s_waitcnt lgkmcnt(0)\n"
        "ds_write_b128 v1, v[12:15] offset:16384\n"
        "s_add_u32 m0, s55, 24576\n"
        "s_nop 0\n"
        "buffer_load_dwordx4 v2, s[44:47], s52 offen lds\n"
        "s_add_u32 s52, s52, 9216\n"
        "s_add_u32 m0, s55, 25600\n"
        "s_nop 0\n"
        "buffer_load_dwordx4 v2, s[44:47], s52 offen lds\n"
        "s_add_u32 s52, s52, 9216\n"
        "s_add_u32 m0, s55, 26624\n"
        "s_nop 0\n"
        "buffer_load_dwordx4 v2, s[44:47], s52 offen lds\n"
        "s_add_u32 s52, s52, 9216\n"
        "s_add_u32 m0, s55, 27648\n"
        "s_nop 0\n"
        "buffer_load_dwordx4 v2, s[44:47], s52 offen lds\n"
        "s_add_u32 s52, s52, 9216\n"
        "s_waitcnt vmcnt(16)\n"
        "ds_read_b64_tr_b16 a[204:205], v3 offset:28672\n"
        "ds_read_b64_tr_b16 a[206:207], v3 offset:29696\n"
        "ds_read_b64_tr_b16 a[208:209], v4 offset:28672\n"
        "ds_read_b64_tr_b16 a[210:211], v4 offset:29696\n"
        "ds_read_b64_tr_b16 a[212:213], v5 offset:28672\n"
        "ds_read_b64_tr_b16 a[214:215], v5 offset:29696\n"
        "ds_read_b64_tr_b16 v[12:13], v6 offset:28672\n"
        "ds_read_b64_tr_b16 v[14:15], v6 offset:29696\n"
        "s_waitcnt lgkmcnt(0)\n"
        "ds_write_b128 v1, v[12:15] offset:17408\n"
        "s_add_u32 m0, s55, 28672\n"
        "s_nop 0\n"
        "buffer_load_dwordx4 v2, s[44:47], s52 offen lds\n"
        "s_add_u32 s52, s52, 9216\n"
        "s_add_u32 m0, s55, 29696\n"
        "s_nop 0\n"
        "buffer_load_dwordx4 v2, s[44:47], s52 offen lds\n"
        "s_add_u32 s52, s52, 9216\n"
        "s_add_u32 m0, s55, 30720\n"
        "s_nop 0\n"
        "buffer_load_dwordx4 v2, s[44:47], s52 offen lds\n"
        "s_add_u32 s52, s52, 9216\n"
        "s_add_u32 m0, s55, 31744\n"
        "s_nop 0\n"
        "buffer_load_dwordx4 v2, s[44:47], s52 offen lds\n"
        "s_add_u32 s52, s52, 9216\n"
        "s_waitcnt vmcnt(16)\n"
        "ds_read_b64_tr_b16 a[216:217], v3 offset:32768\n"
        "ds_read_b64_tr_b16 a[218:219], v3 offset:33792\n"
        "ds_read_b64_tr_b16 a[220:221], v4 offset:32768\n"
        "ds_read_b64_tr_b16 a[222:223], v4 offset:33792\n"
        "ds_read_b64_tr_b16 a[224:225], v5 offset:32768\n"
        "ds_read_b64_tr_b16 a[226:227], v5 offset:33792\n"
        "ds_read_b64_tr_b16 v[12:13], v6 offset:32768\n"
        "ds_read_b64_tr_b16 v[14:15], v6 offset:33792\n"
        "s_waitcnt lgkmcnt(0)\n"
        "ds_write_b128 v1, v[12:15] offset:18432\n"
        "s_add_u32 m0, s55, 32768\n"
        "s_nop 0\n"
        "buffer_load_dwordx4 v2, s[44:47], s52 offen lds\n"
        "s_add_u32 s52, s52, 9216\n"
        "s_add_u32 m0, s55, 33792\n"
        "s_nop 0\n"
        "buffer_load_dwordx4 v2, s[44:47], s52 offen lds\n"
        "s_add_u32 s52, s52, 9216\n"
        "s_add_u32 m0, s55, 34816\n"
        "s_nop 0\n"
        "buffer_load_dwordx4 v2, s[44:47], s52 offen lds\n"
        "s_add_u32 s52, s52, 9216\n"
        "s_add_u32 m0, s55, 35840\n"
        "s_nop 0\n"
        "buffer_load_dwordx4 v2, s[44:47], s52 offen lds\n"
        "s_add_u32 s52, s52, 9216\n"
        "s_waitcnt vmcnt(16)\n"
        "ds_read_b64_tr_b16 a[228:229], v3 offset:36864\n"
        "ds_read_b64_tr_b16 a[230:231], v3 offset:37888\n"
        "ds_read_b64_tr_b16 a[232:233], v4 offset:36864\n"
        "ds_read_b64_tr_b16 a[234:235], v4 offset:37888\n"
        "ds_read_b64_tr_b16 a[236:237], v5 offset:36864\n"
        "ds_read_b64_tr_b16 a[238:239], v5 offset:37888\n"
        "ds_read_b64_tr_b16 v[12:13], v6 offset:36864\n"
        "ds_read_b64_tr_b16 v[14:15], v6 offset:37888\n"
        "s_waitcnt lgkmcnt(0)\n"
        "ds_write_b128 v1, v[12:15] offset:19456\n"
        "s_add_u32 m0, s55, 36864\n"
        "s_nop 0\n"
        "buffer_load_dwordx4 v2, s[44:47], s52 offen lds\n"
        "s_add_u32 s52, s52, 9216\n"
        "s_add_u32 m0, s55, 37888\n"
        "s_nop 0\n"
        "buffer_load_dwordx4 v2, s[44:47], s52 offen lds\n"
        "s_add_u32 s52, s52, 9216\n"
        "s_add_u32 m0, s55, 38912\n"
        "s_nop 0\n"
        "buffer_load_dwordx4 v2, s[44:47], s52 offen lds\n"
        "s_add_u32 s52, s52, 9216\n"
        "s_add_u32 m0, s55, 39936\n"
        "s_nop 0\n"
        "buffer_load_dwordx4 v2, s[44:47], s52 offen lds\n"
        "s_add_u32 s52, s52, 9216\n"
        "s_waitcnt vmcnt(16)\n"
        "ds_read_b64_tr_b16 a[240:241], v3 offset:20480\n"
        "ds_read_b64_tr_b16 a[242:243], v3 offset:21504\n"
        "ds_read_b64_tr_b16 a[244:245], v4 offset:20480\n"
        "ds_read_b64_tr_b16 a[246:247], v4 offset:21504\n"
        "ds_read_b64_tr_b16 a[248:249], v5 offset:20480\n"
        "ds_read_b64_tr_b16 a[250:251], v5 offset:21504\n"
        "ds_read_b64_tr_b16 v[16:17], v6 offset:20480\n"
        "ds_read_b64_tr_b16 v[18:19], v6 offset:21504\n"
        "s_waitcnt lgkmcnt(0)\n"
        "s_add_u32 m0, s55, 20480\n"
        "s_nop 0\n"
        "buffer_load_dwordx4 v2, s[44:47], s52 offen lds\n"
        "s_add_u32 s52, s52, 9216\n"
        "s_add_u32 m0, s55, 21504\n"
        "s_nop 0\n"
        "buffer_load_dwordx4 v2, s[44:47], s52 offen lds\n"
        "s_add_u32 s52, s52, 9216\n"
        "s_add_u32 m0, s55, 22528\n"
        "s_nop 0\n"
        "buffer_load_dwordx4 v2, s[44:47], s52 offen lds\n"
        "s_add_u32 s52, s52, 9216\n"
        "s_add_u32 m0, s55, 23552\n"
        "s_nop 0\n"
        "buffer_load_dwordx4 v2, s[44:47], s52 offen lds\n"
        "s_add_u32 s52, s52, 9216\n"
        "s_waitcnt vmcnt(16)\n"
        "ds_read_b64_tr_b16 a[252:253], v3 offset:24576\n"
        "ds_read_b64_tr_b16 a[254:255], v3 offset:25600\n"
        "ds_read_b64_tr_b16 v[128:129], v4 offset:24576\n"
        "ds_read_b64_tr_b16 v[130:131], v4 offset:25600\n"
        "ds_read_b64_tr_b16 v[132:133], v5 offset:24576\n"
        "ds_read_b64_tr_b16 v[134:135], v5 offset:25600\n"
        "ds_read_b64_tr_b16 v[20:21], v6 offset:24576\n"
        "ds_read_b64_tr_b16 v[22:23], v6 offset:25600\n"
        "s_waitcnt lgkmcnt(0)\n"
        "s_add_u32 m0, s55, 24576\n"
        "s_nop 0\n"
        "buffer_load_dwordx4 v2, s[44:47], s52 offen lds\n"
        "s_add_u32 s52, s52, 9216\n"
        "s_add_u32 m0, s55, 25600\n"
        "s_nop 0\n"
        "buffer_load_dwordx4 v2, s[44:47], s52 offen lds\n"
        "s_add_u32 s52, s52, 9216\n"
        "s_add_u32 m0, s55, 26624\n"
        "s_nop 0\n"
        "buffer_load_dwordx4 v2, s[44:47], s52 offen lds\n"
        "s_add_u32 s52, s52, 9216\n"
        "s_add_u32 m0, s55, 27648\n"
        "s_nop 0\n"
        "buffer_load_dwordx4 v2, s[44:47], s52 offen lds\n"
        "s_add_u32 s52, s52, 9216\n"
        "s_waitcnt vmcnt(16)\n"
        "ds_read_b64_tr_b16 v[136:137], v3 offset:28672\n"
        "ds_read_b64_tr_b16 v[138:139], v3 offset:29696\n"
        "ds_read_b64_tr_b16 v[140:141], v4 offset:28672\n"
        "ds_read_b64_tr_b16 v[142:143], v4 offset:29696\n"
        "ds_read_b64_tr_b16 v[144:145], v5 offset:28672\n"
        "ds_read_b64_tr_b16 v[146:147], v5 offset:29696\n"
        "ds_read_b64_tr_b16 v[24:25], v6 offset:28672\n"
        "ds_read_b64_tr_b16 v[26:27], v6 offset:29696\n"
        "s_waitcnt lgkmcnt(0)\n"
        "s_add_u32 m0, s55, 28672\n"
        "s_nop 0\n"
        "buffer_load_dwordx4 v2, s[44:47], s52 offen lds\n"
        "s_add_u32 s52, s52, 9216\n"
        "s_add_u32 m0, s55, 29696\n"
        "s_nop 0\n"
        "buffer_load_dwordx4 v2, s[44:47], s52 offen lds\n"
        "s_add_u32 s52, s52, 9216\n"
        "s_add_u32 m0, s55, 30720\n"
        "s_nop 0\n"
        "buffer_load_dwordx4 v2, s[44:47], s52 offen lds\n"
        "s_add_u32 s52, s52, 9216\n"
        "s_add_u32 m0, s55, 31744\n"
        "s_nop 0\n"
        "buffer_load_dwordx4 v2, s[44:47], s52 offen lds\n"
        "s_add_u32 s52, s52, 9216\n"
        "s_waitcnt vmcnt(16)\n"
        "ds_read_b64_tr_b16 v[148:149], v3 offset:32768\n"
        "ds_read_b64_tr_b16 v[150:151], v3 offset:33792\n"
        "ds_read_b64_tr_b16 v[152:153], v4 offset:32768\n"
        "ds_read_b64_tr_b16 v[154:155], v4 offset:33792\n"
        "ds_read_b64_tr_b16 v[156:157], v5 offset:32768\n"
        "ds_read_b64_tr_b16 v[158:159], v5 offset:33792\n"
        "ds_read_b64_tr_b16 v[28:29], v6 offset:32768\n"
        "ds_read_b64_tr_b16 v[30:31], v6 offset:33792\n"
        "s_waitcnt lgkmcnt(0)\n"
        "s_add_u32 m0, s55, 32768\n"
        "s_nop 0\n"
        "buffer_load_dwordx4 v2, s[44:47], s52 offen lds\n"
        "s_add_u32 s52, s52, 9216\n"
        "s_add_u32 m0, s55, 33792\n"
        "s_nop 0\n"
        "buffer_load_dwordx4 v2, s[44:47], s52 offen lds\n"
        "s_add_u32 s52, s52, 9216\n"
        "s_add_u32 m0, s55, 34816\n"
        "s_nop 0\n"
        "buffer_load_dwordx4 v2, s[44:47], s52 offen lds\n"
        "s_add_u32 s52, s52, 9216\n"
        "s_add_u32 m0, s55, 35840\n"
        "s_nop 0\n"
        "buffer_load_dwordx4 v2, s[44:47], s52 offen lds\n"
        "s_add_u32 s52, s52, 9216\n"
        "s_waitcnt vmcnt(16)\n"
        "ds_read_b64_tr_b16 v[160:161], v3 offset:36864\n"
        "ds_read_b64_tr_b16 v[162:163], v3 offset:37888\n"
        "ds_read_b64_tr_b16 v[164:165], v4 offset:36864\n"
        "ds_read_b64_tr_b16 v[166:167], v4 offset:37888\n"
        "ds_read_b64_tr_b16 v[168:169], v5 offset:36864\n"
        "ds_read_b64_tr_b16 v[170:171], v5 offset:37888\n"
        "ds_read_b64_tr_b16 v[32:33], v6 offset:36864\n"
        "ds_read_b64_tr_b16 v[34:35], v6 offset:37888\n"
        "s_waitcnt lgkmcnt(0)\n"
        "s_add_u32 m0, s55, 36864\n"
        "s_nop 0\n"
        "buffer_load_dwordx4 v2, s[44:47], s52 offen lds\n"
        "s_add_u32 s52, s52, 9216\n"
        "s_add_u32 m0, s55, 37888\n"
        "s_nop 0\n"
        "buffer_load_dwordx4 v2, s[44:47], s52 offen lds\n"
        "s_add_u32 s52, s52, 9216\n"
        "s_add_u32 m0, s55, 38912\n"
        "s_nop 0\n"
        "buffer_load_dwordx4 v2, s[44:47], s52 offen lds\n"
        "s_add_u32 s52, s52, 9216\n"
        "s_add_u32 m0, s55, 39936\n"
        "s_nop 0\n"
        "buffer_load_dwordx4 v2, s[44:47], s52 offen lds\n"
        "s_add_u32 s52, s52, 9216\n"
        "s_waitcnt vmcnt(16)\n"
        "ds_read_b64_tr_b16 v[172:173], v3 offset:20480\n"
        "ds_read_b64_tr_b16 v[174:175], v3 offset:21504\n"
        "ds_read_b64_tr_b16 v[176:177], v4 offset:20480\n"
        "ds_read_b64_tr_b16 v[178:179], v4 offset:21504\n"
        "ds_read_b64_tr_b16 v[180:181], v5 offset:20480\n"
        "ds_read_b64_tr_b16 v[182:183], v5 offset:21504\n"
        "ds_read_b64_tr_b16 v[36:37], v6 offset:20480\n"
        "ds_read_b64_tr_b16 v[38:39], v6 offset:21504\n"
        "s_waitcnt lgkmcnt(0)\n"
        "s_add_u32 m0, s55, 20480\n"
        "s_nop 0\n"
        "buffer_load_dwordx4 v2, s[44:47], s52 offen lds\n"
        "s_add_u32 s52, s52, 9216\n"
        "s_add_u32 m0, s55, 21504\n"
        "s_nop 0\n"
        "buffer_load_dwordx4 v2, s[44:47], s52 offen lds\n"
        "s_add_u32 s52, s52, 9216\n"
        "s_add_u32 m0, s55, 22528\n"
        "s_nop 0\n"
        "buffer_load_dwordx4 v2, s[44:47], s52 offen lds\n"
        "s_add_u32 s52, s52, 9216\n"
        "s_add_u32 m0, s55, 23552\n"
        "s_nop 0\n"
        "buffer_load_dwordx4 v2, s[44:47], s52 offen lds\n"
        "s_add_u32 s52, s52, 9216\n"
        "s_waitcnt vmcnt(16)\n"
        "ds_read_b64_tr_b16 v[184:185], v3 offset:24576\n"
        "ds_read_b64_tr_b16 v[186:187], v3 offset:25600\n"
        "ds_read_b64_tr_b16 v[188:189], v4 offset:24576\n"
        "ds_read_b64_tr_b16 v[190:191], v4 offset:25600\n"
        "ds_read_b64_tr_b16 v[192:193], v5 offset:24576\n"
        "ds_read_b64_tr_b16 v[194:195], v5 offset:25600\n"
        "ds_read_b64_tr_b16 v[40:41], v6 offset:24576\n"
        "ds_read_b64_tr_b16 v[42:43], v6 offset:25600\n"
        "s_waitcnt lgkmcnt(0)\n"
        "s_add_u32 m0, s55, 24576\n"
        "s_nop 0\n"
        "buffer_load_dwordx4 v2, s[44:47], s52 offen lds\n"
        "s_add_u32 s52, s52, 9216\n"
        "s_add_u32 m0, s55, 25600\n"
        "s_nop 0\n"
        "buffer_load_dwordx4 v2, s[44:47], s52 offen lds\n"
        "s_add_u32 s52, s52, 9216\n"
        "s_add_u32 m0, s55, 26624\n"
        "s_nop 0\n"
        "buffer_load_dwordx4 v2, s[44:47], s52 offen lds\n"
        "s_add_u32 s52, s52, 9216\n"
        "s_add_u32 m0, s55, 27648\n"
        "s_nop 0\n"
        "buffer_load_dwordx4 v2, s[44:47], s52 offen lds\n"
        "s_add_u32 s52, s52, 9216\n"
        "s_waitcnt vmcnt(16)\n"
        "ds_read_b64_tr_b16 v[196:197], v3 offset:28672\n"
        "ds_read_b64_tr_b16 v[198:199], v3 offset:29696\n"
        "ds_read_b64_tr_b16 v[200:201], v4 offset:28672\n"
        "ds_read_b64_tr_b16 v[202:203], v4 offset:29696\n"
        "ds_read_b64_tr_b16 v[204:205], v5 offset:28672\n"
        "ds_read_b64_tr_b16 v[206:207], v5 offset:29696\n"
        "ds_read_b64_tr_b16 v[44:45], v6 offset:28672\n"
        "ds_read_b64_tr_b16 v[46:47], v6 offset:29696\n"
        "s_waitcnt lgkmcnt(0)\n"
        "s_waitcnt vmcnt(12)\n"
        "ds_read_b64_tr_b16 v[208:209], v3 offset:32768\n"
        "ds_read_b64_tr_b16 v[210:211], v3 offset:33792\n"
        "ds_read_b64_tr_b16 v[212:213], v4 offset:32768\n"
        "ds_read_b64_tr_b16 v[214:215], v4 offset:33792\n"
        "ds_read_b64_tr_b16 v[216:217], v5 offset:32768\n"
        "ds_read_b64_tr_b16 v[218:219], v5 offset:33792\n"
        "ds_read_b64_tr_b16 v[48:49], v6 offset:32768\n"
        "ds_read_b64_tr_b16 v[50:51], v6 offset:33792\n"
        "s_waitcnt lgkmcnt(0)\n"
        "s_waitcnt vmcnt(8)\n"
        "ds_read_b64_tr_b16 v[220:221], v3 offset:36864\n"
        "ds_read_b64_tr_b16 v[222:223], v3 offset:37888\n"
        "ds_read_b64_tr_b16 v[224:225], v4 offset:36864\n"
        "ds_read_b64_tr_b16 v[226:227], v4 offset:37888\n"
        "ds_read_b64_tr_b16 v[228:229], v5 offset:36864\n"
        "ds_read_b64_tr_b16 v[230:231], v5 offset:37888\n"
        "ds_read_b64_tr_b16 v[52:53], v6 offset:36864\n"
        "ds_read_b64_tr_b16 v[54:55], v6 offset:37888\n"
        "s_waitcnt lgkmcnt(0)\n"
        "s_waitcnt vmcnt(4)\n"
        "ds_read_b64_tr_b16 v[232:233], v3 offset:20480\n"
        "ds_read_b64_tr_b16 v[234:235], v3 offset:21504\n"
        "ds_read_b64_tr_b16 v[236:237], v4 offset:20480\n"
        "ds_read_b64_tr_b16 v[238:239], v4 offset:21504\n"
        "ds_read_b64_tr_b16 v[240:241], v5 offset:20480\n"
        "ds_read_b64_tr_b16 v[242:243], v5 offset:21504\n"
        "ds_read_b64_tr_b16 v[56:57], v6 offset:20480\n"
        "ds_read_b64_tr_b16 v[58:59], v6 offset:21504\n"
        "s_waitcnt lgkmcnt(0)\n"
        "s_waitcnt vmcnt(0)\n"
        "ds_read_b64_tr_b16 v[244:245], v3 offset:24576\n"
        "ds_read_b64_tr_b16 v[246:247], v3 offset:25600\n"
        "ds_read_b64_tr_b16 v[248:249], v4 offset:24576\n"
        "ds_read_b64_tr_b16 v[250:251], v4 offset:25600\n"
        "ds_read_b64_tr_b16 v[252:253], v5 offset:24576\n"
        "ds_read_b64_tr_b16 v[254:255], v5 offset:25600\n"
        "ds_read_b64_tr_b16 v[60:61], v6 offset:24576\n"
        "ds_read_b64_tr_b16 v[62:63], v6 offset:25600\n"
        "s_waitcnt lgkmcnt(0)\n"
        "ds_write_b128 v1, v[16:19] offset:20480\n"
        "ds_write_b128 v1, v[20:23] offset:21504\n"
        "ds_write_b128 v1, v[24:27] offset:22528\n"
        "ds_write_b128 v1, v[28:31] offset:23552\n"
        "ds_write_b128 v1, v[32:35] offset:24576\n"
        "s_waitcnt lgkmcnt(0)\n"
        "ds_write_b128 v1, v[36:39] offset:25600\n"
        "ds_write_b128 v1, v[40:43] offset:26624\n"
        "ds_write_b128 v1, v[44:47] offset:27648\n"
        "ds_write_b128 v1, v[48:51] offset:28672\n"
        "ds_write_b128 v1, v[52:55] offset:29696\n"
        "ds_write_b128 v1, v[56:59] offset:30720\n"
        "ds_write_b128 v1, v[60:63] offset:31744\n"
        "s_mov_b32 s53, 0x10000\n"
        "s_lshl_b32 s52, %10, 7\n"
        "s_add_u32 s52, s52, 0x440000\n"
        "60:\n"
        "v_cmp_ne_u32 vcc, v7, v8\n"
        "s_cbranch_vccnz 61f\n"
        "s_sub_u32 s53, s53, 1\n"
        "s_cbranch_scc1 61f\n"
        "s_and_b32 vcc_lo, s53, 15\n"
        "s_cbranch_scc1 62f\n"
        "s_memrealtime vcc\n"
        "s_waitcnt lgkmcnt(0)\n"
        "v_mov_b32 v10, vcc_lo\n"
        "buffer_store_dword v10, off, s[40:43], s58 sc1\n"
        "62:\n"
        "buffer_load_dwordx2 v[8:9], off, s[40:43], s52 sc1\n"
        "s_waitcnt vmcnt(0)\n"
        "s_branch 60b\n"
        "61:\n"
        "s_lshl2_add_u32 s52, %10, %8\n"
        "s_lshl2_add_u32 s52, s52, 0x425800\n"
        "s_memrealtime vcc\n"
        "s_waitcnt lgkmcnt(0)\n"
        "v_or_b32 v7, vcc_lo, 1\n"
        "buffer_store_dword v7, off, s[40:43], s52 sc1\n"
        "v_lshrrev_b32 v8, 4, v11\n"
        "v_and_b32 v9, 15, v11\n"
        "v_lshlrev_b32 v7, 3, v11\n"
        "v_mul_u32_u24 v10, 1152, v8\n"
        "v_lshl_add_u32 v10, v9, 3, v10\n"
        "v_xor_b32 v5, v8, v9\n"
        "v_lshlrev_b32 v5, 4, v5\n"
        "s_lshl_b32 s52, %8, 6\n"
        "v_xor_b32 v6, s52, v5\n"
        "v_lshl_add_u32 v6, v9, 8, v6\n"
        "v_add_u32 v6, 36864, v6\n"
        "v_lshl_add_u32 v5, v8, 8, v5\n"
        "v_add_u32 v5, s55, v5\n"
        "v_lshrrev_b32 v8, 5, v11\n"
        "v_lshl_add_u32 v4, v8, 1, 1\n"
        "v_add_u32 v4, s48, v4\n"
        "v_and_b32 v2, 31, v11\n"
        "s_and_b32 s52, %6, 7\n"
        "s_lshl_b32 s52, s52, 12\n"
        "v_lshl_add_u32 v2, v2, 7, s52\n"
        "v_mad_u32_u24 v2, v8, %3, v2\n"
        "s_add_u32 s50, s50, 0x404000\n"
        "v_and_b32 v9, 31, v11\n"
        "v_min_u32 v9, 7, v9\n"
        "v_lshl_add_u32 v9, v9, 2, s50\n"
        "v_lshl_add_u32 v3, v8, 11, v9\n"
        "v_writelane_b32 v3, s57, 31\n"
        "v_writelane_b32 v3, s57, 63\n"
        "s_mov_b64 s[44:45], %5\n"
        "s_mov_b32 s46, 2359296\n"
        "s_mov_b32 s58, 0x7fffffff\n"
        "s_mov_b32 s59, 0x7fffffff\n"
        "s_mov_b32 s66, 62\n"
        "s_waitcnt lgkmcnt(0)\n"
        "s_mov_b32 s67, %13\n"
        "s_cmp_eq_u32 %12, 0\n"
        "s_cbranch_scc1 31f\n"
        "s_mov_b32 s67, 0\n"
        "s_add_u32 s52, s56, 73728\n"
        "s_mov_b32 s53, 0x3800000+14680064\n"
        "buffer_load_dword v9, v10, s[60:63], s52 offen\n"
        "buffer_store_dwordx2 v[124:125], v10, s[44:47], s52 offen sc1\n"
        "buffer_load_dword v9, off, s[40:43], s53 sc1\n"
        "v_or_b32 v0, 0x80000000, v0\n"
        "v_or_b32 v10, 0x80000000, v10\n"
        "s_branch 32f\n"
        "31:\n"
        "buffer_load_dword v8, v3, s[40:43], 0 offen sc1\n"
        "s_waitcnt vmcnt(0)\n"
        "v_cmp_gt_u32 vcc, v4, v8\n"
        "s_cmp_eq_u32 vcc_lo, 0\n"
        "s_cbranch_scc1 32f\n"
        "s_cmp_eq_u32 s54, 0\n"
        "s_cbranch_scc1 32f\n"
        "s_sub_u32 s54, s54, 1\n"
        "s_sleep 2\n"
        "s_branch 31b\n"
        "32:\n"
        "s_cmp_eq_u32 vcc_hi, 0\n"
        "s_cselect_b32 exec_hi, -1, 0\n"
        "s_nop 0\n"
        "s_mov_b32 exec_hi, -1\n"
        "s_add_u32 s52, s49, 0\n"
        "buffer_load_dwordx4 v[36:39], v0, s[40:43], s52 offen offset:0 sc1\n"
        "buffer_load_dwordx4 v[40:43], v0, s[40:43], s52 offen offset:1024 sc1\n"
        "buffer_load_dwordx4 v[44:47], v0, s[40:43], s52 offen offset:2048 sc1\n"
        "buffer_load_dwordx4 v[48:51], v0, s[40:43], s52 offen offset:3072 sc1\n"
        "s_add_u32 s52, s49, 4096\n"
        "buffer_load_dwordx4 v[52:55], v0, s[40:43], s52 offen offset:0 sc1\n"
        "buffer_load_dwordx4 v[56:59], v0, s[40:43], s52 offen offset:1024 sc1\n"
        "buffer_load_dwordx4 v[60:63], v0, s[40:43], s52 offen offset:2048 sc1\n"
        "buffer_load_dwordx4 v[64:67], v0, s[40:43], s52 offen offset:3072 sc1\n"
        "s_add_u32 s52, s49, 8192\n"
        "buffer_load_dwordx4 v[68:71], v0, s[40:43], s52 offen offset:0 sc1\n"
        "buffer_load_dwordx4 v[72:75], v0, s[40:43], s52 offen offset:1024 sc1\n"
        "buffer_load_dwordx4 v[76:79], v0, s[40:43], s52 offen offset:2048 sc1\n"
        "buffer_load_dwordx4 v[80:83], v0, s[40:43], s52 offen offset:3072 sc1\n"
        "v_add_u32 v4, 2, v4\n"
        "s_mov_b64 exec, s[58:59]\n"
        "v_add_u32 v3, 2048, v3\n"
        "s_mov_b64 exec, -1\n"
        "33:\n"
        "ds_read_b128 v[28:31], v1 offset:0\n"
        "buffer_load_dwordx2 v[126:127], v7, s[40:43], s51 offen sc1\n"
        "buffer_load_dwordx2 v[124:125], v10, s[60:63], s56 offen\n"
        "s_waitcnt vmcnt(13)\n"
        "v_mfma_f32_16x16x32_bf16 v[12:15], a[0:3], v[36:39], 0\n"
        "s_add_u32 s52, s49, 12288\n"
        "buffer_load_dwordx4 v[84:87], v0, s[40:43], s52 offen offset:0 sc1\n"
        "v_mfma_f32_16x16x32_bf16 v[16:19], a[4:7], v[36:39], 0\n"
        "ds_read_b128 v[32:35], v1 offset:1024\n"
        "v_mfma_f32_16x16x32_bf16 v[20:23], a[8:11], v[36:39], 0\n"
        "s_waitcnt lgkmcnt(1)\n"
        "v_mfma_f32_16x16x32_bf16 v[24:27], v[28:31], v[36:39], 0\n"
        "s_waitcnt vmcnt(13)\n"
        "v_mfma_f32_16x16x32_bf16 v[12:15], a[12:15], v[40:43], v[12:15]\n"
        "buffer_load_dwordx4 v[36:39], v0, s[40:43], s52 offen offset:1024 sc1\n"
        "v_mfma_f32_16x16x32_bf16 v[16:19], a[16:19], v[40:43], v[16:19]\n"
        "ds_read_b128 v[28:31], v1 offset:2048\n"
        "v_mfma_f32_16x16x32_bf16 v[20:23], a[20:23], v[40:43], v[20:23]\n"
        "s_waitcnt lgkmcnt(1)\n"
        "v_mfma_f32_16x16x32_bf16 v[24:27], v[32:35], v[40:43], v[24:27]\n"
        "s_waitcnt vmcnt(13)\n"
        "v_mfma_f32_16x16x32_bf16 v[12:15], a[24:27], v[44:47], v[12:15]\n"
        "buffer_load_dwordx4 v[40:43], v0, s[40:43], s52 offen offset:2048 sc1\n"
        "v_mfma_f32_16x16x32_bf16 v[16:19], a[28:31], v[44:47], v[16:19]\n"
        "ds_read_b128 v[32:35], v1 offset:3072\n"
        "v_mfma_f32_16x16x32_bf16 v[20:23], a[32:35], v[44:47], v[20:23]\n"
        "s_waitcnt lgkmcnt(1)\n"
        "v_mfma_f32_16x16x32_bf16 v[24:27], v[28:31], v[44:47], v[24:27]\n"
        "s_waitcnt vmcnt(13)\n"
        "v_mfma_f32_16x16x32_bf16 v[12:15], a[36:39], v[48:51], v[12:15]\n"
        "buffer_load_dwordx4 v[44:47], v0, s[40:43], s52 offen offset:3072 sc1\n"
        "v_mfma_f32_16x16x32_bf16 v[16:19], a[40:43], v[48:51], v[16:19]\n"
        "ds_read_b128 v[28:31], v1 offset:4096\n"
        "v_mfma_f32_16x16x32_bf16 v[20:23], a[44:47], v[48:51], v[20:23]\n"
        "s_waitcnt lgkmcnt(1)\n"
        "v_mfma_f32_16x16x32_bf16 v[24:27], v[32:35], v[48:51], v[24:27]\n"
        "s_waitcnt vmcnt(13)\n"
        "v_mfma_f32_16x16x32_bf16 v[12:15], a[48:51], v[52:55], v[12:15]\n"
        "s_add_u32 s52, s49, 16384\n"
        "buffer_load_dwordx4 v[48:51], v0, s[40:43], s52 offen offset:0 sc1\n"
        "v_mfma_f32_16x16x32_bf16 v[16:19], a[52:55], v[52:55], v[16:19]\n"
        "ds_read_b128 v[32:35], v1 offset:5120\n"
        "v_mfma_f32_16x16x32_bf16 v[20:23], a[56:59], v[52:55], v[20:23]\n"
        "s_waitcnt lgkmcnt(1)\n"
        "v_mfma_f32_16x16x32_bf16 v[24:27], v[28:31], v[52:55], v[24:27]\n"
        "s_waitcnt vmcnt(13)\n"
        "v_mfma_f32_16x16x32_bf16 v[12:15], a[60:63], v[56:59], v[12:15]\n"
        "buffer_load_dwordx4 v[52:55], v0, s[40:43], s52 offen offset:1024 sc1\n"
        "v_mfma_f32_16x16x32_bf16 v[16:19], a[64:67], v[56:59], v[16:19]\n"
        "ds_read_b128 v[28:31], v1 offset:6144\n"
        "v_mfma_f32_16x16x32_bf16 v[20:23], a[68:71], v[56:59], v[20:23]\n"
        "s_waitcnt lgkmcnt(1)\n"
        "v_mfma_f32_16x16x32_bf16 v[24:27], v[32:35], v[56:59], v[24:27]\n"
        "s_waitcnt vmcnt(13)\n"
        "v_mfma_f32_16x16x32_bf16 v[12:15], a[72:75], v[60:63], v[12:15]\n"
        "buffer_load_dwordx4 v[56:59], v0, s[40:43], s52 offen offset:2048 sc1\n"
        "v_mfma_f32_16x16x32_bf16 v[16:19], a[76:79], v[60:63], v[16:19]\n"
        "ds_read_b128 v[32:35], v1 offset:7168\n"
        "v_mfma_f32_16x16x32_bf16 v[20:23], a[80:83], v[60:63], v[20:23]\n"
        "s_waitcnt lgkmcnt(1)\n"
        "v_mfma_f32_16x16x32_bf16 v[24:27], v[28:31], v[60:63], v[24:27]\n"
        "s_waitcnt vmcnt(13)\n"
        "v_mfma_f32_16x16x32_bf16 v[12:15], a[84:87], v[64:67], v[12:15]\n"
        "buffer_load_dwordx4 v[60:63], v0, s[40:43], s52 offen offset:3072 sc1\n"
        "v_mfma_f32_16x16x32_bf16 v[16:19], a[88:91], v[64:67], v[16:19]\n"
        "ds_read_b128 v[28:31], v1 offset:8192\n"
        "v_mfma_f32_16x16x32_bf16 v[20:23], a[92:95], v[64:67], v[20:23]\n"
        "s_waitcnt lgkmcnt(1)\n"
        "v_mfma_f32_16x16x32_bf16 v[24:27], v[32:35], v[64:67], v[24:27]\n"
        "s_waitcnt vmcnt(13)\n"
        "v_mfma_f32_16x16x32_bf16 v[12:15], a[96:99], v[68:71], v[12:15]\n"
        "s_add_u32 s52, s49, 20480\n"
        "buffer_load_dwordx4 v[64:67], v0, s[40:43], s52 offen offset:0 sc1\n"
        "v_mfma_f32_16x16x32_bf16 v[16:19], a[100:103], v[68:71], v[16:19]\n"
        "ds_read_b128 v[32:35], v1 offset:9216\n"
        "v_mfma_f32_16x16x32_bf16 v[20:23], a[104:107], v[68:71], v[20:23]\n"
        "s_waitcnt lgkmcnt(1)\n"
        "v_mfma_f32_16x16x32_bf16 v[24:27], v[28:31], v[68:71], v[24:27]\n"
        "s_waitcnt vmcnt(13)\n"
        "v_mfma_f32_16x16x32_bf16 v[12:15], a[108:111], v[72:75], v[12:15]\n"
        "buffer_load_dwordx4 v[68:71], v0, s[40:43], s52 offen offset:1024 sc1\n"
        "v_mfma_f32_16x16x32_bf16 v[16:19], a[112:115], v[72:75], v[16:19]\n"
        "ds_read_b128 v[28:31], v1 offset:10240\n"
        "v_mfma_f32_16x16x32_bf16 v[20:23], a[116:119], v[72:75], v[20:23]\n"
        "s_waitcnt lgkmcnt(1)\n"
        "v_mfma_f32_16x16x32_bf16 v[24:27], v[32:35], v[72:75], v[24:27]\n"
        "s_waitcnt vmcnt(13)\n"
        "v_mfma_f32_16x16x32_bf16 v[12:15], a[120:123], v[76:79], v[12:15]\n"
        "buffer_load_dwordx4 v[72:75], v0, s[40:43], s52 offen offset:2048 sc1\n"
        "v_mfma_f32_16x16x32_bf16 v[16:19], a[124:127], v[76:79], v[16:19]\n"
        "ds_read_b128 v[32:35], v1 offset:11264\n"
        "v_mfma_f32_16x16x32_bf16 v[20:23], a[128:131], v[76:79], v[20:23]\n"
        "s_waitcnt lgkmcnt(1)\n"
        "v_mfma_f32_16x16x32_bf16 v[24:27], v[28:31], v[76:79], v[24:27]\n"
        "s_waitcnt vmcnt(13)\n"
        "v_mfma_f32_16x16x32_bf16 v[12:15], a[132:135], v[80:83], v[12:15]\n"
        "buffer_load_dwordx4 v[76:79], v0, s[40:43], s52 offen offset:3072 sc1\n"
        "v_mfma_f32_16x16x32_bf16 v[16:19], a[136:139], v[80:83], v[16:19]\n"
        "ds_read_b128 v[28:31], v1 offset:12288\n"
        "v_mfma_f32_16x16x32_bf16 v[20:23], a[140:143], v[80:83], v[20:23]\n"
        "s_waitcnt lgkmcnt(1)\n"
        "v_mfma_f32_16x16x32_bf16 v[24:27], v[32:35], v[80:83], v[24:27]\n"
        "s_waitcnt vmcnt(11)\n"
        "v_mfma_f32_16x16x32_bf16 v[12:15], a[144:147], v[84:87], v[12:15]\n"
        "s_add_u32 s52, s49, 24576\n"
        "buffer_load_dwordx4 v[80:83], v0, s[40:43], s52 offen offset:0 sc1\n"
        "v_mfma_f32_16x16x32_bf16 v[16:19], a[148:151], v[84:87], v[16:19]\n"
        "ds_read_b128 v[32:35], v1 offset:13312\n"
        "v_mfma_f32_16x16x32_bf16 v[20:23], a[152:155], v[84:87], v[20:23]\n"
        "s_waitcnt lgkmcnt(1)\n"
        "v_mfma_f32_16x16x32_bf16 v[24:27], v[28:31], v[84:87], v[24:27]\n"
        "s_waitcnt vmcnt(11)\n"
        "v_mfma_f32_16x16x32_bf16 v[12:15], a[156:159], v[36:39], v[12:15]\n"
        "buffer_load_dwordx4 v[84:87], v0, s[40:43], s52 offen offset:1024 sc1\n"
        "v_mfma_f32_16x16x32_bf16 v[16:19], a[160:163], v[36:39], v[16:19]\n"
        "ds_read_b128 v[28:31], v1 offset:14336\n"
        "v_mfma_f32_16x16x32_bf16 v[20:23], a[164:167], v[36:39], v[20:23]\n"
        "s_waitcnt lgkmcnt(1)\n"
        "v_mfma_f32_16x16x32_bf16 v[24:27], v[32:35], v[36:39], v[24:27]\n"
        "s_waitcnt vmcnt(11)\n"
        "v_mfma_f32_16x16x32_bf16 v[12:15], a[168:171], v[40:43], v[12:15]\n"
        "buffer_load_dwordx4 v[36:39], v0, s[40:43], s52 offen offset:2048 sc1\n"
        "v_mfma_f32_16x16x32_bf16 v[16:19], a[172:175], v[40:43], v[16:19]\n"
        "ds_read_b128 v[32:35], v1 offset:15360\n"
        "v_mfma_f32_16x16x32_bf16 v[20:23], a[176:179], v[40:43], v[20:23]\n"
        "s_waitcnt lgkmcnt(1)\n"
        "v_mfma_f32_16x16x32_bf16 v[24:27], v[28:31], v[40:43], v[24:27]\n"
        "s_waitcnt vmcnt(11)\n"
        "v_mfma_f32_16x16x32_bf16 v[12:15], a[180:183], v[44:47], v[12:15]\n"
        "buffer_load_dwordx4 v[40:43], v0, s[40:43], s52 offen offset:3072 sc1\n"
        "v_mfma_f32_16x16x32_bf16 v[16:19], a[184:187], v[44:47], v[16:19]\n"
        "ds_read_b128 v[28:31], v1 offset:16384\n"
        "v_mfma_f32_16x16x32_bf16 v[20:23], a[188:191], v[44:47], v[20:23]\n"
        "s_waitcnt lgkmcnt(1)\n"
        "v_mfma_f32_16x16x32_bf16 v[24:27], v[32:35], v[44:47], v[24:27]\n"
        "s_waitcnt vmcnt(11)\n"
        "v_mfma_f32_16x16x32_bf16 v[12:15], a[192:195], v[48:51], v[12:15]\n"
        "s_add_u32 s52, s49, 28672\n"
        "buffer_load_dwordx4 v[44:47], v0, s[40:43], s52 offen offset:0 sc1\n"
        "v_mfma_f32_16x16x32_bf16 v[16:19], a[196:199], v[48:51], v[16:19]\n"
        "ds_read_b128 v[32:35], v1 offset:17408\n"
        "v_mfma_f32_16x16x32_bf16 v[20:23], a[200:203], v[48:51], v[20:23]\n"
        "s_waitcnt lgkmcnt(1)\n"
        "v_mfma_f32_16x16x32_bf16 v[24:27], v[28:31], v[48:51], v[24:27]\n"
        "s_waitcnt vmcnt(11)\n"
        "v_mfma_f32_16x16x32_bf16 v[12:15], a[204:207], v[52:55], v[12:15]\n"
        "buffer_load_dwordx4 v[48:51], v0, s[40:43], s52 offen offset:1024 sc1\n"
        "v_mfma_f32_16x16x32_bf16 v[16:19], a[208:211], v[52:55], v[16:19]\n"
        "ds_read_b128 v[28:31], v1 offset:18432\n"
        "v_mfma_f32_16x16x32_bf16 v[20:23], a[212:215], v[52:55], v[20:23]\n"
        "s_waitcnt lgkmcnt(1)\n"
        "v_mfma_f32_16x16x32_bf16 v[24:27], v[32:35], v[52:55], v[24:27]\n"
        "s_waitcnt vmcnt(11)\n"
        "v_mfma_f32_16x16x32_bf16 v[12:15], a[216:219], v[56:59], v[12:15]\n"
        "buffer_load_dwordx4 v[52:55], v0, s[40:43], s52 offen offset:2048 sc1\n"
        "v_mfma_f32_16x16x32_bf16 v[16:19], a[220:223], v[56:59], v[16:19]\n"
        "ds_read_b128 v[32:35], v1 offset:19456\n"
        "v_mfma_f32_16x16x32_bf16 v[20:23], a[224:227], v[56:59], v[20:23]\n"
        "s_waitcnt lgkmcnt(1)\n"
        "v_mfma_f32_16x16x32_bf16 v[24:27], v[28:31], v[56:59], v[24:27]\n"
        "s_waitcnt vmcnt(11)\n"
        "v_mfma_f32_16x16x32_bf16 v[12:15], a[228:231], v[60:63], v[12:15]\n"
        "buffer_load_dwordx4 v[56:59], v0, s[40:43], s52 offen offset:3072 sc1\n"
        "v_mfma_f32_16x16x32_bf16 v[16:19], a[232:235], v[60:63], v[16:19]\n"
        "ds_read_b128 v[28:31], v1 offset:20480\n"
        "v_mfma_f32_16x16x32_bf16 v[20:23], a[236:239], v[60:63], v[20:23]\n"
        "s_waitcnt lgkmcnt(1)\n"
        "v_mfma_f32_16x16x32_bf16 v[24:27], v[32:35], v[60:63], v[24:27]\n"
        "s_waitcnt vmcnt(11)\n"
        "v_mfma_f32_16x16x32_bf16 v[12:15], a[240:243], v[64:67], v[12:15]\n"
        "buffer_load_dword v8, v3, s[40:43], 0 offen sc1\n"
        "s_add_u32 s49, s49, %3\n"
        "v_mfma_f32_16x16x32_bf16 v[16:19], a[244:247], v[64:67], v[16:19]\n"
        "ds_read_b128 v[32:35], v1 offset:21504\n"
        "v_mfma_f32_16x16x32_bf16 v[20:23], a[248:251], v[64:67], v[20:23]\n"
        "s_waitcnt lgkmcnt(1)\n"
        "v_mfma_f32_16x16x32_bf16 v[24:27], v[28:31], v[64:67], v[24:27]\n"
        "s_waitcnt vmcnt(11)\n"
        "v_mfma_f32_16x16x32_bf16 v[12:15], a[252:255], v[68:71], v[12:15]\n"
        "v_mfma_f32_16x16x32_bf16 v[16:19], v[128:131], v[68:71], v[16:19]\n"
        "ds_read_b128 v[28:31], v1 offset:22528\n"
        "v_mfma_f32_16x16x32_bf16 v[20:23], v[132:135], v[68:71], v[20:23]\n"
        "s_waitcnt lgkmcnt(1)\n"
        "v_mfma_f32_16x16x32_bf16 v[24:27], v[32:35], v[68:71], v[24:27]\n"
        "s_waitcnt vmcnt(10)\n"
        "v_mfma_f32_16x16x32_bf16 v[12:15], v[136:139], v[72:75], v[12:15]\n"
        "v_mfma_f32_16x16x32_bf16 v[16:19], v[140:143], v[72:75], v[16:19]\n"
        "ds_read_b128 v[32:35], v1 offset:23552\n"
        "v_mfma_f32_16x16x32_bf16 v[20:23], v[144:147], v[72:75], v[20:23]\n"
        "s_waitcnt lgkmcnt(1)\n"
        "v_mfma_f32_16x16x32_bf16 v[24:27], v[28:31], v[72:75], v[24:27]\n"
        "s_waitcnt vmcnt(9)\n"
        "v_mfma_f32_16x16x32_bf16 v[12:15], v[148:151], v[76:79], v[12:15]\n"
        "v_mfma_f32_16x16x32_bf16 v[16:19], v[152:155], v[76:79], v[16:19]\n"
        "ds_read_b128 v[28:31], v1 offset:24576\n"
        "v_mfma_f32_16x16x32_bf16 v[20:23], v[156:159], v[76:79], v[20:23]\n"
        "s_waitcnt lgkmcnt(1)\n"
        "v_mfma_f32_16x16x32_bf16 v[24:27], v[32:35], v[76:79], v[24:27]\n"
        "s_waitcnt vmcnt(8)\n"
        "v_mfma_f32_16x16x32_bf16 v[12:15], v[160:163], v[80:83], v[12:15]\n"
        "v_mfma_f32_16x16x32_bf16 v[16:19], v[164:167], v[80:83], v[16:19]\n"
        "ds_read_b128 v[32:35], v1 offset:25600\n"
        "v_mfma_f32_16x16x32_bf16 v[20:23], v[168:171], v[80:83], v[20:23]\n"
        "s_waitcnt lgkmcnt(1)\n"
        "v_mfma_f32_16x16x32_bf16 v[24:27], v[28:31], v[80:83], v[24:27]\n"
        "s_waitcnt vmcnt(7)\n"
        "v_mfma_f32_16x16x32_bf16 v[12:15], v[172:175], v[84:87], v[12:15]\n"
        "v_mfma_f32_16x16x32_bf16 v[16:19], v[176:179], v[84:87], v[16:19]\n"
        "ds_read_b128 v[28:31], v1 offset:26624\n"
        "v_mfma_f32_16x16x32_bf16 v[20:23], v[180:183], v[84:87], v[20:23]\n"
        "s_waitcnt lgkmcnt(1)\n"
        "v_mfma_f32_16x16x32_bf16 v[24:27], v[32:35], v[84:87], v[24:27]\n"
        "s_waitcnt vmcnt(6)\n"
        "v_mfma_f32_16x16x32_bf16 v[12:15], v[184:187], v[36:39], v[12:15]\n"
        "v_mfma_f32_16x16x32_bf16 v[16:19], v[188:191], v[36:39], v[16:19]\n"
        "ds_read_b128 v[32:35], v1 offset:27648\n"
        "v_mfma_f32_16x16x32_bf16 v[20:23], v[192:195], v[36:39], v[20:23]\n"
        "s_waitcnt lgkmcnt(1)\n"
        "v_mfma_f32_16x16x32_bf16 v[24:27], v[28:31], v[36:39], v[24:27]\n"
        "s_waitcnt vmcnt(5)\n"
        "v_mfma_f32_16x16x32_bf16 v[12:15], v[196:199], v[40:43], v[12:15]\n"
        "v_mfma_f32_16x16x32_bf16 v[16:19], v[200:203], v[40:43], v[16:19]\n"
        "ds_read_b128 v[28:31], v1 offset:28672\n"
        "v_mfma_f32_16x16x32_bf16 v[20:23], v[204:207], v[40:43], v[20:23]\n"
        "s_waitcnt lgkmcnt(1)\n"
        "v_mfma_f32_16x16x32_bf16 v[24:27], v[32:35], v[40:43], v[24:27]\n"
        "s_waitcnt vmcnt(4)\n"
        "v_mfma_f32_16x16x32_bf16 v[12:15], v[208:211], v[44:47], v[12:15]\n"
        "v_mfma_f32_16x16x32_bf16 v[16:19], v[212:215], v[44:47], v[16:19]\n"
        "ds_read_b128 v[32:35], v1 offset:29696\n"
        "v_mfma_f32_16x16x32_bf16 v[20:23], v[216:219], v[44:47], v[20:23]\n"
        "s_waitcnt lgkmcnt(1)\n"
        "v_mfma_f32_16x16x32_bf16 v[24:27], v[28:31], v[44:47], v[24:27]\n"
        "s_waitcnt vmcnt(3)\n"
        "v_mfma_f32_16x16x32_bf16 v[12:15], v[220:223], v[48:51], v[12:15]\n"
        "v_mfma_f32_16x16x32_bf16 v[16:19], v[224:227], v[48:51], v[16:19]\n"
        "ds_read_b128 v[28:31], v1 offset:30720\n"
        "v_mfma_f32_16x16x32_bf16 v[20:23], v[228:231], v[48:51], v[20:23]\n"
        "s_waitcnt lgkmcnt(1)\n"
        "v_mfma_f32_16x16x32_bf16 v[24:27], v[32:35], v[48:51], v[24:27]\n"
        "s_waitcnt vmcnt(2)\n"
        "v_mfma_f32_16x16x32_bf16 v[12:15], v[232:235], v[52:55], v[12:15]\n"
        "v_mfma_f32_16x16x32_bf16 v[16:19], v[236:239], v[52:55], v[16:19]\n"
        "ds_read_b128 v[32:35], v1 offset:31744\n"
        "v_mfma_f32_16x16x32_bf16 v[20:23], v[240:243], v[52:55], v[20:23]\n"
        "s_waitcnt lgkmcnt(1)\n"
        "v_mfma_f32_16x16x32_bf16 v[24:27], v[28:31], v[52:55], v[24:27]\n"
        "s_waitcnt vmcnt(1)\n"
        "v_mfma_f32_16x16x32_bf16 v[12:15], v[244:247], v[56:59], v[12:15]\n"
        "v_xor_b32 v28, 64, v5\n"
        "v_mfma_f32_16x16x32_bf16 v[16:19], v[248:251], v[56:59], v[16:19]\n"
        "v_xor_b32 v29, 128, v5\n"
        "v_mfma_f32_16x16x32_bf16 v[20:23], v[252:255], v[56:59], v[20:23]\n"
        "s_waitcnt lgkmcnt(0)\n"
        "v_xor_b32 v30, 192, v5\n"
        "v_mfma_f32_16x16x32_bf16 v[24:27], v[32:35], v[56:59], v[24:27]\n"
        "s_waitcnt vmcnt(0)\n"
        "v_cmp_gt_u32 vcc, v4, v8\n"
        "ds_write_b128 v5, v[12:15] offset:36864\n"
        "ds_write_b128 v28, v[16:19] offset:37888\n"
        "ds_write_b128 v29, v[20:23] offset:38912\n"
        "s_nop 2\n"
        "ds_write_b128 v30, v[24:27] offset:39936\n"
        "s_cmp_ge_u32 s48, s67\n"
        "s_cbranch_scc1 34f\n"
        "s_cmp_eq_u32 vcc_lo, 0\n"
        "s_cbranch_scc0 34f\n"
        "s_cmp_eq_u32 vcc_hi, 0\n"
        "s_cselect_b32 exec_hi, -1, 0\n"
        "s_nop 0\n"
        "s_mov_b32 exec_hi, -1\n"
        "s_add_u32 s52, s49, 0\n"
        "buffer_load_dwordx4 v[36:39], v0, s[40:43], s52 offen offset:0 sc1\n"
        "buffer_load_dwordx4 v[40:43], v0, s[40:43], s52 offen offset:1024 sc1\n"
        "buffer_load_dwordx4 v[44:47], v0, s[40:43], s52 offen offset:2048 sc1\n"
        "buffer_load_dwordx4 v[48:51], v0, s[40:43], s52 offen offset:3072 sc1\n"
        "s_add_u32 s52, s49, 4096\n"
        "buffer_load_dwordx4 v[52:55], v0, s[40:43], s52 offen offset:0 sc1\n"
        "buffer_load_dwordx4 v[56:59], v0, s[40:43], s52 offen offset:1024 sc1\n"
        "buffer_load_dwordx4 v[60:63], v0, s[40:43], s52 offen offset:2048 sc1\n"
        "buffer_load_dwordx4 v[64:67], v0, s[40:43], s52 offen offset:3072 sc1\n"
        "s_add_u32 s52, s49, 8192\n"
        "buffer_load_dwordx4 v[68:71], v0, s[40:43], s52 offen offset:0 sc1\n"
        "buffer_load_dwordx4 v[72:75], v0, s[40:43], s52 offen offset:1024 sc1\n"
        "buffer_load_dwordx4 v[76:79], v0, s[40:43], s52 offen offset:2048 sc1\n"
        "buffer_load_dwordx4 v[80:83], v0, s[40:43], s52 offen offset:3072 sc1\n"
        "v_add_u32 v4, 2, v4\n"
        "s_mov_b64 exec, s[58:59]\n"
        "v_add_u32 v3, 2048, v3\n"
        "s_mov_b64 exec, -1\n"
        "v_add_u32 v31, 81920, v6\n"
        "s_waitcnt lgkmcnt(0)\n"
        "s_barrier\n"
        "ds_read_b128 v[12:15], v6 offset:0\n"
        "ds_read_b128 v[16:19], v6 offset:40960\n"
        "ds_read_b128 v[20:23], v31 offset:0\n"
        "ds_read_b128 v[24:27], v31 offset:40960\n"
        "s_waitcnt lgkmcnt(2)\n"
        "v_add_f32 v12, v12, v16\n"
        "v_add_f32 v13, v13, v17\n"
        "v_add_f32 v14, v14, v18\n"
        "v_add_f32 v15, v15, v19\n"
        "s_waitcnt lgkmcnt(0)\n"
        "v_add_f32 v20, v20, v24\n"
        "v_add_f32 v21, v21, v25\n"
        "v_add_f32 v22, v22, v26\n"
        "v_add_f32 v23, v23, v27\n"
        "s_barrier\n"
        "v_add_f32 v12, v12, v20\n"
        "v_add_f32 v13, v13, v21\n"
        "v_add_f32 v14, v14, v22\n"
        "v_add_f32 v15, v15, v23\n"
        "v_cvt_pk_bf16_f32 v16, v12, v13\n"
        "v_cvt_pk_bf16_f32 v17, v14, v15\n"
        "v_and_b32 v13, 0xffff0000, v16\n"
        "v_lshlrev_b32 v12, 16, v16\n"
        "v_and_b32 v15, 0xffff0000, v17\n"
        "v_lshlrev_b32 v14, 16, v17\n"
        "v_and_b32 v21, 0xffff0000, v126\n"
        "v_lshlrev_b32 v20, 16, v126\n"
        "v_and_b32 v23, 0xffff0000, v127\n"
        "v_lshlrev_b32 v22, 16, v127\n"
        "v_mul_f32 v12, v12, v20\n"
        "v_mul_f32 v13, v13, v21\n"
        "v_mul_f32 v14, v14, v22\n"
        "v_mul_f32 v15, v15, v23\n"
        "v_cvt_pk_bf16_f32 v16, v12, v13\n"
        "v_cvt_pk_bf16_f32 v17, v14, v15\n"
        "v_and_b32 v13, 0xffff0000, v16\n"
        "v_lshlrev_b32 v12, 16, v16\n"
        "v_and_b32 v15, 0xffff0000, v17\n"
        "v_lshlrev_b32 v14, 16, v17\n"
        "v_and_b32 v21, 0xffff0000, v124\n"
        "v_lshlrev_b32 v20, 16, v124\n"
        "v_and_b32 v23, 0xffff0000, v125\n"
        "v_lshlrev_b32 v22, 16, v125\n"
        "v_add_f32 v12, v12, v20\n"
        "v_add_f32 v13, v13, v21\n"
        "v_add_f32 v14, v14, v22\n"
        "v_add_f32 v15, v15, v23\n"
        "v_cvt_pk_bf16_f32 v16, v12, v13\n"
        "v_cvt_pk_bf16_f32 v17, v14, v15\n"
        "buffer_store_dwordx2 v[16:17], v10, s[44:47], s56 offen sc1\n"
        "s_add_u32 s51, s51, 73728\n"
        "s_add_u32 s56, s56, 73728\n"
        "s_add_u32 s48, s48, 2\n"
        "s_branch 33b\n"
        "34:\n"
        "s_cmp_lt_u32 s48, s66\n"
        "s_cbranch_scc1 35f\n"
        "v_cmp_u_f32 vcc, v12, v12\n"
        "s_cbranch_vccz 35f\n"
        "s_mov_b32 s66, 64\n"
        "s_sub_u32 s49, s49, %3\n"
        "v_subrev_u32 v4, 2, v4\n"
        "s_mov_b64 exec, s[58:59]\n"
        "v_add_u32 v4, 64, v4\n"
        "v_subrev_u32 v3, 2048, v3\n"
        "s_mov_b64 exec, -1\n"
        "s_branch 31b\n"
        "35:\n"
        "v_add_u32 v31, 81920, v6\n"
        "s_waitcnt lgkmcnt(0)\n"
        "s_barrier\n"
        "ds_read_b128 v[12:15], v6 offset:0\n"
        "ds_read_b128 v[16:19], v6 offset:40960\n"
        "ds_read_b128 v[20:23], v31 offset:0\n"
        "ds_read_b128 v[24:27], v31 offset:40960\n"
        "s_waitcnt lgkmcnt(2)\n"
        "v_add_f32 v12, v12, v16\n"
        "v_add_f32 v13, v13, v17\n"
        "v_add_f32 v14, v14, v18\n"
        "v_add_f32 v15, v15, v19\n"
        "s_waitcnt lgkmcnt(0)\n"
        "v_add_f32 v20, v20, v24\n"
        "v_add_f32 v21, v21, v25\n"
        "v_add_f32 v22, v22, v26\n"
        "v_add_f32 v23, v23, v27\n"
        "s_barrier\n"
        "v_add_f32 v12, v12, v20\n"
        "v_add_f32 v13, v13, v21\n"
        "v_add_f32 v14, v14, v22\n"
        "v_add_f32 v15, v15, v23\n"
        "v_cvt_pk_bf16_f32 v16, v12, v13\n"
        "v_cvt_pk_bf16_f32 v17, v14, v15\n"
        "v_and_b32 v13, 0xffff0000, v16\n"
        "v_lshlrev_b32 v12, 16, v16\n"
        "v_and_b32 v15, 0xffff0000, v17\n"
        "v_lshlrev_b32 v14, 16, v17\n"
        "v_and_b32 v21, 0xffff0000, v126\n"
        "v_lshlrev_b32 v20, 16, v126\n"
        "v_and_b32 v23, 0xffff0000, v127\n"
        "v_lshlrev_b32 v22, 16, v127\n"
        "v_mul_f32 v12, v12, v20\n"
        "v_mul_f32 v13, v13, v21\n"
        "v_mul_f32 v14, v14, v22\n"
        "v_mul_f32 v15, v15, v23\n"
        "v_cvt_pk_bf16_f32 v16, v12, v13\n"
        "v_cvt_pk_bf16_f32 v17, v14, v15\n"
        "v_and_b32 v13, 0xffff0000, v16\n"
        "v_lshlrev_b32 v12, 16, v16\n"
        "v_and_b32 v15, 0xffff0000, v17\n"
        "v_lshlrev_b32 v14, 16, v17\n"
        "v_and_b32 v21, 0xffff0000, v124\n"
        "v_lshlrev_b32 v20, 16, v124\n"
        "v_and_b32 v23, 0xffff0000, v125\n"
        "v_lshlrev_b32 v22, 16, v125\n"
        "v_add_f32 v12, v12, v20\n"
        "v_add_f32 v13, v13, v21\n"
        "v_add_f32 v14, v14, v22\n"
        "v_add_f32 v15, v15, v23\n"
        "v_cvt_pk_bf16_f32 v16, v12, v13\n"
        "v_cvt_pk_bf16_f32 v17, v14, v15\n"
        "buffer_store_dwordx2 v[16:17], v10, s[44:47], s56 offen sc1\n"
        "s_add_u32 s51, s51, 73728\n"
        "s_add_u32 s56, s56, 73728\n"
        "s_add_u32 s48, s48, 2\n"
        "v_and_b32 v0, 0x7fffffff, v0\n"
        "v_and_b32 v10, 0x7fffffff, v10\n"
        "s_cmp_le_u32 s48, %13\n"
        "s_cbranch_scc1 31b\n"
        "s_waitcnt vmcnt(0)\n"
        "40:\n"
        :: "s"(ws), "s"(W), "s"(src), "s"(stride), "s"(x), "s"(out), "s"(cb), "s"(grp), "s"(w), "s"(kind), "s"(blk), "s"(go0), "s"(from), "s"(last)
        : "v0", "v1", "v2", "v3", "v4", "v5", "v6", "v7", "v8", "v9", "v10", "v11", "v12", "v13", "v14", "v15", "v16", "v17", "v18", "v19", "v20", "v21", "v22", "v23", "v24", "v25", "v26", "v27", "v28", "v29", "v30", "v31", "v32", "v33", "v34", "v35", "v36", "v37", "v38", "v39", "v40", "v41", "v42", "v43", "v44", "v45", "v46", "v47", "v48", "v49", "v50", "v51", "v52", "v53", "v54", "v55", "v56", "v57", "v58", "v59", "v60", "v61", "v62", "v63", "v64", "v65", "v66", "v67", "v68", "v69", "v70", "v71", "v72", "v73", "v74", "v75", "v76", "v77", "v78", "v79", "v80", "v81", "v82", "v83", "v84", "v85", "v86", "v87", "v88", "v89", "v90", "v91", "v92", "v93", "v94", "v95", "v96", "v97", "v98", "v99", "v100", "v101", "v102", "v103", "v104", "v105", "v106", "v107", "v108", "v109", "v110", "v111", "v112", "v113", "v114", "v115", "v116", "v117", "v118", "v119", "v120", "v121", "v122", "v123", "v124", "v125", "v126", "v127", "v128", "v129", "v130", "v131", "v132", "v133", "v134", "v135", "v136", "v137", "v138", "v139", "v140", "v141", "v142", "v143", "v144", "v145", "v146", "v147", "v148", "v149", "v150", "v151", "v152", "v153", "v154", "v155", "v156", "v157", "v158", "v159", "v160", "v161", "v162", "v163", "v164", "v165", "v166", "v167", "v168", "v169", "v170", "v171", "v172", "v173", "v174", "v175", "v176", "v177", "v178", "v179", "v180", "v181", "v182", "v183", "v184", "v185", "v186", "v187", "v188", "v189", "v190", "v191", "v192", "v193", "v194", "v195", "v196", "v197", "v198", "v199", "v200", "v201", "v202", "v203", "v204", "v205", "v206", "v207", "v208", "v209", "v210", "v211", "v212", "v213", "v214", "v215", "v216", "v217", "v218", "v219", "v220", "v221", "v222", "v223", "v224", "v225", "v226", "v227", "v228", "v229", "v230", "v231", "v232", "v233", "v234", "v235", "v236", "v237", "v238", "v239", "v240", "v241", "v242", "v243", "v244", "v245", "v246", "v247", "v248", "v249", "v250", "v251", "v252", "v253", "v254", "v255", "a0", "a1", "a2", "a3", "a4", "a5", "a6", "a7", "a8", "a9", "a10", "a11", "a12", "a13", "a14", "a15", "a16", "a17", "a18", "a19", "a20", "a21", "a22", "a23", "a24", "a25", "a26", "a27", "a28", "a29", "a30", "a31", "a32", "a33", "a34", "a35", "a36", "a37", "a38", "a39", "a40", "a41", "a42", "a43", "a44", "a45", "a46", "a47", "a48", "a49", "a50", "a51", "a52", "a53", "a54", "a55", "a56", "a57", "a58", "a59", "a60", "a61", "a62", "a63", "a64", "a65", "a66", "a67", "a68", "a69", "a70", "a71", "a72", "a73", "a74", "a75", "a76", "a77", "a78", "a79", "a80", "a81", "a82", "a83", "a84", "a85", "a86", "a87", "a88", "a89", "a90", "a91", "a92", "a93", "a94", "a95", "a96", "a97", "a98", "a99", "a100", "a101", "a102", "a103", "a104", "a105", "a106", "a107", "a108", "a109", "a110", "a111", "a112", "a113", "a114", "a115", "a116", "a117", "a118", "a119", "a120", "a121", "a122", "a123", "a124", "a125", "a126", "a127", "a128", "a129", "a130", "a131", "a132", "a133", "a134", "a135", "a136", "a137", "a138", "a139", "a140", "a141", "a142", "a143", "a144", "a145", "a146", "a147", "a148", "a149", "a150", "a151", "a152", "a153", "a154", "a155", "a156", "a157", "a158", "a159", "a160", "a161", "a162", "a163", "a164", "a165", "a166", "a167", "a168", "a169", "a170", "a171", "a172", "a173", "a174", "a175", "a176", "a177", "a178", "a179", "a180", "a181", "a182", "a183", "a184", "a185", "a186", "a187", "a188", "a189", "a190", "a191", "a192", "a193", "a194", "a195", "a196", "a197", "a198", "a199", "a200", "a201", "a202", "a203", "a204", "a205", "a206", "a207", "a208", "a209", "a210", "a211", "a212", "a213", "a214", "a215", "a216", "a217", "a218", "a219", "a220", "a221", "a222", "a223", "a224", "a225", "a226", "a227", "a228", "a229", "a230", "a231", "a232", "a233", "a234", "a235", "a236", "a237", "a238", "a239", "a240", "a241", "a242", "a243", "a244", "a245", "a246", "a247", "a248", "a249", "a250", "a251", "a252", "a253", "a254", "a255", "s40", "s41", "s42", "s43", "s44", "s45", "s46", "s47", "s48", "s49", "s50", "s51", "s52", "s53", "s54", "s55", "s56", "s57", "s58", "s59", "s60", "s61", "s62", "s63", "s64", "s65", "s66", "s67", "vcc", "scc", "memory");
}

struct Args {
    const u16 *x, *mem0, *Wq, *Wk, *Wv, *Wo, *w1, *w2;
    u16* out;
    unsigned char* ws;
    int mask, d0, d1, d2, d3, d4, d5, d6, d7, d8;
};

typedef const __attribute__((address_space(4))) Args* ArgP;
__device__ __forceinline__ Args args() { ArgP p = (ArgP)__builtin_amdgcn_kernarg_segment_ptr(); asm volatile("" : "+s"(p)); Args a; __builtin_memcpy(&a, p, sizeof a); return a; }

__global__ void __launch_bounds__(256, 1) main_kernel(Args arg) {
    extern __shared__ __attribute__((aligned(16))) char lds[];
    const int w = __builtin_amdgcn_readfirstlane(threadIdx.x >> 6);
    const int b = blockIdx.x, xc = b & 7, i = b >> 3;
    asm volatile("s_getpc_b64 vcc\n.Lcode_from:\n\tv_min_u32 v255, .Lcode_to-.Lcode_from, %0\n\tglobal_load_dword v255, v255, vcc" :: "v"(((i & 3) * 256 + threadIdx.x) * 64) : "v255", "vcc");
    v2u go = {0, 0};
    const int mask = 31;
    if (xc >= 2 && (xc < 4 || i < 18 || i == 31)) {
        const Args a = args();
        const rsrc_t rs = ws_rsrc(a.ws);
        const unsigned go0 = ld4(rs, 0, OFF_GO + b * 128);
        if (!(mask & 16) || b == COORD) ;
        else if ((xc >> 1) == 1 || (i == 31 && xc >= 4)) {
            if (i < 16 && w >= 2) probe_pages(rs, b, w - 2, i * 2 + xc - 2);
            else if (w) __builtin_amdgcn_s_sleep(LND);
            if (xc < 4 || w == 3) layer_norm(rs, a.x, xc < 4 ? w * 64 + (b == 251 ? 26 : i * 2 + xc - 2) : xc * 64 - 193, lane_id());
        }
        else if (xc >= 4 && i < 18) {
            pull_keys(w & 1 ? a.Wv : a.Wk, xc - 4, i * 4 + w, lane_id());
            prepare(rs, a.mem0, (i * 4 + xc - 4) * 256 + threadIdx.x);
        }
        if (QE && (xc >> 1) == 1 && i >= 16) { WAIT_VM(0); st4(now(), rs, 0, OFF_RDY + b * 128 + w * 4); __builtin_amdgcn_s_sleep(QE); attend_weights(a.Wq + (i + xc * 16 - 48) * 128, w, lane_id()); }
        if (b == COORD) coordinate(rs, (unsigned*)lds, a.d8, !(mask & 16));
        else if (xc >= 4 && i < 18 && (mask & 8)) { WAIT_VM(0); st4(now(), rs, 0, OFF_RDY + b * 128 + w * 4); go[0] = __builtin_amdgcn_readfirstlane(go0); }
        else go = gate(rs, __builtin_amdgcn_readfirstlane(go0), b, w);
    }
    if (xc < 2) {
        const Args a = args();
        if (i >= 16) pull_weights(a.Wq, a.Wk, a.Wv, a.Wo, xc, i * 4 + w - 64, lane_id());
        else if (mask & 2) chain_fast((i >> NTO << (NTO + 1)) + (xc << NTO) + (i & ((1 << NTO) - 1)), a.Wq, a.Wk, a.Wv, a.Wo, a.ws, b, a.d0, a.d1, a.d2, a.d4, a.d5, a.d6, lds, w, lane_id());
    } else if (xc < 4 && i >= 16) {
        const Args a = args();
        if (!QE) attend_weights(a.Wq + (i + xc * 16 - 48) * 128, w, lane_id());
        if (mask & 4) attend(a.ws, i + xc * 16 - 48, w, go[0], go[1] + (w - 1) * ((a.d0 + a.d1) >> 1) + QF);
    } else if (xc < 4) {
        const Args a = args();
        const int u = ((i + 2 * w) % 9) * 8 + 2 * w + xc - 2;
        if (i < 9 && (mask & 2)) reducer(u, a.ws, go[0], go[1], lane_id(), a.d0, a.d1, ((u >> 3) * a.d3) >> 3, a.d4, a.d6);
    } else {
        if (i < 18) {
            pull_flat(args().w2, (i * 4 + xc - 4) * 4 + w, w, lane_id());
            for (int n = args().d7; n > 0; --n) __builtin_amdgcn_s_sleep(16);
        } else {
            const Args a = args();
            const int k = i < 26 ? xc * 8 + i - 50 : (i - 18) * 4 + xc - 4, c = (k - 32) * 4 + w, fh = FL_HF - 3072 + c / 18 * 128 + c % 18 * 4;
            if (k < 50 && (mask & 1))
                proj_in(a.ws, k < 32 ? (w < 2 ? a.Wk : a.Wv) + k * 128 + (w & 1) * 64 : a.w1 + c * 64, k < 32 ? 8192 : 2 * FF,
                        k < 32 ? (w < 2 ? OFF_KF : OFF_VF) + k * 8192 + (w & 1) * 4096 : OFF_HF + c * 2048, k < 32 ? 262144 : 294912, k < 32 ? FL_KV + k * 128 + w * 4 : fh, w, k < 32 ? w >> 1 : 2,
                        (k < 32 ? OFF_MEM + 20480 - 3 * 32768 + (k >> 1) * 256 + (k & 1) * 64 + 16 + w * 4 : fh + 512) - 8192, b);
            RAW_BARRIER();
        }
        const Args a = args();
        const int help = i >= 18, j = help ? min(i, 26) + xc * 9 - 54 : (xc & 1) * 18 + i, kind = help | (xc >> 1 & 1), grp = j / 9 ^ help * 2, cb = j % 9, q = grp * 4 + w;
        if ((i < 26 || i == 31) && (H4 || !help || j < 18) && (mask & 8))
            proj_out(a.ws, (kind ? a.Wo + w * 1024 * DM : a.w2 + w * 1152 * DM) + cb * 64, kind ? OFF_OF + q * 32768 + help * 30 * 524288 : OFF_HF + q * 36864, kind ? 524288 : 589824,
                     a.x, a.out, cb, grp, w, kind, b, go[0], help * 30, xc >= 7 - H4 && !help ? 54 + xc : 63);
    }
}

extern "C" void kernel_launch(void* const* d_in, const int* in_sizes, int n_in, void* d_out, int out_size, void* d_ws, size_t ws_size, hipStream_t stream) {
    unsigned char* ws = (unsigned char*)(((uintptr_t)d_ws + (2 << 20) - 1) & ~(uintptr_t)((2 << 20) - 1));
    Args a;
    a.x = (const u16*)d_in[0]; a.mem0 = (const u16*)d_in[1]; a.Wq = (const u16*)d_in[2]; a.Wk = (const u16*)d_in[3]; a.Wv = (const u16*)d_in[4]; a.Wo = (const u16*)d_in[5];
    a.w1 = (const u16*)d_in[8]; a.w2 = (const u16*)d_in[9]; a.out = (u16*)d_out; a.ws = ws;
    a.d0 = D0; a.d1 = D1; a.d2 = D2; a.d3 = D3; a.d4 = D4; a.d5 = D5; a.d6 = D6; a.d7 = D7; a.d8 = D8;
    (void)hipFuncSetAttribute((const void*)main_kernel, hipFuncAttributeMaxDynamicSharedMemorySize, 163840);
    a.mask = 31;
    main_kernel<<<256, 256, 163840, stream>>>(a);
}
```
